```python
import torch

def _softmax(x, axis=-1):
    return torch.softmax(x, dim=axis)

def _gelu(x):
    return torch.nn.functional.gelu(x, approximate='tanh')

def _silu(x):
    return torch.nn.functional.silu(x)

def _sigmoid(x):
    return torch.sigmoid(x)

def _layernorm(x, axis=-1, eps=1e-05):
    m = x.mean(dim=axis, keepdim=True)
    v = x.var(dim=axis, keepdim=True, unbiased=False)
    return (x - m) / torch.sqrt(v + eps)

def _scatter_add(tgt, idx, src, axis=0):
    return tgt.clone().index_add_(axis, idx, src)

def _segment_sum(src, seg, n):
    out = torch.zeros((n,) + src.shape[1:], dtype=src.dtype)
    return out.index_add_(0, seg, src)

def _pad(x, pw):
    flat = tuple((v for pair in reversed(pw) for v in pair))
    return torch.nn.functional.pad(x, flat)
kernel_name = 'gated_mix__mha_block__mixer_gmlp__prompt'

def setup_inputs():

    _w = lambda a: (lambda t: t.to(torch.bfloat16) if t.is_floating_point() else t)(torch.as_tensor(a))

    def _A_setup():
        B, S, D, H = (1, 2048, 512, 32)
        return {'x': _w((lambda *s: torch.randn(*s, dtype=torch.bfloat16) * 0.05)(B, S, D)), 'wqkv': _w((lambda *s: torch.randn(*s, dtype=torch.bfloat16) * 1.6)(D, 3 * D)), 'wo': _w((lambda *s: torch.randn(*s, dtype=torch.bfloat16) * 0.1)(D, D))}
    _A_in = _A_setup()

    def _B_setup():
        B, S, D, F = (1, 2048, 512, 1792)
        Fh = F // 2
        return {'x': _w((lambda *s: torch.randn(*s, dtype=torch.bfloat16) * 0.05)(B, S, D)), 'w_in': _w((lambda *s: torch.randn(*s, dtype=torch.bfloat16) * 0.8)(D, 2 * Fh)), 'w_sp': _w(0.01 * (lambda *s: torch.randn(*s, dtype=torch.bfloat16) * 0.8)(S, S)), 'b_sp': _w(1.0 + 0.01 * (lambda *s: torch.randn(*s, dtype=torch.bfloat16) * 0.05)(S)), 'w_out': _w((lambda *s: torch.randn(*s, dtype=torch.bfloat16) * 0.05)(Fh, D))}
    _B_in = _B_setup()
    return {'A_x': _A_in['x'], 'A_wqkv': _A_in['wqkv'], 'A_wo': _A_in['wo'], 'B_x': _B_in['x'], 'B_w_in': _B_in['w_in'], 'B_w_sp': _B_in['w_sp'], 'B_b_sp': _B_in['b_sp'], 'B_w_out': _B_in['w_out'], 'gate_w': _w((lambda *s: torch.randn(*s, dtype=torch.bfloat16) * 0.05)(512) * 16.0)}

def reference(A_x, A_wqkv, A_wo, B_x, B_w_in, B_w_sp, B_b_sp, B_w_out, gate_w):

    def _A_ref(x, wqkv, wo):
        B, S, D = x.shape
        H = 32
        Dh = D // H
        qkv = (lambda x, *d: x.permute(*d))((x @ wqkv).reshape(B, S, 3, H, Dh), 2, 0, 3, 1, 4)
        q, k, v = (qkv[0], qkv[1], qkv[2])
        scores = torch.einsum('bhqd,bhkd->bhqk', q, k) * Dh ** (-0.5)
        p = _softmax(scores, axis=-1)
        o = torch.einsum('bhqk,bhkd->bhqd', p, v)
        return (lambda x, *d: x.permute(*d))(o, 0, 2, 1, 3).reshape(B, S, D) @ wo

    def _B_ref(x, w_in, w_sp, b_sp, w_out):
        B, S, D = x.shape
        F = w_in.shape[-1]
        Fh = F // 2
        z = _gelu(torch.einsum('bsd,df->bsf', x, w_in))
        z1 = z[..., :Fh].clone()
        z2 = _layernorm(z[..., Fh:], axis=-1)
        g = torch.einsum('ts,bsf->btf', w_sp, z2) + b_sp[None, :, None]
        return torch.einsum('bsf,fd->bsd', z1 * g, w_out)
    x = A_x
    g = _sigmoid((x * gate_w).sum(-1, keepdims=True))
    a = _A_ref(x, A_wqkv, A_wo)
    b = _B_ref(x, B_w_in, B_w_sp, B_b_sp, B_w_out)
    return g * a + (1 - g) * b
```

```cpp
#include <hip/hip_runtime.h>
#include <hip/hip_bf16.h>
#include <stdint.h>

typedef __attribute__((ext_vector_type(8))) short bf16x8;
typedef __attribute__((ext_vector_type(4))) short s16x4;
typedef __attribute__((ext_vector_type(16))) float f32x16;
typedef __attribute__((ext_vector_type(4))) unsigned u32x4;
typedef __attribute__((ext_vector_type(2))) float f32x2;
typedef __attribute__((ext_vector_type(4))) float f32x4;
typedef __attribute__((ext_vector_type(2))) __bf16 bf16x2_t;
#define LDS_AS __attribute__((address_space(3)))
typedef LDS_AS const char* lds_cptr;
typedef LDS_AS char* lds_ptr;
typedef short v4i16_t __attribute__((ext_vector_type(4)));
typedef unsigned short u16;

#define DI __device__ __forceinline__
#define MFMA(a, b, c) __builtin_amdgcn_mfma_f32_32x32x16_bf16((a), (b), (c), 0, 0, 0)
#define WAIT_BAR(N) asm volatile("s_waitcnt vmcnt(" #N ") lgkmcnt(0)\n\ts_barrier" ::: "memory")

constexpr int S_ = 2048, D_ = 512, FH = 896;
constexpr float QSCALE = 0.25f * 1.4426950408889634f;

constexpr size_t WS_QF    = 0;
constexpr size_t WS_KV    = WS_QF + 32u * 64 * 1024;
constexpr size_t WS_Z1    = WS_KV + 32u * 64 * 2048;
constexpr size_t WS_Z2    = WS_Z1 + 64u * 56 * 1024;
constexpr size_t WS_Z2N   = WS_Z2 + 64u * 56 * 1024;
constexpr size_t WS_HO    = WS_Z2N + 28u * 128 * 1024;
constexpr size_t WS_STATS = WS_HO + 64u * 88 * 1024;
constexpr size_t WS_KMAX  = WS_STATS + 2048u * 8 * 8;
constexpr size_t WS_GATE  = WS_KMAX + 64u * 32 * 4;
constexpr size_t WS_END   = WS_GATE + 2048u * 4;

DI unsigned cvtpk(float a, float b) { f32x2 f = {a, b}; bf16x2_t r = __builtin_convertvector(f, bf16x2_t); return __builtin_bit_cast(unsigned, r); }
DI float bf_lo(unsigned u) { return __uint_as_float(u << 16); }
DI float bf_hi(unsigned u) { return __uint_as_float(u & 0xffff0000u); }
DI void glds16(const void* g, unsigned lds_base) {
    unsigned sv;
    asm volatile("s_mov_b32 %0, m0\n\ts_mov_b32 m0, %2\n\ts_nop 0\n\tglobal_load_lds_dwordx4 %1, off\n\ts_mov_b32 m0, %0"
                 : "=&s"(sv) : "v"(g), "s"(lds_base) : "memory");
}
DI s16x4 vtr(lds_cptr p) { return __builtin_bit_cast(s16x4, __builtin_amdgcn_ds_read_tr16_b64_v4i16((LDS_AS v4i16_t*)p)); }
DI bf16x8 trfrag(lds_cptr p0, lds_cptr p1) { s16x4 lo = vtr(p0), hi = vtr(p1); return __builtin_shufflevector(lo, hi, 0, 1, 2, 3, 4, 5, 6, 7); }
DI bf16x8 ldsfrag(lds_cptr p) { return *(const LDS_AS bf16x8*)p; }
DI u32x4 pack8(const f32x16& a, int o, float sc) {
    u32x4 r; r[0] = cvtpk(a[o] * sc, a[o + 1] * sc); r[1] = cvtpk(a[o + 2] * sc, a[o + 3] * sc);
    r[2] = cvtpk(a[o + 4] * sc, a[o + 5] * sc); r[3] = cvtpk(a[o + 6] * sc, a[o + 7] * sc); return r;
}
DI float swap32_sum(float v) { auto r = __builtin_amdgcn_permlane32_swap(__float_as_uint(v), __float_as_uint(v), false, false); return __uint_as_float(r[0]) + __uint_as_float(r[1]); }
DI float swap32_max(float v) { auto r = __builtin_amdgcn_permlane32_swap(__float_as_uint(v), __float_as_uint(v), false, false); return fmaxf(__uint_as_float(r[0]), __uint_as_float(r[1])); }
DI float wave_max(float v) {
#pragma unroll
    for (int o = 1; o < 32; o <<= 1) v = fmaxf(v, __shfl_xor(v, o));
    return swap32_max(v);
}
DI float gelu_tanh(float x) {
    const float c1 = -2.0f * 0.7978845608028654f * 1.4426950408889634f, c2 = c1 * 0.044715f;
    const float a = x * __builtin_fmaf(x * x, c2, c1);
    return x * __builtin_amdgcn_rcpf(1.0f + __builtin_amdgcn_exp2f(a));
}

constexpr int K1_XB = 65536, K1_WST = 28672, K1_LDS = K1_XB + 3 * K1_WST;

__global__ __launch_bounds__(512) void k1_proj(const u16* __restrict__ x, const u16* __restrict__ wqkv, const u16* __restrict__ w_in,
                                               const u16* __restrict__ gate_w, char* __restrict__ ws) {
    __shared__ __attribute__((aligned(1024))) char lds[K1_LDS];
    const int tid = threadIdx.x, l = tid & 63, w = __builtin_amdgcn_readfirstlane(tid >> 6);
    const int b = blockIdx.x, xcd = b & 7, idx = b >> 3;
    const int nt = (xcd & 3) * 2 + (idx & 1), mt = (xcd >> 2) * 16 + (idx >> 1);
    const int rb = w & 1, ch = (w >> 1) & 1, role = w >> 2;
    const int zg0 = 7 * (nt >> 1) + ((nt & 1) ? 4 : 0), nzg = (nt & 1) ? 3 : 4;
    const unsigned lds0 = (unsigned)(uintptr_t)lds;
    const u16* xsrc; { const int row = 8 * w + (l >> 3), chunk = (l & 7) ^ ((row >> 1) & 7); xsrc = x + (size_t)(mt * 64 + row) * D_ + chunk * 8; }
    const int wq = w & 3, g0 = w >> 2;
    const u16 *wp0, *wp1, *wp2, *wp3; int ld0, ld1;
    {
        const int row = 8 * wq + (l >> 3), chunk = (l & 7) ^ (((row >> 1) & 1) << 2);
        const u16* qk = wqkv + (size_t)row * 1536 + nt * 64 + chunk * 8;
        const u16* zb = w_in + (size_t)row * 1792 + zg0 * 64 + chunk * 8;
        if (g0 == 0) { wp0 = qk; ld0 = 1536; wp1 = qk + 1024; ld1 = 1536; wp2 = zb + 64; wp3 = zb + 192; }
        else         { wp0 = qk + 512; ld0 = 1536; wp1 = zb; ld1 = 1792; wp2 = zb + 128; wp3 = zb; }
    }
    const bool big = (w < 4) && !(nt & 1);
    auto issueW = [&](int s) {
        const unsigned dst = lds0 + K1_XB + (s % 3) * K1_WST + wq * 1024 + g0 * 4096;
        glds16(wp0 + (size_t)s * 32 * ld0, dst);
        glds16(wp1 + (size_t)s * 32 * ld1, dst + 2 * 4096);
        glds16(wp2 + (size_t)s * 32 * 1792, dst + 4 * 4096);
        if (big) glds16(wp3 + (size_t)s * 32 * 1792, dst + 6 * 4096);
    };
    glds16(xsrc, lds0 + w * 1024);
    issueW(0); issueW(1);
#pragma unroll
    for (int m = 1; m < 8; ++m) glds16(xsrc + m * 64, lds0 + m * 8192 + w * 1024);

    const int h = l >> 5, r32 = l & 31;
    lds_cptr xb0, xb1, xb2, xb3;
    { const int row = rb * 32 + r32, f = (row >> 1) & 7; lds_cptr xr = (lds_cptr)lds + row * 128;
      xb0 = xr + (((0 + h) ^ f) << 4); xb1 = xr + (((2 + h) ^ f) << 4); xb2 = xr + (((4 + h) ^ f) << 4); xb3 = xr + (((6 + h) ^ f) << 4); }
    const int q4 = (l & 15) >> 2, p4 = l & 3, b16 = (l >> 4) & 1;
    const int wl = (8 * h + q4) * 128 + (((ch * 4 + b16 * 2 + (p4 >> 1)) ^ ((q4 >> 1) << 2)) << 4) + (p4 & 1) * 8;
    f32x16 acc[4]; acc[0] = f32x16{}; acc[1] = f32x16{}; acc[2] = f32x16{}; acc[3] = f32x16{};

    auto stage = [&](int s, lds_cptr xa, lds_cptr xb_) {
        if (s < 2) { if (big) WAIT_BAR(11); else WAIT_BAR(10); }
        else if (s < 15) { if (big) WAIT_BAR(4); else WAIT_BAR(3); }
        else WAIT_BAR(0);
        if (s + 2 < 16) issueW(s + 2);
        lds_cptr wp = (lds_cptr)lds + K1_XB + (s % 3) * K1_WST + wl;
        const int xo = (s >> 1) * 8192;
#pragma unroll
        for (int kk = 0; kk < 2; ++kk) {
            const bf16x8 xf = ldsfrag((kk ? xb_ : xa) + xo);
            if (role == 0) {
                const bf16x8 wq_ = trfrag(wp + 0 * 4096 + kk * 2048, wp + 0 * 4096 + kk * 2048 + 512);
                const bf16x8 wk_ = trfrag(wp + 1 * 4096 + kk * 2048, wp + 1 * 4096 + kk * 2048 + 512);
                const bf16x8 wv_ = trfrag(wp + 2 * 4096 + kk * 2048, wp + 2 * 4096 + kk * 2048 + 512);
                acc[0] = MFMA(wq_, xf, acc[0]); acc[1] = MFMA(wk_, xf, acc[1]); acc[2] = MFMA(xf, wv_, acc[2]);
            } else {
#pragma unroll
                for (int i = 0; i < 4; ++i) {
                    if (i < 3 || nzg == 4) {
                        const bf16x8 wz = trfrag(wp + (3 + i) * 4096 + kk * 2048, wp + (3 + i) * 4096 + kk * 2048 + 512);
                        acc[i] = MFMA(wz, xf, acc[i]);
                    }
                }
            }
        }
    };
#pragma unroll 1
    for (int s2 = 0; s2 < 8; ++s2) { stage(2 * s2, xb0, xb1); stage(2 * s2 + 1, xb2, xb3); }
    const int ttile = mt * 2 + rb;
    if (role == 0) {
        const int hd0 = 4 * nt + 2 * ch;
        char* qf = ws + WS_QF; char* kv = ws + WS_KV;
        *(u32x4*)(qf + ((size_t)(hd0 * 64 + ttile) * 1024) + l * 16) = pack8(acc[0], 0, QSCALE);
        *(u32x4*)(qf + ((size_t)((hd0 + 1) * 64 + ttile) * 1024) + l * 16) = pack8(acc[0], 8, QSCALE);
        *(u32x4*)(kv + ((size_t)(hd0 * 64 + ttile) * 2048) + l * 16) = pack8(acc[1], 0, 1.0f);
        *(u32x4*)(kv + ((size_t)((hd0 + 1) * 64 + ttile) * 2048) + l * 16) = pack8(acc[1], 8, 1.0f);
        { const int hd = hd0 + (r32 >> 4), d = r32 & 15;
          char* vp = kv + ((size_t)(hd * 64 + ttile) * 2048) + 1024 + h * 256 + d * 16;
          *(u32x4*)(vp) = pack8(acc[2], 0, 1.0f); *(u32x4*)(vp + 512) = pack8(acc[2], 8, 1.0f); }
        float n0 = 0.f, n1 = 0.f;
#pragma unroll
        for (int r = 0; r < 8; ++r) { n0 = __builtin_fmaf(acc[1][r], acc[1][r], n0); n1 = __builtin_fmaf(acc[1][8 + r], acc[1][8 + r], n1); }
        n0 = wave_max(swap32_sum(n0)); n1 = wave_max(swap32_sum(n1));
        if (l == 0) { float* km = (float*)(ws + WS_KMAX) + ttile * 32 + hd0; km[0] = n0 * 1.01f; km[1] = n1 * 1.01f; }
    } else {
        float s1 = 0.f, s2 = 0.f;
#pragma unroll
        for (int i = 0; i < 4; ++i) {
            if (i < 3 || nzg == 4) {
                const int fb = 2 * (zg0 + i) + ch;
#pragma unroll
                for (int r = 0; r < 16; ++r) { const float z = gelu_tanh(acc[i][r]); acc[i][r] = z; s1 += z; s2 = __builtin_fmaf(z, z, s2); }
                char* zp = (fb < 28) ? (ws + WS_Z1 + ((size_t)(ttile * 56 + 2 * fb) * 1024)) : (ws + WS_Z2 + ((size_t)(ttile * 56 + 2 * (fb - 28)) * 1024));
                *(u32x4*)(zp + l * 16) = pack8(acc[i], 0, 1.0f); *(u32x4*)(zp + 1024 + l * 16) = pack8(acc[i], 8, 1.0f);
            }
        }
        if (nt >= 4) {
            s1 = swap32_sum(s1); s2 = swap32_sum(s2);
            if (h == 0) ((f32x2*)(ws + WS_STATS))[(size_t)(ttile * 32 + r32) * 8 + (nt - 4) * 2 + ch] = f32x2{s1, s2};
        }
    }
    if (nt == 1) {
        const int tok = tid >> 3, m = tid & 7, f = (tok >> 1) & 7;
        float a = 0.f;
#pragma unroll
        for (int sl = 0; sl < 8; ++sl) {
            const u32x4 xv = *(const LDS_AS u32x4*)((lds_cptr)lds + m * 8192 + tok * 128 + sl * 16);
            const u32x4 gv = *(const u32x4*)(gate_w + m * 64 + (sl ^ f) * 8);
#pragma unroll
            for (int e = 0; e < 4; ++e) { a = __builtin_fmaf(bf_lo(xv[e]), bf_lo(gv[e]), a); a = __builtin_fmaf(bf_hi(xv[e]), bf_hi(gv[e]), a); }
        }
        a += __shfl_xor(a, 1); a += __shfl_xor(a, 2); a += __shfl_xor(a, 4);
        if (m == 0) ((float*)(ws + WS_GATE))[mt * 64 + tok] = __builtin_amdgcn_rcpf(1.0f + __builtin_amdgcn_exp2f(-1.4426950408889634f * a));
    }
}

DI void ln_apply(char* __restrict__ ws, int b, int tid) {
    const int l = tid & 63, w = tid >> 6, h = l >> 5, r32 = l & 31;
    const int ttile = b >> 2, qu = b & 3;
    const f32x4* st = (const f32x4*)(ws + WS_STATS) + (size_t)(ttile * 32 + r32) * 4;
    float s1 = 0.f, s2 = 0.f;
#pragma unroll
    for (int i = 0; i < 4; ++i) { const f32x4 v = st[i]; s1 += v[0] + v[2]; s2 += v[1] + v[3]; }
    const float mean = s1 * (1.0f / FH);
    const float var = fmaxf(s2 * (1.0f / FH) - mean * mean, 0.f);
    const float rstd = __builtin_amdgcn_rsqf(var + 1e-5f), nm = -mean * rstd;
    for (int i = w; i < 7; i += 4) {
        const int fb = qu * 7 + i;
#pragma unroll
        for (int g = 0; g < 2; ++g) {
            const u32x4 v = *(const u32x4*)(ws + WS_Z2 + ((size_t)(ttile * 56 + 2 * fb + g) * 1024) + l * 16);
            unsigned o[4];
#pragma unroll
            for (int e = 0; e < 4; ++e) o[e] = cvtpk(__builtin_fmaf(bf_lo(v[e]), rstd, nm), __builtin_fmaf(bf_hi(v[e]), rstd, nm));
            auto r0 = __builtin_amdgcn_permlane32_swap(o[0], o[2], false, false);
            auto r1 = __builtin_amdgcn_permlane32_swap(o[1], o[3], false, false);
            u32x4 ov = {r0[0], r1[0], r0[1], r1[1]};
            *(u32x4*)(ws + WS_Z2N + ((size_t)(fb * 128 + ttile * 2 + (r32 >> 4)) * 1024) + (r32 & 15) * 64 + g * 32 + h * 16) = ov;
        }
    }
}
__global__ __launch_bounds__(256) void k_ln(char* __restrict__ ws) { ln_apply(ws, blockIdx.x, threadIdx.x); }

constexpr int AT_CH = 16384, AT_LDS = 4 * AT_CH;

#define AT_RK(kf, addr, off) asm volatile("ds_read_b128 %0, %1 offset:%2" : "=v"(kf) : "v"(addr), "i"(off) : "memory")
#define AT_RV(v0, v1, addr, off) asm volatile("s_mov_b64 exec, %3\n\tds_read_b128 %0, %2 offset:%4\n\tds_read_b128 %1, %2 offset:%5\n\ts_mov_b64 exec, -1" \
    : "+v"(v0), "+v"(v1) : "v"(addr), "s"(vmask), "i"((off) + 1024), "i"((off) + 1536) : "memory")

DI void attn_block(char* __restrict__ ws, char* lds, int head, int chunk, int tid) {
    const int l = tid & 63, w = __builtin_amdgcn_readfirstlane(tid >> 6), h = l >> 5;
    const int qtile = chunk * 8 + w;
    const unsigned lds0 = (unsigned)(uintptr_t)lds;
    const char* kvsrc = ws + WS_KV + (size_t)head * 64 * 2048 + w * 2048 + l * 16;
    auto issue = [&](int c) { const unsigned dst = lds0 + (c & 3) * AT_CH + w * 2048; glds16(kvsrc + c * AT_CH, dst); glds16(kvsrc + c * AT_CH + 1024, dst + 1024); };
    const bf16x8 qf = *(const bf16x8*)(ws + WS_QF + ((size_t)(head * 64 + qtile) * 1024) + l * 16);
    const float km = ((const float*)(ws + WS_KMAX))[l * 32 + head];
    const float gate = ((const float*)(ws + WS_GATE))[qtile * 32 + (l & 31)];
    float qn = 0.f;
    { const u32x4 qu = __builtin_bit_cast(u32x4, qf);
#pragma unroll
      for (int e = 0; e < 4; ++e) { qn = __builtin_fmaf(bf_lo(qu[e]), bf_lo(qu[e]), qn); qn = __builtin_fmaf(bf_hi(qu[e]), bf_hi(qu[e]), qn); } }
    qn = swap32_sum(qn);
    float ref = __builtin_sqrtf(qn * wave_max(km));
    const unsigned long long vmask = 0x0000ffff0000ffffull;
    const unsigned kl = lds0 + l * 16, vl = lds0 + h * 256 + (l & 15) * 16;
    f32x16 o;
    for (int pass = 0;; ++pass) {
        f32x16 negm;
#pragma unroll
        for (int r = 0; r < 16; ++r) negm[r] = -ref;
        asm volatile("" : "+v"(negm));
        o = f32x16{};
        bf16x8 kfA, kfB, vA0, vA1, vB0, vB1;
        { const u32x4 ones = {0x3f803f80u, 0x3f803f80u, 0x3f803f80u, 0x3f803f80u};
          vA0 = __builtin_bit_cast(bf16x8, ones); vA1 = vA0; vB0 = vA0; vB1 = vA0;
          asm volatile("" : "+v"(vA0), "+v"(vA1), "+v"(vB0), "+v"(vB1)); }
        issue(0); issue(1); issue(2);
        WAIT_BAR(4);
        f32x16 sA, sB;
        unsigned ka = kl, va = vl;
        AT_RK(kfA, ka, 0); AT_RK(kfB, ka, 2048); AT_RV(vA0, vA1, va, 0);
        asm volatile("s_waitcnt lgkmcnt(0)" : "+v"(kfA), "+v"(kfB), "+v"(vA0), "+v"(vA1));
        sA = MFMA(kfA, qf, negm);
#define AT_STEP(SC, SN, KN2, KN1, VC0, VC1, VN0, VN1, KADDR, KOFF, VADDR, VOFF, DOK, DOV, DOQ) do { \
            if (DOK) AT_RK(KN2, KADDR, KOFF); \
            if (DOV) AT_RV(VN0, VN1, VADDR, VOFF); \
            if (DOQ) SN = MFMA(KN1, qf, negm); \
            _Pragma("unroll") for (int r = 0; r < 16; ++r) SC[r] = __builtin_amdgcn_exp2f(SC[r]); \
            const u32x4 p0 = pack8(SC, 0, 1.0f), p1 = pack8(SC, 8, 1.0f); \
            o = MFMA(VC0, __builtin_bit_cast(bf16x8, p0), o); o = MFMA(VC1, __builtin_bit_cast(bf16x8, p1), o); \
            asm volatile("s_waitcnt lgkmcnt(0)" : "+v"(KN2), "+v"(VN0), "+v"(VN1)); \
        } while (0)
#pragma unroll 1
        for (int c = 0; c < 8; ++c) {
            const unsigned kn = kl + ((c + 1) & 3) * AT_CH, vn = vl + ((c + 1) & 3) * AT_CH;
            const bool more = c < 7;
            AT_STEP(sA, sB, kfA, kfB, vA0, vA1, vB0, vB1, ka, 2 * 2048, va, 1 * 2048, true, true, true);
            AT_STEP(sB, sA, kfB, kfA, vB0, vB1, vA0, vA1, ka, 3 * 2048, va, 2 * 2048, true, true, true);
            AT_STEP(sA, sB, kfA, kfB, vA0, vA1, vB0, vB1, ka, 4 * 2048, va, 3 * 2048, true, true, true);
            AT_STEP(sB, sA, kfB, kfA, vB0, vB1, vA0, vA1, ka, 5 * 2048, va, 4 * 2048, true, true, true);
            AT_STEP(sA, sB, kfA, kfB, vA0, vA1, vB0, vB1, ka, 6 * 2048, va, 5 * 2048, true, true, true);
            AT_STEP(sB, sA, kfB, kfA, vB0, vB1, vA0, vA1, ka, 7 * 2048, va, 6 * 2048, true, true, true);
            if (more) { if (c < 6) WAIT_BAR(2); else WAIT_BAR(0); if (c + 3 < 8) issue(c + 3); }
            AT_STEP(sA, sB, kfA, kfB, vA0, vA1, vB0, vB1, kn, 0, va, 7 * 2048, more, true, true);
            AT_STEP(sB, sA, kfB, kfA, vB0, vB1, vA0, vA1, kn, 2048, vn, 0, more, more, more);
            ka = kn; va = vn;
        }
        const float lsum = o[8];
        const bool bad = !(lsum >= 9.094947e-13f);
        if (pass == 1) break;
        if (!__syncthreads_or(bad ? 1 : 0)) break;
        float m = -3.0e38f;
        for (int t = 0; t < 64; ++t) {
            const bf16x8 kf = *(const bf16x8*)(ws + WS_KV + ((size_t)(head * 64 + t) * 2048) + l * 16);
            const f32x16 s = MFMA(kf, qf, f32x16{});
#pragma unroll
            for (int r = 0; r < 16; ++r) m = fmaxf(m, s[r]);
        }
        ref = swap32_max(m);
    }
    const float sc = gate * __builtin_amdgcn_rcpf(o[8]);
    *(u32x4*)(ws + WS_HO + ((size_t)(qtile * 88 + head) * 1024) + l * 16) = pack8(o, 0, sc);
}
__global__ __launch_bounds__(512) void k_attn(char* __restrict__ ws) {
    __shared__ __attribute__((aligned(1024))) char lds[AT_LDS];
    const int b = blockIdx.x;
    attn_block(ws, lds, (b & 7) * 4 + ((b >> 3) & 3), b >> 5, threadIdx.x);
}

constexpr int SP_ST = 24576, SP_NS = 3, SP_LDS = SP_NS * SP_ST;

DI void spatial_block(const u16* __restrict__ w_sp, const u16* __restrict__ b_sp, char* __restrict__ ws, char* lds, int tt, int ft, int tid) {
    const int l = tid & 63, w = __builtin_amdgcn_readfirstlane(tid >> 6), h = l >> 5, r32 = l & 31;
    const int fb = w & 3, kh = w >> 2;
    const unsigned lds0 = (unsigned)(uintptr_t)lds;
    const u16* asrc; { const int row = 8 * w + (l >> 3), chunk = (l & 7) ^ ((row >> 1) & 7); asrc = w_sp + (size_t)(tt * 64 + row) * S_ + chunk * 8; }
    const char* zsrc = ws + WS_Z2N + ((size_t)((ft * 4 + fb) * 128 + 2 * kh) * 1024) + l * 16;
    auto issue = [&](int s) {
        const unsigned dst = lds0 + (s % SP_NS) * SP_ST;
        glds16(asrc + s * 64, dst + w * 1024);
        glds16(zsrc + (size_t)s * 4096, dst + 8192 + fb * 4096 + kh * 2048);
        glds16(zsrc + (size_t)s * 4096 + 1024, dst + 8192 + fb * 4096 + kh * 2048 + 1024);
    };
    issue(0); issue(1);
    int boff[2][2];
#pragma unroll
    for (int tb = 0; tb < 2; ++tb)
#pragma unroll
        for (int kk = 0; kk < 2; ++kk) { const int row = tb * 32 + r32, f = (row >> 1) & 7; boff[tb][kk] = row * 128 + ((((2 * kh + kk) * 2 + h) ^ f) << 4); }
    const int q4 = (l & 15) >> 2, p4 = l & 3, b16 = (l >> 4) & 1;
    const int zoff = 8192 + fb * 4096 + kh * 2048 + (8 * h + q4) * 64 + b16 * 32 + p4 * 8;
    f32x16 acc[2]; acc[0] = f32x16{}; acc[1] = f32x16{};
#pragma unroll 1
    for (int s = 0; s < 32; ++s) {
        if (s < 31) WAIT_BAR(3); else WAIT_BAR(0);
        if (s + 2 < 32) issue(s + 2);
        lds_cptr base = (lds_cptr)lds + (s % SP_NS) * SP_ST;
#pragma unroll
        for (int kk = 0; kk < 2; ++kk) {
            const bf16x8 zf = trfrag(base + zoff + kk * 1024, base + zoff + kk * 1024 + 256);
            const bf16x8 a0 = ldsfrag(base + boff[0][kk]), a1 = ldsfrag(base + boff[1][kk]);
            acc[0] = MFMA(zf, a0, acc[0]); acc[1] = MFMA(zf, a1, acc[1]);
        }
    }
    __syncthreads();
    {
        LDS_AS f32x4* red = (LDS_AS f32x4*)lds;
        const f32x16& snd = kh ? acc[0] : acc[1];
#pragma unroll
        for (int i = 0; i < 4; ++i) red[(w * 4 + i) * 64 + l] = f32x4{snd[4 * i], snd[4 * i + 1], snd[4 * i + 2], snd[4 * i + 3]};
        __syncthreads();
        f32x16 a = kh ? acc[1] : acc[0];
        const int pw = w ^ 4;
#pragma unroll
        for (int i = 0; i < 4; ++i) { const f32x4 v = red[(pw * 4 + i) * 64 + l]; a[4 * i] += v[0]; a[4 * i + 1] += v[1]; a[4 * i + 2] += v[2]; a[4 * i + 3] += v[3]; }
        const int ttile = tt * 2 + kh, tok = ttile * 32 + r32, fg = 2 * (ft * 4 + fb);
        const float bs = __uint_as_float((unsigned)b_sp[tok] << 16);
        const float og = 1.0f - ((const float*)(ws + WS_GATE))[tok];
#pragma unroll
        for (int g = 0; g < 2; ++g) {
            const u32x4 z = *(const u32x4*)(ws + WS_Z1 + ((size_t)(ttile * 56 + fg + g) * 1024) + l * 16);
            u32x4 ov;
#pragma unroll
            for (int e = 0; e < 4; ++e)
                ov[e] = cvtpk((a[8 * g + 2 * e] + bs) * bf_lo(z[e]) * og, (a[8 * g + 2 * e + 1] + bs) * bf_hi(z[e]) * og);
            *(u32x4*)(ws + WS_HO + ((size_t)(ttile * 88 + 32 + fg + g) * 1024) + l * 16) = ov;
        }
    }
}
__global__ __launch_bounds__(512) void k_spatial(const u16* __restrict__ w_sp, const u16* __restrict__ b_sp, char* __restrict__ ws) {
    __shared__ __attribute__((aligned(1024))) char lds[SP_LDS];
    const int b = blockIdx.x;
    const int x = b & 7, i = b >> 3;
    spatial_block(w_sp, b_sp, ws, lds, x * 4 + (i & 3), i >> 2, threadIdx.x);
}

constexpr int FN_ST = 16384, FN_NS = 6, FN_LDS = FN_NS * FN_ST;

__global__ __launch_bounds__(512) void k_final(const u16* __restrict__ wo, const u16* __restrict__ w_out, char* __restrict__ ws, u16* __restrict__ out) {
    __shared__ __attribute__((aligned(1024))) char lds[FN_LDS];
    const int tid = threadIdx.x, l = tid & 63, w = __builtin_amdgcn_readfirstlane(tid >> 6), h = l >> 5, r32 = l & 31;
    const int b = blockIdx.x, x = b & 7, i = b >> 3;
    const int tt = x * 4 + (i & 3), dt = i >> 2;
    const int db = w & 1, kq = w >> 1;
    const unsigned lds0 = (unsigned)(uintptr_t)lds;
    const char* hsrc = ws + WS_HO + ((size_t)((tt * 2 + (w & 1)) * 88 + (w >> 1)) * 1024) + l * 16;
    const int wrow = 8 * w + (l >> 3), wchunk = (l & 7) ^ (((wrow >> 1) & 1) << 2);
    const u16* wsrc0 = wo + (size_t)wrow * D_ + dt * 64 + wchunk * 8;
    const u16* wsrc1 = w_out + (size_t)wrow * D_ + dt * 64 + wchunk * 8;
    auto issue = [&](int s) {
        const unsigned dst = lds0 + (s % FN_NS) * FN_ST;
        glds16(hsrc + (size_t)s * 4096, dst + (w >> 1) * 2048 + (w & 1) * 1024);
        glds16(s < 8 ? wsrc0 + (size_t)s * 64 * D_ : wsrc1 + (size_t)(s - 8) * 64 * D_, dst + 8192 + w * 1024);
    };
#pragma unroll
    for (int s = 0; s < FN_NS - 1; ++s) issue(s);
    const int q4 = (l & 15) >> 2, p4 = l & 3, b16 = (l >> 4) & 1;
    const int woff = 8192 + (kq * 16 + 4 * h + q4) * 128 + (((db * 4 + b16 * 2 + (p4 >> 1)) ^ ((q4 >> 1) << 2)) << 4) + (p4 & 1) * 8;
    const int hoff = kq * 2048 + l * 16;
    f32x16 acc[2]; acc[0] = f32x16{}; acc[1] = f32x16{};
#pragma unroll 1
    for (int s = 0; s < 22; ++s) {
        if (s < 18) WAIT_BAR(8); else if (s == 18) WAIT_BAR(6); else if (s == 19) WAIT_BAR(4); else if (s == 20) WAIT_BAR(2); else WAIT_BAR(0);
        if (s + FN_NS - 1 < 22) issue(s + FN_NS - 1);
        lds_cptr base = (lds_cptr)lds + (s % FN_NS) * FN_ST;
        const bf16x8 wf = trfrag(base + woff, base + woff + 8 * 128);
        const bf16x8 h0 = ldsfrag(base + hoff), h1 = ldsfrag(base + hoff + 1024);
        acc[0] = MFMA(wf, h0, acc[0]); acc[1] = MFMA(wf, h1, acc[1]);
    }
    __syncthreads();
    LDS_AS f32x4* red = (LDS_AS f32x4*)lds;
#pragma unroll
    for (int tb = 0; tb < 2; ++tb)
#pragma unroll
        for (int j = 0; j < 4; ++j) red[((w * 2 + tb) * 4 + j) * 64 + l] = f32x4{acc[tb][4 * j], acc[tb][4 * j + 1], acc[tb][4 * j + 2], acc[tb][4 * j + 3]};
    __syncthreads();
    const int tb = kq & 1, g = kq >> 1;
    f32x4 v0 = {0.f, 0.f, 0.f, 0.f}, v1 = v0;
#pragma unroll
    for (int q = 0; q < 4; ++q) { const int ww = q * 2 + db; v0 += red[((ww * 2 + tb) * 4 + 2 * g) * 64 + l]; v1 += red[((ww * 2 + tb) * 4 + 2 * g + 1) * 64 + l]; }
    const unsigned a0 = cvtpk(v0[0], v0[1]), a1 = cvtpk(v0[2], v0[3]), b0 = cvtpk(v1[0], v1[1]), b1 = cvtpk(v1[2], v1[3]);
    auto r0 = __builtin_amdgcn_permlane32_swap(a0, b0, false, false);
    auto r1 = __builtin_amdgcn_permlane32_swap(a1, b1, false, false);
    u32x4 ov = {r0[0], r1[0], r0[1], r1[1]};
    *(u32x4*)(out + (size_t)(tt * 64 + tb * 32 + r32) * D_ + dt * 64 + db * 32 + g * 16 + h * 8) = ov;
}

extern "C" void kernel_launch(void* const* d_in, const int* in_sizes, int n_in, void* d_out, int out_size, void* d_ws, size_t ws_size, hipStream_t stream) {
    const u16* x = (const u16*)d_in[0];
    const u16* wqkv = (const u16*)d_in[1];
    const u16* wo = (const u16*)d_in[2];
    const u16* w_in = (const u16*)d_in[4];
    const u16* w_sp = (const u16*)d_in[5];
    const u16* b_sp = (const u16*)d_in[6];
    const u16* w_out = (const u16*)d_in[7];
    const u16* gate_w = (const u16*)d_in[8];
    char* ws = (char*)d_ws;
    k1_proj<<<256, 512, 0, stream>>>(x, wqkv, w_in, gate_w, ws);
    k_ln<<<256, 256, 0, stream>>>(ws);
    k_attn<<<256, 512, 0, stream>>>(ws);
    k_spatial<<<224, 512, 0, stream>>>(w_sp, b_sp, ws);
    k_final<<<256, 512, 0, stream>>>(wo, w_out, ws, (u16*)d_out);
}
```

```cpp
#include <hip/hip_runtime.h>
#include <hip/hip_bf16.h>
#include <stdint.h>

typedef __attribute__((ext_vector_type(8))) short bf16x8;
typedef __attribute__((ext_vector_type(4))) short s16x4;
typedef __attribute__((ext_vector_type(16))) float f32x16;
typedef __attribute__((ext_vector_type(4))) unsigned u32x4;
typedef __attribute__((ext_vector_type(2))) float f32x2;
typedef __attribute__((ext_vector_type(4))) float f32x4;
typedef __attribute__((ext_vector_type(2))) __bf16 bf16x2_t;
#define LDS_AS __attribute__((address_space(3)))
typedef LDS_AS const char* lds_cptr;
typedef LDS_AS char* lds_ptr;
typedef short v4i16_t __attribute__((ext_vector_type(4)));
typedef unsigned short u16;
#define DI __device__ __forceinline__

#define MFMA(a, b, c) __builtin_amdgcn_mfma_f32_32x32x16_bf16((a), (b), (c), 0, 0, 0)
#define SB() __builtin_amdgcn_sched_barrier(0)
template <int NM, int ND> DI void interleave() {
#pragma unroll
    for (int i = 0; i < NM; ++i) { __builtin_amdgcn_sched_group_barrier(0x008, 1, 0); __builtin_amdgcn_sched_group_barrier(0x100, ND, 0); }
}
#define TL(kid, who, idx) do {} while (0)
#define WAIT_BAR(N) asm volatile("s_waitcnt vmcnt(" #N ") lgkmcnt(0)\n\ts_barrier" ::: "memory")

#define K1_ROT(mt, p) ((mt) & 7)
#define SP_ROT(tl, fl) ((tl) * 4 + (fl) * 2)
#define PACE 1
#define PACE_() do { __builtin_amdgcn_s_sleep(PACE); SB(); } while (0)
#define GPRIO 2
#define FN_ROT(i, dt) ((((i) & 3) * 3 + (dt)) % 11)
constexpr int S_ = 2048, D_ = 512, FH = 896;
constexpr float QSCALE = 0.25f * 1.4426950408889634f;

constexpr size_t WS_QF    = 0;
constexpr size_t WS_KV    = WS_QF + 32u * 64 * 1024;
constexpr size_t WS_Z2    = WS_KV + 32u * 64 * 2048;
constexpr size_t WS_Z2N   = WS_Z2 + 64u * 56 * 1024;
constexpr size_t WS_HO    = WS_Z2N + 28u * 128 * 1024;
constexpr size_t WS_STATS = WS_HO + 64u * 88 * 1024;
constexpr size_t WS_KMAX  = WS_STATS + 2048u * 28 * 8;
constexpr size_t WS_GATE  = WS_KMAX + 64u * 32 * 4;
constexpr size_t WS_END   = WS_GATE + 2048u * 4;

#define STAMP(kid, slot) do {} while (0)
DI unsigned cvtpk(float a, float b) { f32x2 f = {a, b}; bf16x2_t r = __builtin_convertvector(f, bf16x2_t); return __builtin_bit_cast(unsigned, r); }
DI float bf_lo(unsigned u) { return __uint_as_float(u << 16); }
DI float bf_hi(unsigned u) { return __uint_as_float(u & 0xffff0000u); }
DI void glds16(const void* g, unsigned lds_base) {
    unsigned sv;
    asm volatile("s_mov_b32 %0, m0\n\ts_mov_b32 m0, %2\n\ts_nop 0\n\tglobal_load_lds_dwordx4 %1, off\n\ts_mov_b32 m0, %0"
                 : "=&s"(sv) : "v"(g), "s"(lds_base) : "memory");
}
DI void glds16s(const void* ubase, unsigned voff, unsigned lds_base) {
    asm volatile("s_mov_b32 m0, %2\n\ts_nop 0\n\tglobal_load_lds_dwordx4 %0, %1" :: "v"(voff), "s"(ubase), "s"(lds_base) : "memory");
}
DI void bar_only() { asm volatile("" ::: "memory"); __builtin_amdgcn_s_waitcnt(0xC07F); __builtin_amdgcn_s_barrier(); asm volatile("" ::: "memory"); }
template <int N> DI void wait_bar() { asm volatile("s_waitcnt vmcnt(%0)" :: "i"(N) : "memory"); bar_only(); }
DI s16x4 vtr(lds_cptr p) { return __builtin_bit_cast(s16x4, __builtin_amdgcn_ds_read_tr16_b64_v4i16((LDS_AS v4i16_t*)p)); }
DI bf16x8 trfrag(lds_cptr p0, lds_cptr p1) { s16x4 lo = vtr(p0), hi = vtr(p1); return __builtin_shufflevector(lo, hi, 0, 1, 2, 3, 4, 5, 6, 7); }
DI bf16x8 ldsfrag(lds_cptr p) { return *(const LDS_AS bf16x8*)p; }
DI u32x4 pack8(const f32x16& a, int o, float sc) {
    u32x4 r; r[0] = cvtpk(a[o] * sc, a[o + 1] * sc); r[1] = cvtpk(a[o + 2] * sc, a[o + 3] * sc);
    r[2] = cvtpk(a[o + 4] * sc, a[o + 5] * sc); r[3] = cvtpk(a[o + 6] * sc, a[o + 7] * sc); return r;
}
DI float swap32_sum(float v) { auto r = __builtin_amdgcn_permlane32_swap(__float_as_uint(v), __float_as_uint(v), false, false); return __uint_as_float(r[0]) + __uint_as_float(r[1]); }
DI float swap32_max(float v) { auto r = __builtin_amdgcn_permlane32_swap(__float_as_uint(v), __float_as_uint(v), false, false); return fmaxf(__uint_as_float(r[0]), __uint_as_float(r[1])); }
DI float dpp_max16(float v) {
    v = fmaxf(v, __uint_as_float(__builtin_amdgcn_update_dpp(0u, __float_as_uint(v), 0x128, 0xf, 0xf, false)));
    v = fmaxf(v, __uint_as_float(__builtin_amdgcn_update_dpp(0u, __float_as_uint(v), 0x124, 0xf, 0xf, false)));
    v = fmaxf(v, __uint_as_float(__builtin_amdgcn_update_dpp(0u, __float_as_uint(v), 0x122, 0xf, 0xf, false)));
    v = fmaxf(v, __uint_as_float(__builtin_amdgcn_update_dpp(0u, __float_as_uint(v), 0x121, 0xf, 0xf, false)));
    return v;
}
DI float wave_max_fast(float v) {
    v = dpp_max16(v);
    auto r = __builtin_amdgcn_permlane16_swap(__float_as_uint(v), __float_as_uint(v), false, false);
    v = fmaxf(__uint_as_float(r[0]), __uint_as_float(r[1]));
    return swap32_max(v);
}

DI float gelu_tanh(float x) {
    const float c1 = -2.0f * 0.7978845608028654f * 1.4426950408889634f, c2 = c1 * 0.044715f;
    const float a = x * __builtin_fmaf(x * x, c2, c1);
    return x * __builtin_amdgcn_rcpf(1.0f + __builtin_amdgcn_exp2f(a));
}

constexpr int K1_ST = 40960, K1_LDS = 3 * K1_ST + 1024;

template <int TYPE>
DI void k1_body(const u16* __restrict__ x, const u16* __restrict__ wqkv, const u16* __restrict__ w_in, const u16* __restrict__ gate_w,
                char* __restrict__ ws, char* lds, int mt, int p, int tid) {
    if (tid == 0) TL(0, 1, 99);
    constexpr int NG = TYPE == 0 ? 3 : (TYPE == 1 ? 2 : 1);
    constexpr int NA = TYPE == 2 ? 1 : (TYPE == 3 ? 2 : NG);
    constexpr int P = 4 + 2 * NG;
    const int l = tid & 63, w = __builtin_amdgcn_readfirstlane(tid >> 6);
    const int rot = K1_ROT(mt, p);
    if (w >= 4) {
        const int j = w - 4;
        const unsigned lds0 = (unsigned)(uintptr_t)lds;
        const int row = 8 * j + (l >> 3);
        const unsigned xv = (unsigned)(row * D_ + ((l & 7) ^ ((row >> 1) & 7)) * 8) * 2;
        const int wch = ((l & 7) ^ (((row >> 1) & 1) << 2)) * 8;
        const unsigned qv = (unsigned)(row * 1536 + wch) * 2, zv = (unsigned)(row * 1792 + wch) * 2;
        const char* xu = (const char*)(x + (size_t)mt * 128 * D_);
        const char* g0 = (const char*)(wqkv + p * 64 + (TYPE == 0 ? 0 : 1024));
        const char* g1 = TYPE == 0 ? (const char*)(wqkv + p * 64 + 512) : (const char*)(w_in + FH + (8 + p) * 64);
        const char* g2 = (const char*)(w_in + FH + p * 64);
        auto issue = [&](int s) {
            const unsigned dst = lds0 + (s % 3) * K1_ST + j * 1024;
            const int sr = (s + rot) & 7;
#pragma unroll
            for (int i = 0; i < 4; ++i) glds16s(xu + (size_t)i * 32 * D_ * 2 + sr * 128, xv, dst + i * 4096);
            glds16s(g0 + (size_t)sr * 64 * 3072, qv, dst + 16384); glds16s(g0 + (size_t)(sr * 64 + 32) * 3072, qv, dst + 16384 + 4096);
            if (NG >= 2) { constexpr int ld = TYPE == 0 ? 3072 : 3584; const unsigned gv = TYPE == 0 ? qv : zv;
                glds16s(g1 + (size_t)sr * 64 * ld, gv, dst + 16384 + 8192); glds16s(g1 + (size_t)(sr * 64 + 32) * ld, gv, dst + 16384 + 8192 + 4096); }
            if (NG >= 3) { glds16s(g2 + (size_t)sr * 64 * 3584, zv, dst + 32768); glds16s(g2 + (size_t)(sr * 64 + 32) * 3584, zv, dst + 32768 + 4096); }
        };
        if (TYPE == 3 && j == 0) glds16s(gate_w, l * 16, lds0 + 3 * K1_ST);
        issue(0); issue(1); issue(2);
        wait_bar<2 * P>();
#pragma unroll 1
        for (int s = 0; s < 7; ++s) {
            if (j == 0) TL(0, 0, 3 * s);
            if (s + 2 < 8) asm volatile("s_waitcnt vmcnt(%0)" :: "i"(P) : "memory"); else asm volatile("s_waitcnt vmcnt(0)" ::: "memory");
            if (j == 0) TL(0, 0, 3 * s + 1);
            bar_only();
            if (j == 0) TL(0, 0, 3 * s + 2);
            if (s + 3 < 8) issue(s + 3);
        }
        return;
    }
    const int rh = w & 1, ch = w >> 1, h = l >> 5, r32 = l & 31;
    int xo[4];
    { const int row = rh * 64 + r32, f = (row >> 1) & 7;
#pragma unroll
      for (int c = 0; c < 4; ++c) xo[c] = row * 128 + (((2 * c + h) ^ f) << 4); }
    const int q4 = (l & 15) >> 2, p4 = l & 3, b16 = (l >> 4) & 1;
    const int wl = 16384 + (8 * h + q4) * 128 + (((ch * 4 + b16 * 2 + (p4 >> 1)) ^ ((q4 >> 1) << 2)) << 4) + (p4 & 1) * 8;
    f32x16 acc[NA][2];
#pragma unroll
    for (int g = 0; g < NA; ++g) { acc[g][0] = f32x16{}; acc[g][1] = f32x16{}; }
    bf16x8 xa[2][2], wa[NG][2], xb[2][2], wb[NG][2], ga[2], gb[2];
    auto rdh = [&](int s, int half, bf16x8 (&xf)[2][2], bf16x8 (&wf)[NG][2], bf16x8 (&gf)[2]) {
        lds_cptr base = (lds_cptr)lds + (s % 3) * K1_ST;
#pragma unroll
        for (int kk = 0; kk < 2; ++kk) {
            const int ks = 2 * half + kk;
            xf[0][kk] = ldsfrag(base + xo[ks]); xf[1][kk] = ldsfrag(base + xo[ks] + 4096);
#pragma unroll
            for (int g = 0; g < NG; ++g) wf[g][kk] = trfrag(base + wl + g * 8192 + ks * 2048, base + wl + g * 8192 + ks * 2048 + 512);
            if (TYPE == 3) gf[kk] = ldsfrag((lds_cptr)lds + 3 * K1_ST + (((s + rot) & 7) * 4 + ks) * 32 + h * 16);
        }
    };
    auto mm = [&](bf16x8 (&xf)[2][2], bf16x8 (&wf)[NG][2], bf16x8 (&gf)[2]) {
#pragma unroll
        for (int kk = 0; kk < 2; ++kk)
#pragma unroll
            for (int rb = 0; rb < 2; ++rb) {
                if (TYPE == 0) {
#pragma unroll
                    for (int g = 0; g < 3; ++g) acc[g % NA][rb] = MFMA(wf[g % NG][kk], xf[rb][kk], acc[g % NA][rb]);
                } else {
                    acc[0][rb] = MFMA(xf[rb][kk], wf[0][kk], acc[0][rb]);
                    if (TYPE == 1) acc[NA - 1][rb] = MFMA(wf[NG - 1][kk], xf[rb][kk], acc[NA - 1][rb]);
                    if (TYPE == 3) acc[NA - 1][rb] = MFMA(gf[kk], xf[rb][kk], acc[NA - 1][rb]);
                }
            }
    };
    if (w == 0) TL(0, 1, 100);
    bar_only();
    if (w == 0) TL(0, 1, 101);
    rdh(0, 0, xa, wa, ga);
#pragma unroll 1
    for (int s = 0; s < 7; ++s) {
        rdh(s, 1, xb, wb, gb); mm(xa, wa, ga); interleave<4 * NA, 2>(); SB();
        if (w == 0) TL(0, 1, 2 * s);
        bar_only();
        if (w == 0) TL(0, 1, 2 * s + 1);
        rdh(s + 1, 0, xa, wa, ga); mm(xb, wb, gb); interleave<4 * NA, 2>(); SB();
    }
    rdh(7, 1, xb, wb, gb); mm(xa, wa, ga); interleave<4 * NA, 2>(); SB();
    mm(xb, wb, gb);
    STAMP(0, 1);
    if (w == 0) TL(0, 1, 102);
    char* kv = ws + WS_KV;
#pragma unroll
    for (int rb = 0; rb < 2; ++rb) {
        const int ttile = mt * 4 + rh * 2 + rb;
        if (TYPE == 0) {
            const int hd0 = 4 * p + 2 * ch;
            char* qf = ws + WS_QF;
            const f32x16 &q = acc[0][rb], &k = acc[1 % NA][rb];
            *(u32x4*)(qf + ((size_t)(hd0 * 64 + ttile) * 1024) + l * 16) = pack8(q, 0, QSCALE);
            *(u32x4*)(qf + ((size_t)((hd0 + 1) * 64 + ttile) * 1024) + l * 16) = pack8(q, 8, QSCALE);
            *(u32x4*)(kv + ((size_t)(hd0 * 64 + ttile) * 2048) + l * 16) = pack8(k, 0, 1.0f);
            *(u32x4*)(kv + ((size_t)((hd0 + 1) * 64 + ttile) * 2048) + l * 16) = pack8(k, 8, 1.0f);
            float n0 = 0.f, n1 = 0.f;
#pragma unroll
            for (int r = 0; r < 8; ++r) { n0 = __builtin_fmaf(k[r], k[r], n0); n1 = __builtin_fmaf(k[8 + r], k[8 + r], n1); }
            n0 = wave_max_fast(swap32_sum(n0)); n1 = wave_max_fast(swap32_sum(n1));
            if (l == 0) { float* km = (float*)(ws + WS_KMAX) + ttile * 32 + hd0; km[0] = n0 * 1.01f; km[1] = n1 * 1.01f; }
        } else {
            const int hd = 4 * p + 2 * ch + (r32 >> 4), d = r32 & 15;
            char* vp = kv + ((size_t)(hd * 64 + ttile) * 2048) + 1024 + h * 256 + d * 16;
            *(u32x4*)(vp) = pack8(acc[0][rb], 0, 1.0f); *(u32x4*)(vp + 512) = pack8(acc[0][rb], 8, 1.0f);
            if (TYPE == 3 && ch == 0 && h == 0)
                ((float*)(ws + WS_GATE))[ttile * 32 + r32] = __builtin_amdgcn_rcpf(1.0f + __builtin_amdgcn_exp2f(-1.4426950408889634f * acc[NA - 1][rb][0]));
        }
        if (TYPE <= 1) {
            f32x16& z = acc[NA - 1][rb];
            const int zg = TYPE == 0 ? p : 8 + p;
            float s1 = 0.f, s2 = 0.f;
#pragma unroll
            for (int r = 0; r < 16; ++r) { const float v = gelu_tanh(z[r]); z[r] = v; s1 += v; s2 = __builtin_fmaf(v, v, s2); }
            const int fb = 2 * zg + ch;
            char* zp = ws + WS_Z2 + ((size_t)(ttile * 56 + 2 * fb) * 1024);
            *(u32x4*)(zp + l * 16) = pack8(z, 0, 1.0f); *(u32x4*)(zp + 1024 + l * 16) = pack8(z, 8, 1.0f);
            s1 = swap32_sum(s1); s2 = swap32_sum(s2);
            if (h == 0) ((f32x2*)(ws + WS_STATS))[(size_t)(ttile * 32 + r32) * 28 + fb] = f32x2{s1, s2};
        }
    }
    if (w == 0) TL(0, 1, 103);
}

__global__ __launch_bounds__(512) void k1_proj(const u16* __restrict__ x, const u16* __restrict__ wqkv, const u16* __restrict__ w_in,
                                               const u16* __restrict__ gate_w, char* __restrict__ ws) {
    __shared__ __attribute__((aligned(1024))) char lds[K1_LDS];
    const int b = blockIdx.x, xcd = b & 7, idx = b >> 3;
    const int mt = (xcd & 1) * 8 + (idx & 7), nt = (xcd >> 1) * 4 + (idx >> 3), p = nt >> 1;
    STAMP(0, 0);
    if (!(nt & 1)) k1_body<0>(x, wqkv, w_in, gate_w, ws, lds, mt, p, threadIdx.x);
    else if (p < 6) k1_body<1>(x, wqkv, w_in, gate_w, ws, lds, mt, p, threadIdx.x);
    else if (p == 6) k1_body<2>(x, wqkv, w_in, gate_w, ws, lds, mt, p, threadIdx.x);
    else k1_body<3>(x, wqkv, w_in, gate_w, ws, lds, mt, p, threadIdx.x);
    STAMP(0, 2);
}

DI void ln_apply(char* __restrict__ ws, char* lds, int unit, int tid) {
    const int l = tid & 63, w = (tid >> 6) & 3, h = l >> 5, r32 = l & 31;
    const int ttile = unit >> 2, qu = unit & 3;
    const f32x4* st = (const f32x4*)(ws + WS_STATS) + (size_t)(ttile * 32 + r32) * 14 + h * 7;
    float s1 = 0.f, s2 = 0.f;
#pragma unroll
    for (int i = 0; i < 7; ++i) { const f32x4 v = st[i]; s1 += v[0] + v[2]; s2 += v[1] + v[3]; }
    s1 = swap32_sum(s1); s2 = swap32_sum(s2);
    const float mean = s1 * (1.0f / FH);
    const float var = fmaxf(s2 * (1.0f / FH) - mean * mean, 0.f);
    const float rstd = __builtin_amdgcn_rsqf(var + 1e-5f), nm = -mean * rstd;
    lds_ptr tile = (lds_ptr)lds + w * 2048;
    const int q4 = (l & 15) >> 2, p4 = l & 3, b16 = (l >> 4) & 1;
    for (int i = w; i < 7; i += 4) {
        const int fb = qu * 7 + i;
#pragma unroll
        for (int g = 0; g < 2; ++g) {
            const u32x4 v = *(const u32x4*)(ws + WS_Z2 + ((size_t)(ttile * 56 + 2 * fb + g) * 1024) + l * 16);
            unsigned o[4];
#pragma unroll
            for (int e = 0; e < 4; ++e) o[e] = cvtpk(__builtin_fmaf(bf_lo(v[e]), rstd, nm), __builtin_fmaf(bf_hi(v[e]), rstd, nm));
            auto r0 = __builtin_amdgcn_permlane32_swap(o[0], o[2], false, false);
            auto r1 = __builtin_amdgcn_permlane32_swap(o[1], o[3], false, false);
            const u32x4 ov = {r0[0], r1[0], r0[1], r1[1]};
            *(LDS_AS u32x4*)(tile + r32 * 64 + g * 32 + h * 16) = ov;
        }
        asm volatile("" ::: "memory");
#pragma unroll
        for (int kk = 0; kk < 2; ++kk) {
            lds_cptr rp = (lds_cptr)tile + (16 * kk + 8 * h + q4) * 64 + b16 * 32 + p4 * 8;
            const bf16x8 fr = trfrag(rp, rp + 256);
            *(bf16x8*)(ws + WS_Z2N + ((size_t)(fb * 128 + ttile * 2 + kk) * 1024) + l * 16) = fr;
        }
        asm volatile("" ::: "memory");
    }
}
__global__ __launch_bounds__(256) void k_ln(char* __restrict__ ws) {
    __shared__ __attribute__((aligned(1024))) char lds[8192];
    STAMP(1, 0); ln_apply(ws, lds, blockIdx.x, threadIdx.x); STAMP(1, 2);
}

constexpr int AT_TS = 2304, AT_SLOT = 8 * AT_TS, AT_LDS = 4 * AT_SLOT + 64;
#define ATTN_ASM \
    "s_mov_b32 %[sv], m0\n\t" \
    "v_mov_b32 v88, 0\n\t" \
    "v_mov_b32 v89, 0\n\t" \
    "v_mov_b32 v90, 0\n\t" \
    "v_mov_b32 v91, 0\n\t" \
    "v_mov_b32 v96, 0\n\t" \
    "v_mov_b32 v97, 0\n\t" \
    "v_mov_b32 v98, 0\n\t" \
    "v_mov_b32 v99, 0\n\t" \
    "v_mov_b32 v0, 0\n\t" \
    "v_mov_b32 v1, 0\n\t" \
    "v_mov_b32 v2, 0\n\t" \
    "v_mov_b32 v3, 0\n\t" \
    "v_mov_b32 v4, 0\n\t" \
    "v_mov_b32 v5, 0\n\t" \
    "v_mov_b32 v6, 0\n\t" \
    "v_mov_b32 v7, 0\n\t" \
    "v_mov_b32 v8, 0\n\t" \
    "v_mov_b32 v9, 0\n\t" \
    "v_mov_b32 v10, 0\n\t" \
    "v_mov_b32 v11, 0\n\t" \
    "v_mov_b32 v12, 0\n\t" \
    "v_mov_b32 v13, 0\n\t" \
    "v_mov_b32 v14, 0\n\t" \
    "v_mov_b32 v15, 0\n\t" \
    "s_mov_b32 %[cnt], 0\n\t" \
    "s_waitcnt vmcnt(4) lgkmcnt(0)\n\t" \
    "s_barrier\n\t" \
    "ds_read_b128 v[68:71], %[kl] offset:0\n\t" \
    "ds_read_b128 v[72:75], %[kl] offset:2304\n\t" \
    "ds_read_b128 v[76:79], %[v0l] offset:0\n\t" \
    "ds_read_b128 v[80:83], %[v1l] offset:0\n\t" \
    "s_waitcnt lgkmcnt(0)\n\t" \
    "v_mfma_f32_32x32x16_bf16 v[32:47], v[68:71], v[64:67], v[16:31]\n\t" \
    "s_nop 7\n\t" \
    "s_nop 3\n\t" \
    "Lloop%=:\n\t" \
    "v_mfma_f32_32x32x16_bf16 v[48:63], v[72:75], v[64:67], v[16:31]\n\t" \
    "v_exp_f32 v32, v32\n\t" \
    "ds_read_b128 v[68:71], %[kl] offset:4608\n\t" \
    "v_exp_f32 v33, v33\n\t" \
    "v_exp_f32 v34, v34\n\t" \
    "v_exp_f32 v35, v35\n\t" \
    "v_exp_f32 v36, v36\n\t" \
    "v_exp_f32 v37, v37\n\t" \
    "v_exp_f32 v38, v38\n\t" \
    "v_mfma_f32_32x32x16_bf16 v[0:15], v[88:91], v[96:99], v[0:15]\n\t" \
    "v_exp_f32 v39, v39\n\t" \
    "ds_read_b128 v[84:87], %[v0l] offset:2304\n\t" \
    "v_cvt_pk_bf16_f32 v92, v32, v33\n\t" \
    "v_cvt_pk_bf16_f32 v93, v34, v35\n\t" \
    "v_cvt_pk_bf16_f32 v94, v36, v37\n\t" \
    "v_cvt_pk_bf16_f32 v95, v38, v39\n\t" \
    "v_exp_f32 v40, v40\n\t" \
    "ds_read_b128 v[88:91], %[v1l] offset:2304\n\t" \
    "v_exp_f32 v41, v41\n\t" \
    "v_exp_f32 v42, v42\n\t" \
    "v_exp_f32 v43, v43\n\t" \
    "s_waitcnt lgkmcnt(4)\n\t" \
    "v_mfma_f32_32x32x16_bf16 v[0:15], v[76:79], v[92:95], v[0:15]\n\t" \
    "v_exp_f32 v44, v44\n\t" \
    "v_exp_f32 v45, v45\n\t" \
    "v_exp_f32 v46, v46\n\t" \
    "v_exp_f32 v47, v47\n\t" \
    "s_waitcnt lgkmcnt(2)\n\t" \
    "v_cvt_pk_bf16_f32 v96, v40, v41\n\t" \
    "v_cvt_pk_bf16_f32 v97, v42, v43\n\t" \
    "v_cvt_pk_bf16_f32 v98, v44, v45\n\t" \
    "v_cvt_pk_bf16_f32 v99, v46, v47\n\t" \
    "v_mfma_f32_32x32x16_bf16 v[32:47], v[68:71], v[64:67], v[16:31]\n\t" \
    "v_exp_f32 v48, v48\n\t" \
    "ds_read_b128 v[72:75], %[kl] offset:6912\n\t" \
    "v_exp_f32 v49, v49\n\t" \
    "v_exp_f32 v50, v50\n\t" \
    "v_exp_f32 v51, v51\n\t" \
    "v_exp_f32 v52, v52\n\t" \
    "v_exp_f32 v53, v53\n\t" \
    "v_exp_f32 v54, v54\n\t" \
    "v_mfma_f32_32x32x16_bf16 v[0:15], v[80:83], v[96:99], v[0:15]\n\t" \
    "v_exp_f32 v55, v55\n\t" \
    "ds_read_b128 v[76:79], %[v0l] offset:4608\n\t" \
    "v_cvt_pk_bf16_f32 v92, v48, v49\n\t" \
    "v_cvt_pk_bf16_f32 v93, v50, v51\n\t" \
    "v_cvt_pk_bf16_f32 v94, v52, v53\n\t" \
    "v_cvt_pk_bf16_f32 v95, v54, v55\n\t" \
    "v_exp_f32 v56, v56\n\t" \
    "ds_read_b128 v[80:83], %[v1l] offset:4608\n\t" \
    "v_exp_f32 v57, v57\n\t" \
    "v_exp_f32 v58, v58\n\t" \
    "v_exp_f32 v59, v59\n\t" \
    "s_waitcnt lgkmcnt(4)\n\t" \
    "v_mfma_f32_32x32x16_bf16 v[0:15], v[84:87], v[92:95], v[0:15]\n\t" \
    "v_exp_f32 v60, v60\n\t" \
    "v_exp_f32 v61, v61\n\t" \
    "v_exp_f32 v62, v62\n\t" \
    "v_exp_f32 v63, v63\n\t" \
    "s_waitcnt lgkmcnt(2)\n\t" \
    "v_cvt_pk_bf16_f32 v96, v56, v57\n\t" \
    "v_cvt_pk_bf16_f32 v97, v58, v59\n\t" \
    "v_cvt_pk_bf16_f32 v98, v60, v61\n\t" \
    "v_cvt_pk_bf16_f32 v99, v62, v63\n\t" \
    "v_mfma_f32_32x32x16_bf16 v[48:63], v[72:75], v[64:67], v[16:31]\n\t" \
    "v_exp_f32 v32, v32\n\t" \
    "ds_read_b128 v[68:71], %[kl] offset:9216\n\t" \
    "v_exp_f32 v33, v33\n\t" \
    "v_exp_f32 v34, v34\n\t" \
    "v_exp_f32 v35, v35\n\t" \
    "v_exp_f32 v36, v36\n\t" \
    "v_exp_f32 v37, v37\n\t" \
    "v_exp_f32 v38, v38\n\t" \
    "v_mfma_f32_32x32x16_bf16 v[0:15], v[88:91], v[96:99], v[0:15]\n\t" \
    "v_exp_f32 v39, v39\n\t" \
    "ds_read_b128 v[84:87], %[v0l] offset:6912\n\t" \
    "v_cvt_pk_bf16_f32 v92, v32, v33\n\t" \
    "v_cvt_pk_bf16_f32 v93, v34, v35\n\t" \
    "v_cvt_pk_bf16_f32 v94, v36, v37\n\t" \
    "v_cvt_pk_bf16_f32 v95, v38, v39\n\t" \
    "v_exp_f32 v40, v40\n\t" \
    "ds_read_b128 v[88:91], %[v1l] offset:6912\n\t" \
    "v_exp_f32 v41, v41\n\t" \
    "v_exp_f32 v42, v42\n\t" \
    "v_exp_f32 v43, v43\n\t" \
    "s_waitcnt lgkmcnt(4)\n\t" \
    "v_mfma_f32_32x32x16_bf16 v[0:15], v[76:79], v[92:95], v[0:15]\n\t" \
    "v_exp_f32 v44, v44\n\t" \
    "v_exp_f32 v45, v45\n\t" \
    "v_exp_f32 v46, v46\n\t" \
    "v_exp_f32 v47, v47\n\t" \
    "s_waitcnt lgkmcnt(2)\n\t" \
    "v_cvt_pk_bf16_f32 v96, v40, v41\n\t" \
    "v_cvt_pk_bf16_f32 v97, v42, v43\n\t" \
    "v_cvt_pk_bf16_f32 v98, v44, v45\n\t" \
    "v_cvt_pk_bf16_f32 v99, v46, v47\n\t" \
    "v_mfma_f32_32x32x16_bf16 v[32:47], v[68:71], v[64:67], v[16:31]\n\t" \
    "v_exp_f32 v48, v48\n\t" \
    "ds_read_b128 v[72:75], %[kl] offset:11520\n\t" \
    "v_exp_f32 v49, v49\n\t" \
    "v_exp_f32 v50, v50\n\t" \
    "v_exp_f32 v51, v51\n\t" \
    "v_exp_f32 v52, v52\n\t" \
    "v_exp_f32 v53, v53\n\t" \
    "v_exp_f32 v54, v54\n\t" \
    "v_mfma_f32_32x32x16_bf16 v[0:15], v[80:83], v[96:99], v[0:15]\n\t" \
    "v_exp_f32 v55, v55\n\t" \
    "ds_read_b128 v[76:79], %[v0l] offset:9216\n\t" \
    "v_cvt_pk_bf16_f32 v92, v48, v49\n\t" \
    "v_cvt_pk_bf16_f32 v93, v50, v51\n\t" \
    "v_cvt_pk_bf16_f32 v94, v52, v53\n\t" \
    "v_cvt_pk_bf16_f32 v95, v54, v55\n\t" \
    "v_exp_f32 v56, v56\n\t" \
    "ds_read_b128 v[80:83], %[v1l] offset:9216\n\t" \
    "v_exp_f32 v57, v57\n\t" \
    "v_exp_f32 v58, v58\n\t" \
    "v_exp_f32 v59, v59\n\t" \
    "s_waitcnt lgkmcnt(4)\n\t" \
    "v_mfma_f32_32x32x16_bf16 v[0:15], v[84:87], v[92:95], v[0:15]\n\t" \
    "v_exp_f32 v60, v60\n\t" \
    "v_exp_f32 v61, v61\n\t" \
    "v_exp_f32 v62, v62\n\t" \
    "v_exp_f32 v63, v63\n\t" \
    "s_waitcnt lgkmcnt(2)\n\t" \
    "v_cvt_pk_bf16_f32 v96, v56, v57\n\t" \
    "v_cvt_pk_bf16_f32 v97, v58, v59\n\t" \
    "v_cvt_pk_bf16_f32 v98, v60, v61\n\t" \
    "v_cvt_pk_bf16_f32 v99, v62, v63\n\t" \
    "v_mfma_f32_32x32x16_bf16 v[48:63], v[72:75], v[64:67], v[16:31]\n\t" \
    "v_exp_f32 v32, v32\n\t" \
    "ds_read_b128 v[68:71], %[kl] offset:13824\n\t" \
    "v_exp_f32 v33, v33\n\t" \
    "v_exp_f32 v34, v34\n\t" \
    "v_exp_f32 v35, v35\n\t" \
    "v_exp_f32 v36, v36\n\t" \
    "v_exp_f32 v37, v37\n\t" \
    "v_exp_f32 v38, v38\n\t" \
    "v_mfma_f32_32x32x16_bf16 v[0:15], v[88:91], v[96:99], v[0:15]\n\t" \
    "v_exp_f32 v39, v39\n\t" \
    "ds_read_b128 v[84:87], %[v0l] offset:11520\n\t" \
    "v_cvt_pk_bf16_f32 v92, v32, v33\n\t" \
    "v_cvt_pk_bf16_f32 v93, v34, v35\n\t" \
    "v_cvt_pk_bf16_f32 v94, v36, v37\n\t" \
    "v_cvt_pk_bf16_f32 v95, v38, v39\n\t" \
    "v_exp_f32 v40, v40\n\t" \
    "ds_read_b128 v[88:91], %[v1l] offset:11520\n\t" \
    "v_exp_f32 v41, v41\n\t" \
    "v_exp_f32 v42, v42\n\t" \
    "v_exp_f32 v43, v43\n\t" \
    "s_waitcnt lgkmcnt(4)\n\t" \
    "v_mfma_f32_32x32x16_bf16 v[0:15], v[76:79], v[92:95], v[0:15]\n\t" \
    "v_exp_f32 v44, v44\n\t" \
    "v_exp_f32 v45, v45\n\t" \
    "v_exp_f32 v46, v46\n\t" \
    "v_exp_f32 v47, v47\n\t" \
    "s_waitcnt lgkmcnt(2)\n\t" \
    "v_cvt_pk_bf16_f32 v96, v40, v41\n\t" \
    "v_cvt_pk_bf16_f32 v97, v42, v43\n\t" \
    "v_cvt_pk_bf16_f32 v98, v44, v45\n\t" \
    "v_cvt_pk_bf16_f32 v99, v46, v47\n\t" \
    "v_mfma_f32_32x32x16_bf16 v[32:47], v[68:71], v[64:67], v[16:31]\n\t" \
    "v_exp_f32 v48, v48\n\t" \
    "ds_read_b128 v[72:75], %[kl] offset:16128\n\t" \
    "s_and_b32 %[t0], %[cnt], 3\n\t" \
    "s_cmp_eq_u32 %[t0], 3\n\t" \
    "s_mov_b32 %[dl], 18432\n\t" \
    "s_cselect_b32 %[dl], -55296, %[dl]\n\t" \
    "v_add_u32 %[kl], %[dl], %[kl]\n\t" \
    "v_exp_f32 v49, v49\n\t" \
    "v_exp_f32 v50, v50\n\t" \
    "v_exp_f32 v51, v51\n\t" \
    "v_exp_f32 v52, v52\n\t" \
    "v_exp_f32 v53, v53\n\t" \
    "v_exp_f32 v54, v54\n\t" \
    "v_mfma_f32_32x32x16_bf16 v[0:15], v[80:83], v[96:99], v[0:15]\n\t" \
    "v_exp_f32 v55, v55\n\t" \
    "ds_read_b128 v[76:79], %[v0l] offset:13824\n\t" \
    "v_cvt_pk_bf16_f32 v92, v48, v49\n\t" \
    "v_cvt_pk_bf16_f32 v93, v50, v51\n\t" \
    "v_cvt_pk_bf16_f32 v94, v52, v53\n\t" \
    "v_cvt_pk_bf16_f32 v95, v54, v55\n\t" \
    "v_exp_f32 v56, v56\n\t" \
    "ds_read_b128 v[80:83], %[v1l] offset:13824\n\t" \
    "v_exp_f32 v57, v57\n\t" \
    "v_exp_f32 v58, v58\n\t" \
    "v_exp_f32 v59, v59\n\t" \
    "s_waitcnt lgkmcnt(4)\n\t" \
    "v_mfma_f32_32x32x16_bf16 v[0:15], v[84:87], v[92:95], v[0:15]\n\t" \
    "v_exp_f32 v60, v60\n\t" \
    "v_exp_f32 v61, v61\n\t" \
    "v_exp_f32 v62, v62\n\t" \
    "v_exp_f32 v63, v63\n\t" \
    "s_waitcnt lgkmcnt(2)\n\t" \
    "v_cvt_pk_bf16_f32 v96, v56, v57\n\t" \
    "v_cvt_pk_bf16_f32 v97, v58, v59\n\t" \
    "v_cvt_pk_bf16_f32 v98, v60, v61\n\t" \
    "v_cvt_pk_bf16_f32 v99, v62, v63\n\t" \
    "s_cmp_eq_u32 %[cnt], 7\n\t" \
    "s_cbranch_scc1 Lskip%=\n\t" \
    "s_cmp_eq_u32 %[cnt], 6\n\t" \
    "s_cbranch_scc1 Lw0%=\n\t" \
    "s_waitcnt vmcnt(2)\n\t" \
    "s_barrier\n\t" \
    "s_cmp_gt_u32 %[cnt], 4\n\t" \
    "s_cbranch_scc1 Lskip%=\n\t" \
    "s_add_u32 %[t0], %[cnt], 3\n\t" \
    "s_and_b32 %[t0], %[t0], 3\n\t" \
    "s_mul_i32 %[t0], %[t0], 18432\n\t" \
    "s_add_u32 m0, %[t0], %[dst]\n\t" \
    "s_setprio 3\n\t" \
    "global_load_lds_dwordx4 %[goff], %[base]\n\t" \
    "global_load_lds_dwordx4 %[goff], %[base] offset:1024\n\t" \
    "s_setprio 0\n\t" \
    "v_add_u32 %[goff], 0x4000, %[goff]\n\t" \
    "s_branch Lskip%=\n\t" \
    "Lw0%=:\n\t" \
    "s_waitcnt vmcnt(0)\n\t" \
    "s_barrier\n\t" \
    "Lskip%=:\n\t" \
    "v_mfma_f32_32x32x16_bf16 v[48:63], v[72:75], v[64:67], v[16:31]\n\t" \
    "v_exp_f32 v32, v32\n\t" \
    "ds_read_b128 v[68:71], %[kl] offset:0\n\t" \
    "v_exp_f32 v33, v33\n\t" \
    "v_exp_f32 v34, v34\n\t" \
    "v_exp_f32 v35, v35\n\t" \
    "v_exp_f32 v36, v36\n\t" \
    "v_exp_f32 v37, v37\n\t" \
    "v_exp_f32 v38, v38\n\t" \
    "v_mfma_f32_32x32x16_bf16 v[0:15], v[88:91], v[96:99], v[0:15]\n\t" \
    "v_exp_f32 v39, v39\n\t" \
    "ds_read_b128 v[84:87], %[v0l] offset:16128\n\t" \
    "v_cvt_pk_bf16_f32 v92, v32, v33\n\t" \
    "v_cvt_pk_bf16_f32 v93, v34, v35\n\t" \
    "v_cvt_pk_bf16_f32 v94, v36, v37\n\t" \
    "v_cvt_pk_bf16_f32 v95, v38, v39\n\t" \
    "v_exp_f32 v40, v40\n\t" \
    "ds_read_b128 v[88:91], %[v1l] offset:16128\n\t" \
    "v_add_u32 %[v0l], %[dl], %[v0l]\n\t" \
    "v_add_u32 %[v1l], %[dl], %[v1l]\n\t" \
    "v_exp_f32 v41, v41\n\t" \
    "v_exp_f32 v42, v42\n\t" \
    "v_exp_f32 v43, v43\n\t" \
    "s_waitcnt lgkmcnt(4)\n\t" \
    "v_mfma_f32_32x32x16_bf16 v[0:15], v[76:79], v[92:95], v[0:15]\n\t" \
    "v_exp_f32 v44, v44\n\t" \
    "v_exp_f32 v45, v45\n\t" \
    "v_exp_f32 v46, v46\n\t" \
    "v_exp_f32 v47, v47\n\t" \
    "s_waitcnt lgkmcnt(2)\n\t" \
    "v_cvt_pk_bf16_f32 v96, v40, v41\n\t" \
    "v_cvt_pk_bf16_f32 v97, v42, v43\n\t" \
    "v_cvt_pk_bf16_f32 v98, v44, v45\n\t" \
    "v_cvt_pk_bf16_f32 v99, v46, v47\n\t" \
    "v_mfma_f32_32x32x16_bf16 v[32:47], v[68:71], v[64:67], v[16:31]\n\t" \
    "v_exp_f32 v48, v48\n\t" \
    "ds_read_b128 v[72:75], %[kl] offset:2304\n\t" \
    "v_exp_f32 v49, v49\n\t" \
    "v_exp_f32 v50, v50\n\t" \
    "v_exp_f32 v51, v51\n\t" \
    "v_exp_f32 v52, v52\n\t" \
    "v_exp_f32 v53, v53\n\t" \
    "v_exp_f32 v54, v54\n\t" \
    "v_mfma_f32_32x32x16_bf16 v[0:15], v[80:83], v[96:99], v[0:15]\n\t" \
    "v_exp_f32 v55, v55\n\t" \
    "ds_read_b128 v[76:79], %[v0l] offset:0\n\t" \
    "v_cvt_pk_bf16_f32 v92, v48, v49\n\t" \
    "v_cvt_pk_bf16_f32 v93, v50, v51\n\t" \
    "v_cvt_pk_bf16_f32 v94, v52, v53\n\t" \
    "v_cvt_pk_bf16_f32 v95, v54, v55\n\t" \
    "v_exp_f32 v56, v56\n\t" \
    "ds_read_b128 v[80:83], %[v1l] offset:0\n\t" \
    "v_exp_f32 v57, v57\n\t" \
    "v_exp_f32 v58, v58\n\t" \
    "v_exp_f32 v59, v59\n\t" \
    "s_waitcnt lgkmcnt(4)\n\t" \
    "v_mfma_f32_32x32x16_bf16 v[0:15], v[84:87], v[92:95], v[0:15]\n\t" \
    "v_exp_f32 v60, v60\n\t" \
    "v_exp_f32 v61, v61\n\t" \
    "v_exp_f32 v62, v62\n\t" \
    "v_exp_f32 v63, v63\n\t" \
    "s_waitcnt lgkmcnt(2)\n\t" \
    "v_cvt_pk_bf16_f32 v96, v56, v57\n\t" \
    "v_cvt_pk_bf16_f32 v97, v58, v59\n\t" \
    "v_cvt_pk_bf16_f32 v98, v60, v61\n\t" \
    "v_cvt_pk_bf16_f32 v99, v62, v63\n\t" \
    "s_add_u32 %[cnt], %[cnt], 1\n\t" \
    "s_cmp_lt_u32 %[cnt], 8\n\t" \
    "s_cbranch_scc1 Lloop%=\n\t" \
    "s_waitcnt lgkmcnt(0)\n\t" \
    "v_mfma_f32_32x32x16_bf16 v[0:15], v[88:91], v[96:99], v[0:15]\n\t" \
    "s_mov_b32 m0, %[sv]\n\t" \
    "s_nop 7\n\t" \
    "s_nop 7\n\t"
#define ATTN_CLOBBERS "v32", "v33", "v34", "v35", "v36", "v37", "v38", "v39", "v40", "v41", "v42", "v43", "v44", "v45", "v46", "v47", "v48", "v49", "v50", "v51", "v52", "v53", "v54", "v55", "v56", "v57", "v58", "v59", "v60", "v61", "v62", "v63", "v68", "v69", "v70", "v71", "v72", "v73", "v74", "v75", "v76", "v77", "v78", "v79", "v80", "v81", "v82", "v83", "v84", "v85", "v86", "v87", "v88", "v89", "v90", "v91", "v92", "v93", "v94", "v95", "v96", "v97", "v98", "v99"

DI void attn_block(char* __restrict__ ws, char* lds, int head, int chunk, int tid) {
    const int l = tid & 63, w = __builtin_amdgcn_readfirstlane(tid >> 6), h = l >> 5;
    const int qtile = chunk * 8 + w;
    const unsigned lds0 = (unsigned)(uintptr_t)lds;
    const char* kvbase = ws + WS_KV + (size_t)head * 64 * 2048;
    const unsigned dst = lds0 + w * AT_TS;
    bf16x8 qf; float km, gate;
    asm volatile("global_load_dwordx4 %0, %3, off\n\tglobal_load_dword %1, %4, off\n\tglobal_load_dword %2, %5, off"
                 : "=&v"(qf), "=&v"(km), "=&v"(gate)
                 : "v"(ws + WS_QF + ((size_t)(head * 64 + qtile) * 1024) + l * 16), "v"((const float*)(ws + WS_KMAX) + l * 32 + head),
                   "v"((const float*)(ws + WS_GATE) + qtile * 32 + (l & 31)) : "memory");
    auto issue3 = [&]() {
#pragma unroll
        for (int c = 0; c < 3; ++c) { glds16(kvbase + c * 16384 + w * 2048 + l * 16, dst + c * AT_SLOT); glds16(kvbase + c * 16384 + w * 2048 + 1024 + l * 16, dst + c * AT_SLOT + 1024); }
    };
    issue3();
    {
        const u32x4 ones = {0x3f803f80u, 0x3f803f80u, 0x3f803f80u, 0x3f803f80u};
        *(LDS_AS u32x4*)((lds_ptr)lds + (tid >> 4) * AT_TS + 2048 + (tid & 15) * 16) = ones;
        if (tid == 0) *(LDS_AS unsigned*)((lds_ptr)lds + 4 * AT_SLOT) = 0u;
    }
    asm volatile("s_waitcnt vmcnt(6)" : "+v"(qf), "+v"(km), "+v"(gate));
    float qn = 0.f;
    { const u32x4 qu = __builtin_bit_cast(u32x4, qf);
#pragma unroll
      for (int e = 0; e < 4; ++e) { qn = __builtin_fmaf(bf_lo(qu[e]), bf_lo(qu[e]), qn); qn = __builtin_fmaf(bf_hi(qu[e]), bf_hi(qu[e]), qn); } }
    qn = swap32_sum(qn);
    float ref = __builtin_amdgcn_sqrtf(qn * wave_max_fast(km));
    unsigned kl = lds0 + l * 16;
    const bool onesl = (l & 16) != 0;
    const unsigned ol = lds0 + 2048 + ((((l & 15) >= 4) && ((l & 15) < 12)) ? 64 : 0);
    unsigned v0l = onesl ? ol : lds0 + 1024 + h * 256 + (l & 15) * 16, v1l = onesl ? ol : v0l + 512;
    LDS_AS volatile unsigned* flag = (LDS_AS volatile unsigned*)((lds_ptr)lds + 4 * AT_SLOT);
    char* optr = ws + WS_HO + ((size_t)(qtile * 88 + head) * 1024) + l * 16;
#pragma unroll 1
    for (int pass = 0;; ++pass) {
        f32x16 negm, o;
#pragma unroll
        for (int r = 0; r < 16; ++r) negm[r] = -ref;
        unsigned sv, cnt, t0, dl, goff = 3 * 16384 + w * 2048 + l * 16;
        STAMP(2, 1);
        asm volatile(ATTN_ASM
                     : "=&{v[0:15]}"(o), [sv] "=&s"(sv), [cnt] "=&s"(cnt), [t0] "=&s"(t0), [dl] "=&s"(dl), [goff] "+v"(goff), [kl] "+v"(kl), [v0l] "+v"(v0l), [v1l] "+v"(v1l)
                     : "{v[16:31]}"(negm), "{v[64:67]}"(qf), [dst] "s"(dst), [base] "s"(kvbase)
                     : ATTN_CLOBBERS, "scc", "memory");
        STAMP(2, 3);
        *(u32x4*)optr = pack8(o, 0, gate * __builtin_amdgcn_rcpf(o[8]));
        if (pass == 1) break;
        const bool bad = !(o[8] >= 9.094947e-13f);
        if (__any(bad) && l == 0) *flag = 1u;
        __syncthreads();
        if (*flag == 0u) break;
        float m = -3.0e38f;
        for (int t = 0; t < 64; ++t) {
            const bf16x8 kf = *(const bf16x8*)(kvbase + (size_t)t * 2048 + l * 16);
            const f32x16 s = MFMA(kf, qf, f32x16{});
#pragma unroll
            for (int r = 0; r < 16; ++r) m = fmaxf(m, s[r]);
        }
        ref = swap32_max(m);
        asm volatile("s_waitcnt vmcnt(0)" ::: "memory");
        issue3();
    }
}
__global__ __launch_bounds__(512, 4) void k_attn(char* __restrict__ ws) {
    __shared__ __attribute__((aligned(1024))) char lds[AT_LDS];
    const int b = blockIdx.x;
    STAMP(2, 0);
    attn_block(ws, lds, (b & 7) * 4 + ((b >> 3) & 3), b >> 5, threadIdx.x);
    STAMP(2, 2);
}

constexpr int SP_ST = 24576, SP_LDS = 3 * SP_ST;

DI void spatial_block(const u16* __restrict__ x, const u16* __restrict__ w_in, const u16* __restrict__ w_sp, const u16* __restrict__ b_sp,
                      char* __restrict__ ws, char* lds, int tt, int ft, int rot, int tid) {
    if (tid == 0) TL(3, 1, 99);
    const int l = tid & 63, w = __builtin_amdgcn_readfirstlane(tid >> 6), h = l >> 5, r32 = l & 31;
    if (w >= 4) {
        const int j = w - 4, row = 8 * j + (l >> 3);
        const unsigned lds0 = (unsigned)(uintptr_t)lds;
        const int bch = ((l & 7) ^ ((row >> 1) & 7)) * 8;
        {
            const unsigned xv = (unsigned)(row * D_ + bch) * 2;
            const char* xu = (const char*)(x + (size_t)tt * 64 * D_);
            const int wr = 4 * j + (l >> 4), wc = ((((l & 15) >> 2) ^ (wr & 3)) << 2) | (l & 3);
            const unsigned wv = (unsigned)(wr * 1792 + wc * 8) * 2;
            const char* wu = (const char*)(w_in + ft * 128);
            auto issue = [&](int s) {
                const unsigned dst = lds0 + (s % 3) * SP_ST + j * 1024; const int sr = (s + rot) & 7;
                glds16s(xu + sr * 128, xv, dst); glds16s(xu + 32 * D_ * 2 + sr * 128, xv, dst + 4096);
#pragma unroll
                for (int i = 0; i < 4; ++i) glds16s(wu + (size_t)(sr * 64 + 16 * i) * 3584, wv, dst + 8192 + i * 4096);
            };
            issue(0); issue(1); issue(2);
            wait_bar<12>();
#pragma unroll 1
            for (int s = 0; s < 7; ++s) {
                if (s + 2 < 8) wait_bar<6>(); else wait_bar<0>();
                if (s + 3 < 8) issue(s + 3);
            }
            bar_only();
        }
        const unsigned av = (unsigned)(row * S_ + bch) * 2;
        const char* au = (const char*)(w_sp + (size_t)tt * 64 * S_);
        auto issue = [&](int s) {
            const unsigned dst = lds0 + (s % 6) * 8192 + j * 1024; const int sr = (s + rot) & 31;
            glds16s(au + sr * 128, av, dst); glds16s(au + 32 * S_ * 2 + sr * 128, av, dst + 4096);
        };
#pragma unroll 1
        for (int s = 0; s < 6; ++s) issue(s);
        wait_bar<10>();
#pragma unroll 1
        for (int s = 0; s < 31; ++s) {
            if (j == 0) TL(3, 0, 3 * s);
            if (s < 27) asm volatile("s_waitcnt vmcnt(8)" ::: "memory"); else if (s == 27) asm volatile("s_waitcnt vmcnt(6)" ::: "memory"); else if (s == 28) asm volatile("s_waitcnt vmcnt(4)" ::: "memory");
            else if (s == 29) asm volatile("s_waitcnt vmcnt(2)" ::: "memory"); else asm volatile("s_waitcnt vmcnt(0)" ::: "memory");
            if (j == 0) TL(3, 0, 3 * s + 1);
            bar_only();
            if (j == 0) TL(3, 0, 3 * s + 2);
            if (s + 6 < 32) issue(s + 6);
        }
        return;
    }
    if (GPRIO) __builtin_amdgcn_s_setprio(GPRIO);
    const int fb = w;
    int boff[4];
#pragma unroll
    for (int ks = 0; ks < 4; ++ks) { const int f = (r32 >> 1) & 7; boff[ks] = r32 * 128 + (((ks * 2 + h) ^ f) << 4); }
    const int q4 = (l & 15) >> 2, p4 = l & 3, b16 = (l >> 4) & 1;
    const int fbg = ft * 4 + fb, fg = 2 * fbg;
    const char* zsrc = ws + WS_Z2N + (size_t)fbg * 128 * 1024;
    const unsigned lo = l * 16;
    LDS_AS u32x4* z1p = (LDS_AS u32x4*)((lds_ptr)lds + 49152 + w * 4096) + l;
    {
        f32x16 acc[2]; acc[0] = f32x16{}; acc[1] = f32x16{};
        const int zoff = 8192 + (8 * h + q4) * 256 + ((fb ^ q4) << 6) + b16 * 32 + p4 * 8;
        bf16x8 aa[2], ba[2][2], ab[2], bb[2][2];
        auto rdh = [&](int s, int half, bf16x8 (&af)[2], bf16x8 (&bf)[2][2]) {
            lds_cptr base = (lds_cptr)lds + (s % 3) * SP_ST;
#pragma unroll
            for (int kk = 0; kk < 2; ++kk) {
                const int ks = 2 * half + kk;
                af[kk] = trfrag(base + zoff + ks * 4096, base + zoff + ks * 4096 + 1024);
                bf[0][kk] = ldsfrag(base + boff[ks]); bf[1][kk] = ldsfrag(base + boff[ks] + 4096);
            }
        };
        auto mm = [&](bf16x8 (&af)[2], bf16x8 (&bf)[2][2]) {
#pragma unroll
            for (int kk = 0; kk < 2; ++kk) { acc[0] = MFMA(af[kk], bf[0][kk], acc[0]); acc[1] = MFMA(af[kk], bf[1][kk], acc[1]); }
        };
        if (w == 0) TL(3, 1, 100);
        bar_only();
        if (w == 0) TL(3, 1, 101);
        rdh(0, 0, aa, ba);
#pragma unroll 1
        for (int s = 0; s < 7; ++s) {
            rdh(s, 1, ab, bb); mm(aa, ba); interleave<4, 2>(); SB();
            bar_only();
            rdh(s + 1, 0, aa, ba); mm(ab, bb); interleave<4, 2>(); SB();
        }
        rdh(7, 1, ab, bb); mm(aa, ba); interleave<4, 2>(); SB();
        mm(ab, bb);
        bar_only();
        if (w == 0) TL(3, 1, 102);
#pragma unroll
        for (int tb = 0; tb < 2; ++tb) {
            const float og = 1.0f - ((const float*)(ws + WS_GATE))[(tt * 2 + tb) * 32 + r32];
#pragma unroll
            for (int g = 0; g < 2; ++g) {
                u32x4 zv;
#pragma unroll
                for (int e = 0; e < 4; ++e) zv[e] = cvtpk(gelu_tanh(acc[tb][8 * g + 2 * e]) * og, gelu_tanh(acc[tb][8 * g + 2 * e + 1]) * og);
                z1p[(tb * 2 + g) * 64] = zv;
            }
        }
    }
    f32x16 acc[2]; acc[0] = f32x16{}; acc[1] = f32x16{};
    {
        bf16x8 za[4], zb[4];
        auto ldz = [&](int s, int ks) {
            const int sr = (min(s, 31) + rot) & 31; return *(const bf16x8*)(zsrc + (size_t)((sr * 4 + ks) * 1024) + lo); };
#pragma unroll
        for (int ks = 0; ks < 4; ++ks) za[ks] = ldz(0, ks);
#pragma unroll
        for (int ks = 0; ks < 4; ++ks) zb[ks] = ldz(1, ks);
        bf16x8 bx[2][2], by[2][2];
        auto rdh = [&](int s, int half, bf16x8 (&bf)[2][2]) {
            lds_cptr base = (lds_cptr)lds + (s % 6) * 8192;
#pragma unroll
            for (int kk = 0; kk < 2; ++kk) { bf[0][kk] = ldsfrag(base + boff[2 * half + kk]); bf[1][kk] = ldsfrag(base + boff[2 * half + kk] + 4096); }
        };
        auto stage = [&](int s, bf16x8 (&z)[4], bool last) {
            rdh(s, 1, by); SB();
            acc[0] = MFMA(z[0], bx[0][0], acc[0]); SB(); PACE_(); acc[1] = MFMA(z[0], bx[1][0], acc[1]); SB(); PACE_();
            acc[0] = MFMA(z[1], bx[0][1], acc[0]); SB(); PACE_(); acc[1] = MFMA(z[1], bx[1][1], acc[1]); SB();
            z[0] = ldz(s + 2, 0); z[1] = ldz(s + 2, 1); SB(); PACE_();
            if (!last) { bar_only(); rdh(s + 1, 0, bx); SB(); }
            acc[0] = MFMA(z[2], by[0][0], acc[0]); SB(); PACE_(); acc[1] = MFMA(z[2], by[1][0], acc[1]); SB(); PACE_();
            acc[0] = MFMA(z[3], by[0][1], acc[0]); SB(); PACE_(); acc[1] = MFMA(z[3], by[1][1], acc[1]); SB();
            z[2] = ldz(s + 2, 2); z[3] = ldz(s + 2, 3); SB(); PACE_();
        };
        if (w == 0) TL(3, 1, 103);
        bar_only();
        if (w == 0) TL(3, 1, 104);
        rdh(0, 0, bx);
#pragma unroll 1
        for (int s = 0; s < 30; s += 2) { stage(s, za, false); stage(s + 1, zb, false); }
        stage(30, za, false); stage(31, zb, true);
    }
    if (w == 0) TL(3, 1, 105);
#pragma unroll
    for (int tb = 0; tb < 2; ++tb) {
        const float bs = __uint_as_float((unsigned)b_sp[(tt * 2 + tb) * 32 + r32] << 16);
#pragma unroll
        for (int g = 0; g < 2; ++g) {
            u32x4 ov; const u32x4 zv = z1p[(tb * 2 + g) * 64];
#pragma unroll
            for (int e = 0; e < 4; ++e) ov[e] = cvtpk((acc[tb][8 * g + 2 * e] + bs) * bf_lo(zv[e]), (acc[tb][8 * g + 2 * e + 1] + bs) * bf_hi(zv[e]));
            *(u32x4*)(ws + WS_HO + ((size_t)((tt * 2 + tb) * 88 + 32 + fg + g) * 1024) + l * 16) = ov;
        }
    }
    if (w == 0) TL(3, 1, 106);
}
DI bool spatial_map(int xc, int i, int& tt, int& ft) { tt = (xc & 3) * 8 + (i & 7); ft = (xc >> 2) * 4 + (i >> 3); return ft < 7; }
__global__ __launch_bounds__(512, 4) void k_spatial(const u16* __restrict__ x, const u16* __restrict__ w_in, const u16* __restrict__ w_sp, const u16* __restrict__ b_sp, char* __restrict__ ws) {
    __shared__ __attribute__((aligned(1024))) char lds[SP_LDS];
    const int b = blockIdx.x;
    int tt, ft;
    if (!spatial_map(b & 7, b >> 3, tt, ft)) return;
    STAMP(3, 0);
    spatial_block(x, w_in, w_sp, b_sp, ws, lds, tt, ft, SP_ROT((b >> 3) & 7, (b >> 6)), threadIdx.x);
    STAMP(3, 2);
}

__global__ __launch_bounds__(512, 4) void k_mid(const u16* __restrict__ x, const u16* __restrict__ w_in, const u16* __restrict__ w_sp, const u16* __restrict__ b_sp, char* __restrict__ ws) {
    __shared__ __attribute__((aligned(1024))) char lds[AT_LDS > SP_LDS ? AT_LDS : SP_LDS];
    const int b = blockIdx.x;
    if (b < 256) {
        STAMP(2, 0);
        attn_block(ws, lds, (b & 7) * 4 + ((b >> 3) & 3), b >> 5, threadIdx.x);
        STAMP(2, 2);
    } else {
        const int g = b - 256;
        int tt, ft;
        if (!spatial_map(g & 7, g >> 3, tt, ft)) return;
        STAMP(5, 0);
        spatial_block(x, w_in, w_sp, b_sp, ws, lds, tt, ft, SP_ROT((g >> 3) & 7, (g >> 6)), threadIdx.x);
        STAMP(5, 2);
    }
}

constexpr int FN_ST = 16384, FN_LDS = 8 * FN_ST;

__global__ __launch_bounds__(768) void k_final(const u16* __restrict__ wo, const u16* __restrict__ w_out, char* __restrict__ ws, u16* __restrict__ out) {
    __shared__ __attribute__((aligned(1024))) char lds[FN_LDS];
    const int tid = threadIdx.x, l = tid & 63, w = __builtin_amdgcn_readfirstlane(tid >> 6), h = l >> 5, r32 = l & 31;
    const int b = blockIdx.x, x = b & 7, i = b >> 3;
    const int tt = x * 4 + (i & 3), dt = i >> 2;
    const int rot = FN_ROT(i, dt);
    STAMP(4, 0);
    if (w == 0) TL(4, 1, 99);
    if (w >= 8) {
        const int j = w - 8;
        const unsigned lds0 = (unsigned)(uintptr_t)lds;
        const int wrow = 8 * j + (l >> 3), wchunk = (l & 7) ^ (((wrow >> 1) & 1) << 2);
        const unsigned wv = (unsigned)(wrow * D_ + wchunk * 8) * 2;
        const char* w0 = (const char*)(wo + dt * 64);
        const char* w1 = (const char*)(w_out + dt * 64);
        auto issue = [&](int s) {
            const unsigned dst = lds0 + (s & 7) * FN_ST + j * 1024;
            int sr = s + rot; if (sr >= 11) sr -= 11;
            const char* wp = sr < 4 ? w0 + (size_t)sr * 128 * D_ * 2 : w1 + (size_t)(sr - 4) * 128 * D_ * 2;
#pragma unroll
            for (int q = 0; q < 4; ++q) glds16s(wp + (size_t)q * 32 * D_ * 2, wv, dst + q * 4096);
        };
#pragma unroll 1
        for (int s = 0; s < 8; ++s) issue(s);
        wait_bar<28>();
#pragma unroll 1
        for (int s = 0; s < 10; ++s) {
            if (j == 0) TL(4, 0, 3 * s);
            if (s <= 3) asm volatile("s_waitcnt vmcnt(24)" ::: "memory"); else if (s == 4) asm volatile("s_waitcnt vmcnt(20)" ::: "memory"); else if (s == 5) asm volatile("s_waitcnt vmcnt(16)" ::: "memory");
            else if (s == 6) asm volatile("s_waitcnt vmcnt(12)" ::: "memory"); else if (s == 7) asm volatile("s_waitcnt vmcnt(8)" ::: "memory");
            else if (s == 8) asm volatile("s_waitcnt vmcnt(4)" ::: "memory"); else asm volatile("s_waitcnt vmcnt(0)" ::: "memory");
            if (j == 0) TL(4, 0, 3 * s + 1);
            bar_only();
            if (j == 0) TL(4, 0, 3 * s + 2);
            if (s + 8 < 11) issue(s + 8);
        }
        return;
    }
    const int tb = w & 1, kq = w >> 1;
    const int q4 = (l & 15) >> 2, p4 = l & 3, b16 = (l >> 4) & 1;
    const int woff0 = (kq * 32 + 4 * h + q4) * 128 + (((0 + b16 * 2 + (p4 >> 1)) ^ ((q4 >> 1) << 2)) << 4) + (p4 & 1) * 8, woff1 = woff0 ^ 64;
    const char* hsrc = ws + WS_HO + ((size_t)((tt * 2 + tb) * 88 + 2 * kq) * 1024);
    const unsigned lo = l * 16;
    auto ldh = [&](int s, int half) { int sr = min(s, 10) + rot; if (sr >= 11) sr -= 11; return *(const bf16x8*)(hsrc + (size_t)((sr * 8 + half) * 1024) + lo); };
    f32x16 acc[2]; acc[0] = f32x16{}; acc[1] = f32x16{};
    bf16x8 ha[2], hb[2], wx[2], wy[2];
    ha[0] = ldh(0, 0); ha[1] = ldh(0, 1); hb[0] = ldh(1, 0); hb[1] = ldh(1, 1);
    auto rdh = [&](int s, int half, bf16x8 (&wf)[2]) {
        lds_cptr base = (lds_cptr)lds + (s & 7) * FN_ST + half * 2048;
        wf[0] = trfrag(base + woff0, base + woff0 + 8 * 128); wf[1] = trfrag(base + woff1, base + woff1 + 8 * 128);
    };
    auto stage = [&](int s, bf16x8 (&hf)[2], bool last) {
        rdh(s, 1, wy);
        acc[0] = MFMA(wx[0], hf[0], acc[0]); acc[1] = MFMA(wx[1], hf[0], acc[1]); interleave<2, 2>(); SB();
        hf[0] = ldh(s + 2, 0); SB();
        if (!last) { if (w == 0) TL(4, 1, 2 * s); bar_only(); if (w == 0) TL(4, 1, 2 * s + 1); rdh(s + 1, 0, wx); }
        acc[0] = MFMA(wy[0], hf[1], acc[0]); acc[1] = MFMA(wy[1], hf[1], acc[1]); interleave<2, 2>(); SB();
        hf[1] = ldh(s + 2, 1); SB();
    };
    if (w == 0) TL(4, 1, 100);
    bar_only();
    if (w == 0) TL(4, 1, 101);
    rdh(0, 0, wx);
#pragma unroll 1
    for (int s = 0; s < 10; s += 2) { stage(s, ha, false); stage(s + 1, hb, false); }
    stage(10, ha, true);
    if (w == 0) TL(4, 1, 102);
    bar_only();
    LDS_AS f32x4* red = (LDS_AS f32x4*)lds;
#pragma unroll
    for (int db = 0; db < 2; ++db)
#pragma unroll
        for (int j = 0; j < 4; ++j) red[((w * 2 + db) * 4 + j) * 64 + l] = f32x4{acc[db][4 * j], acc[db][4 * j + 1], acc[db][4 * j + 2], acc[db][4 * j + 3]};
    bar_only();
    const int db = kq & 1, g = kq >> 1;
    f32x4 v0 = {0.f, 0.f, 0.f, 0.f}, v1 = v0;
#pragma unroll
    for (int q = 0; q < 4; ++q) { const int ww = q * 2 + tb; v0 += red[((ww * 2 + db) * 4 + 2 * g) * 64 + l]; v1 += red[((ww * 2 + db) * 4 + 2 * g + 1) * 64 + l]; }
    const unsigned a0 = cvtpk(v0[0], v0[1]), a1 = cvtpk(v0[2], v0[3]), b0 = cvtpk(v1[0], v1[1]), b1 = cvtpk(v1[2], v1[3]);
    auto r0 = __builtin_amdgcn_permlane32_swap(a0, b0, false, false);
    auto r1 = __builtin_amdgcn_permlane32_swap(a1, b1, false, false);
    u32x4 ov = {r0[0], r1[0], r0[1], r1[1]};
    *(u32x4*)(out + (size_t)(tt * 64 + tb * 32 + r32) * D_ + dt * 64 + db * 32 + g * 16 + h * 8) = ov;
    if (w == 0) TL(4, 1, 103);
    STAMP(4, 2);
}

extern "C" void kernel_launch(void* const* d_in, const int* in_sizes, int n_in, void* d_out, int out_size, void* d_ws, size_t ws_size, hipStream_t stream) {
    const u16* x = (const u16*)d_in[0];
    const u16* wqkv = (const u16*)d_in[1];
    const u16* wo = (const u16*)d_in[2];
    const u16* w_in = (const u16*)d_in[4];
    const u16* w_sp = (const u16*)d_in[5];
    const u16* b_sp = (const u16*)d_in[6];
    const u16* w_out = (const u16*)d_in[7];
    const u16* gate_w = (const u16*)d_in[8];
    char* ws = (char*)d_ws;
    k1_proj<<<256, 512, 0, stream>>>(x, wqkv, w_in, gate_w, ws);
    k_ln<<<256, 256, 0, stream>>>(ws);
    k_mid<<<512, 512, 0, stream>>>(x, w_in, w_sp, b_sp, ws);
    k_final<<<256, 768, 0, stream>>>(wo, w_out, ws, (u16*)d_out);
}
```

```cpp
#include <hip/hip_runtime.h>
#include <hip/hip_bf16.h>
#include <stdint.h>

typedef __attribute__((ext_vector_type(8))) short bf16x8;
typedef __attribute__((ext_vector_type(4))) short s16x4;
typedef __attribute__((ext_vector_type(16))) float f32x16;
typedef __attribute__((ext_vector_type(4))) unsigned u32x4;
typedef __attribute__((ext_vector_type(2))) float f32x2;
typedef __attribute__((ext_vector_type(4))) float f32x4;
typedef __attribute__((ext_vector_type(2))) __bf16 bf16x2_t;
#define LDS_AS __attribute__((address_space(3)))
typedef LDS_AS const char* lds_cptr;
typedef LDS_AS char* lds_ptr;
typedef short v4i16_t __attribute__((ext_vector_type(4)));
typedef unsigned short u16;
#define DI __device__ __forceinline__

#define MFMA(a, b, c) __builtin_amdgcn_mfma_f32_32x32x16_bf16((a), (b), (c), 0, 0, 0)
#define SB() __builtin_amdgcn_sched_barrier(0)
template <int NM, int ND> DI void interleave() {
#pragma unroll
    for (int i = 0; i < NM; ++i) { __builtin_amdgcn_sched_group_barrier(0x008, 1, 0); __builtin_amdgcn_sched_group_barrier(0x100, ND, 0); }
}
#define TL(kid, who, idx) do {} while (0)
#define WAIT_BAR(N) asm volatile("s_waitcnt vmcnt(" #N ") lgkmcnt(0)\n\ts_barrier" ::: "memory")

#define K1_ROT(mt, p) ((mt) & 7)
#define SP_ROT(tl, fl) ((tl) * 4 + (fl) * 2)
#define PACE 1
#define PACE_() do { if (PACE == 1) __builtin_amdgcn_s_sleep(1); else if (PACE == 2) asm volatile("s_nop 7"); else if (PACE == 3) asm volatile("s_nop 11"); else if (PACE == 4) asm volatile("s_nop 15"); SB(); } while (0)
#define LPRIO 0
#define GPRIO 2
#define FN_ROT(i, dt) ((((i) & 3) * 3 + (dt)) % 11)
constexpr int S_ = 2048, D_ = 512, FH = 896;
constexpr float QSCALE = 0.25f * 1.4426950408889634f;

constexpr size_t WS_QF    = 0;
constexpr size_t WS_KV    = WS_QF + 32u * 64 * 1024;
constexpr size_t WS_Z2    = WS_KV + 32u * 64 * 2048;
constexpr size_t WS_Z2N   = WS_Z2 + 64u * 56 * 1024;
constexpr size_t WS_HO    = WS_Z2N + 28u * 128 * 1024;
constexpr size_t WS_STATS = WS_HO + 64u * 88 * 1024;
constexpr size_t WS_KMAX  = WS_STATS + 2048u * 28 * 8;
constexpr size_t WS_GATE  = WS_KMAX + 64u * 32 * 4;
constexpr size_t WS_SYNC  = WS_GATE + 2048u * 4;
constexpr int SY_LN = 0, SY_H = 16, SY_O = 48, SY_TMO = 56, SY_N = 57;
constexpr size_t WS_END   = WS_SYNC + SY_N * 256;

#define STAMP(kid, slot) do {} while (0)
DI unsigned cvtpk(float a, float b) { f32x2 f = {a, b}; bf16x2_t r = __builtin_convertvector(f, bf16x2_t); return __builtin_bit_cast(unsigned, r); }
DI float bf_lo(unsigned u) { return __uint_as_float(u << 16); }
DI float bf_hi(unsigned u) { return __uint_as_float(u & 0xffff0000u); }
DI void glds16(const void* g, unsigned lds_base) {
    unsigned sv;
    asm volatile("s_mov_b32 %0, m0\n\ts_mov_b32 m0, %2\n\ts_nop 0\n\tglobal_load_lds_dwordx4 %1, off\n\ts_mov_b32 m0, %0"
                 : "=&s"(sv) : "v"(g), "s"(lds_base) : "memory");
}
DI void glds16s(const void* ubase, unsigned voff, unsigned lds_base) {
    asm volatile("s_mov_b32 m0, %2\n\ts_nop 0\n\tglobal_load_lds_dwordx4 %0, %1" :: "v"(voff), "s"(ubase), "s"(lds_base) : "memory");
}
#define GAS __attribute__((address_space(1)))
typedef GAS unsigned gu32;
#define RLX_AGENT __ATOMIC_RELAXED, __HIP_MEMORY_SCOPE_AGENT
DI gu32* sync_word(char* ws, int i) { return (gu32*)(ws + WS_SYNC + (size_t)i * 256); }
DI void st16_wt(char* ws, size_t off, u32x4 v) {
    __builtin_amdgcn_raw_buffer_store_b128(v, __builtin_amdgcn_make_buffer_rsrc(ws, 0, (int)WS_END, 0x00020000), (int)off, 0, 16);
}
DI bf16x8 ld16_dev(char* ws, size_t off) {
    return __builtin_bit_cast(bf16x8, __builtin_amdgcn_raw_buffer_load_b128(__builtin_amdgcn_make_buffer_rsrc(ws, 0, (int)WS_END, 0x00020000), (int)off, 0, 16));
}
DI void prefetch_line(const void* ubase, unsigned voff, unsigned lds_dump) {
    asm volatile("s_mov_b32 m0, %2\n\ts_nop 0\n\tglobal_load_lds_dword %0, %1" :: "v"(voff), "s"(ubase), "s"(lds_dump) : "memory");
}
DI void drain_stores() { asm volatile("s_waitcnt vmcnt(0)" ::: "memory"); }
DI void wait_words(char* ws, int first, int n, unsigned want, int lane) {
    gu32* p = sync_word(ws, first + lane % n);
    for (unsigned spins = 0;; ++spins) {
        if (__all(__hip_atomic_load(p, RLX_AGENT) >= want)) break;
        if (spins > (1u << 22)) { if (lane == 0) __hip_atomic_store(sync_word(ws, SY_TMO), 1u, RLX_AGENT); break; }
        __builtin_amdgcn_s_sleep(2);
    }
    asm volatile("" ::: "memory");
}
DI void bar_only() { asm volatile("" ::: "memory"); __builtin_amdgcn_s_waitcnt(0xC07F); __builtin_amdgcn_s_barrier(); asm volatile("" ::: "memory"); }
template <int N> DI void wait_bar() { asm volatile("s_waitcnt vmcnt(%0)" :: "i"(N) : "memory"); bar_only(); }
DI s16x4 vtr(lds_cptr p) { return __builtin_bit_cast(s16x4, __builtin_amdgcn_ds_read_tr16_b64_v4i16((LDS_AS v4i16_t*)p)); }
DI bf16x8 trfrag(lds_cptr p0, lds_cptr p1) { s16x4 lo = vtr(p0), hi = vtr(p1); return __builtin_shufflevector(lo, hi, 0, 1, 2, 3, 4, 5, 6, 7); }
DI bf16x8 ldsfrag(lds_cptr p) { return *(const LDS_AS bf16x8*)p; }
DI u32x4 pack8(const f32x16& a, int o, float sc) {
    u32x4 r; r[0] = cvtpk(a[o] * sc, a[o + 1] * sc); r[1] = cvtpk(a[o + 2] * sc, a[o + 3] * sc);
    r[2] = cvtpk(a[o + 4] * sc, a[o + 5] * sc); r[3] = cvtpk(a[o + 6] * sc, a[o + 7] * sc); return r;
}
DI float swap32_sum(float v) { auto r = __builtin_amdgcn_permlane32_swap(__float_as_uint(v), __float_as_uint(v), false, false); return __uint_as_float(r[0]) + __uint_as_float(r[1]); }
DI float swap32_max(float v) { auto r = __builtin_amdgcn_permlane32_swap(__float_as_uint(v), __float_as_uint(v), false, false); return fmaxf(__uint_as_float(r[0]), __uint_as_float(r[1])); }
DI float dpp_max16(float v) {
    v = fmaxf(v, __uint_as_float(__builtin_amdgcn_update_dpp(0u, __float_as_uint(v), 0x128, 0xf, 0xf, false)));
    v = fmaxf(v, __uint_as_float(__builtin_amdgcn_update_dpp(0u, __float_as_uint(v), 0x124, 0xf, 0xf, false)));
    v = fmaxf(v, __uint_as_float(__builtin_amdgcn_update_dpp(0u, __float_as_uint(v), 0x122, 0xf, 0xf, false)));
    v = fmaxf(v, __uint_as_float(__builtin_amdgcn_update_dpp(0u, __float_as_uint(v), 0x121, 0xf, 0xf, false)));
    return v;
}
DI float wave_max_fast(float v) {
    v = dpp_max16(v);
    auto r = __builtin_amdgcn_permlane16_swap(__float_as_uint(v), __float_as_uint(v), false, false);
    v = fmaxf(__uint_as_float(r[0]), __uint_as_float(r[1]));
    return swap32_max(v);
}

DI float gelu_tanh(float x) {
    const float c1 = -2.0f * 0.7978845608028654f * 1.4426950408889634f, c2 = c1 * 0.044715f;
    const float a = x * __builtin_fmaf(x * x, c2, c1);
    return x * __builtin_amdgcn_rcpf(1.0f + __builtin_amdgcn_exp2f(a));
}

constexpr int K1_ST = 40960, K1_LDS = 3 * K1_ST + 1024;

template <int TYPE>
DI void k1_body(const u16* __restrict__ x, const u16* __restrict__ wqkv, const u16* __restrict__ w_in, const u16* __restrict__ gate_w,
                char* __restrict__ ws, char* lds, int mt, int p, int tid) {
    if (tid == 0) TL(0, 1, 99);
    constexpr int NG = TYPE == 0 ? 3 : (TYPE == 1 ? 2 : 1);
    constexpr int NA = TYPE == 2 ? 1 : (TYPE == 3 ? 2 : NG);
    constexpr int P = 4 + 2 * NG;
    const int l = tid & 63, w = __builtin_amdgcn_readfirstlane(tid >> 6);
    const int rot = K1_ROT(mt, p);
    if (w >= 4) {
        const int j = w - 4;
        const unsigned lds0 = (unsigned)(uintptr_t)lds;
        const int row = 8 * j + (l >> 3);
        const unsigned xv = (unsigned)(row * D_ + ((l & 7) ^ ((row >> 1) & 7)) * 8) * 2;
        const int wch = ((l & 7) ^ (((row >> 1) & 1) << 2)) * 8;
        const unsigned qv = (unsigned)(row * 1536 + wch) * 2, zv = (unsigned)(row * 1792 + wch) * 2;
        const char* xu = (const char*)(x + (size_t)mt * 128 * D_);
        const char* g0 = (const char*)(wqkv + p * 64 + (TYPE == 0 ? 0 : 1024));
        const char* g1 = TYPE == 0 ? (const char*)(wqkv + p * 64 + 512) : (const char*)(w_in + FH + (8 + p) * 64);
        const char* g2 = (const char*)(w_in + FH + p * 64);
        auto issue = [&](int s) {
            const unsigned dst = lds0 + (s % 3) * K1_ST + j * 1024;
            const int sr = (s + rot) & 7;
#pragma unroll
            for (int i = 0; i < 4; ++i) glds16s(xu + (size_t)i * 32 * D_ * 2 + sr * 128, xv, dst + i * 4096);
            glds16s(g0 + (size_t)sr * 64 * 3072, qv, dst + 16384); glds16s(g0 + (size_t)(sr * 64 + 32) * 3072, qv, dst + 16384 + 4096);
            if (NG >= 2) { constexpr int ld = TYPE == 0 ? 3072 : 3584; const unsigned gv = TYPE == 0 ? qv : zv;
                glds16s(g1 + (size_t)sr * 64 * ld, gv, dst + 16384 + 8192); glds16s(g1 + (size_t)(sr * 64 + 32) * ld, gv, dst + 16384 + 8192 + 4096); }
            if (NG >= 3) { glds16s(g2 + (size_t)sr * 64 * 3584, zv, dst + 32768); glds16s(g2 + (size_t)(sr * 64 + 32) * 3584, zv, dst + 32768 + 4096); }
        };
        if (TYPE == 3 && j == 0) glds16s(gate_w, l * 16, lds0 + 3 * K1_ST);
        issue(0); issue(1); issue(2);
        wait_bar<2 * P>();
#pragma unroll 1
        for (int s = 0; s < 7; ++s) {
            if (j == 0) TL(0, 0, 3 * s);
            if (s + 2 < 8) asm volatile("s_waitcnt vmcnt(%0)" :: "i"(P) : "memory"); else asm volatile("s_waitcnt vmcnt(0)" ::: "memory");
            if (j == 0) TL(0, 0, 3 * s + 1);
            bar_only();
            if (j == 0) TL(0, 0, 3 * s + 2);
            if (s + 3 < 8) issue(s + 3);
        }
        return;
    }
    const int rh = w & 1, ch = w >> 1, h = l >> 5, r32 = l & 31;
    int xo[4];
    { const int row = rh * 64 + r32, f = (row >> 1) & 7;
#pragma unroll
      for (int c = 0; c < 4; ++c) xo[c] = row * 128 + (((2 * c + h) ^ f) << 4); }
    const int q4 = (l & 15) >> 2, p4 = l & 3, b16 = (l >> 4) & 1;
    const int wl = 16384 + (8 * h + q4) * 128 + (((ch * 4 + b16 * 2 + (p4 >> 1)) ^ ((q4 >> 1) << 2)) << 4) + (p4 & 1) * 8;
    f32x16 acc[NA][2];
#pragma unroll
    for (int g = 0; g < NA; ++g) { acc[g][0] = f32x16{}; acc[g][1] = f32x16{}; }
    bf16x8 xa[2][2], wa[NG][2], xb[2][2], wb[NG][2], ga[2], gb[2];
    auto rdh = [&](int s, int half, bf16x8 (&xf)[2][2], bf16x8 (&wf)[NG][2], bf16x8 (&gf)[2]) {
        lds_cptr base = (lds_cptr)lds + (s % 3) * K1_ST;
#pragma unroll
        for (int kk = 0; kk < 2; ++kk) {
            const int ks = 2 * half + kk;
            xf[0][kk] = ldsfrag(base + xo[ks]); xf[1][kk] = ldsfrag(base + xo[ks] + 4096);
#pragma unroll
            for (int g = 0; g < NG; ++g) wf[g][kk] = trfrag(base + wl + g * 8192 + ks * 2048, base + wl + g * 8192 + ks * 2048 + 512);
            if (TYPE == 3) gf[kk] = ldsfrag((lds_cptr)lds + 3 * K1_ST + (((s + rot) & 7) * 4 + ks) * 32 + h * 16);
        }
    };
    auto mm = [&](bf16x8 (&xf)[2][2], bf16x8 (&wf)[NG][2], bf16x8 (&gf)[2]) {
#pragma unroll
        for (int kk = 0; kk < 2; ++kk)
#pragma unroll
            for (int rb = 0; rb < 2; ++rb) {
                if (TYPE == 0) {
#pragma unroll
                    for (int g = 0; g < 3; ++g) acc[g % NA][rb] = MFMA(wf[g % NG][kk], xf[rb][kk], acc[g % NA][rb]);
                } else {
                    acc[0][rb] = MFMA(xf[rb][kk], wf[0][kk], acc[0][rb]);
                    if (TYPE == 1) acc[NA - 1][rb] = MFMA(wf[NG - 1][kk], xf[rb][kk], acc[NA - 1][rb]);
                    if (TYPE == 3) acc[NA - 1][rb] = MFMA(gf[kk], xf[rb][kk], acc[NA - 1][rb]);
                }
            }
    };
    if (w == 0) TL(0, 1, 100);
    bar_only();
    if (w == 0) TL(0, 1, 101);
    rdh(0, 0, xa, wa, ga);
#pragma unroll 1
    for (int s = 0; s < 7; ++s) {
        rdh(s, 1, xb, wb, gb); mm(xa, wa, ga); interleave<4 * NA, 2>(); SB();
        if (w == 0) TL(0, 1, 2 * s);
        bar_only();
        if (w == 0) TL(0, 1, 2 * s + 1);
        rdh(s + 1, 0, xa, wa, ga); mm(xb, wb, gb); interleave<4 * NA, 2>(); SB();
    }
    rdh(7, 1, xb, wb, gb); mm(xa, wa, ga); interleave<4 * NA, 2>(); SB();
    mm(xb, wb, gb);
    STAMP(0, 1);
    if (w == 0) TL(0, 1, 102);
    char* kv = ws + WS_KV;
#pragma unroll
    for (int rb = 0; rb < 2; ++rb) {
        const int ttile = mt * 4 + rh * 2 + rb;
        if (TYPE == 0) {
            const int hd0 = 4 * p + 2 * ch;
            char* qf = ws + WS_QF;
            const f32x16 &q = acc[0][rb], &k = acc[1 % NA][rb];
            *(u32x4*)(qf + ((size_t)(hd0 * 64 + ttile) * 1024) + l * 16) = pack8(q, 0, QSCALE);
            *(u32x4*)(qf + ((size_t)((hd0 + 1) * 64 + ttile) * 1024) + l * 16) = pack8(q, 8, QSCALE);
            *(u32x4*)(kv + ((size_t)(hd0 * 64 + ttile) * 2048) + l * 16) = pack8(k, 0, 1.0f);
            *(u32x4*)(kv + ((size_t)((hd0 + 1) * 64 + ttile) * 2048) + l * 16) = pack8(k, 8, 1.0f);
            float n0 = 0.f, n1 = 0.f;
#pragma unroll
            for (int r = 0; r < 8; ++r) { n0 = __builtin_fmaf(k[r], k[r], n0); n1 = __builtin_fmaf(k[8 + r], k[8 + r], n1); }
            n0 = wave_max_fast(swap32_sum(n0)); n1 = wave_max_fast(swap32_sum(n1));
            if (l == 0) { float* km = (float*)(ws + WS_KMAX) + ttile * 32 + hd0; km[0] = n0 * 1.01f; km[1] = n1 * 1.01f; }
        } else {
            const int hd = 4 * p + 2 * ch + (r32 >> 4), d = r32 & 15;
            char* vp = kv + ((size_t)(hd * 64 + ttile) * 2048) + 1024 + h * 256 + d * 16;
            *(u32x4*)(vp) = pack8(acc[0][rb], 0, 1.0f); *(u32x4*)(vp + 512) = pack8(acc[0][rb], 8, 1.0f);
            if (TYPE == 3 && ch == 0 && h == 0)
                ((float*)(ws + WS_GATE))[ttile * 32 + r32] = __builtin_amdgcn_rcpf(1.0f + __builtin_amdgcn_exp2f(-1.4426950408889634f * acc[NA - 1][rb][0]));
        }
        if (TYPE <= 1) {
            f32x16& z = acc[NA - 1][rb];
            const int zg = TYPE == 0 ? p : 8 + p;
            float s1 = 0.f, s2 = 0.f;
#pragma unroll
            for (int r = 0; r < 16; ++r) { const float v = gelu_tanh(z[r]); z[r] = v; s1 += v; s2 = __builtin_fmaf(v, v, s2); }
            const int fb = 2 * zg + ch;
            char* zp = ws + WS_Z2 + ((size_t)(ttile * 56 + 2 * fb) * 1024);
            *(u32x4*)(zp + l * 16) = pack8(z, 0, 1.0f); *(u32x4*)(zp + 1024 + l * 16) = pack8(z, 8, 1.0f);
            s1 = swap32_sum(s1); s2 = swap32_sum(s2);
            if (h == 0) ((f32x2*)(ws + WS_STATS))[(size_t)(ttile * 32 + r32) * 28 + fb] = f32x2{s1, s2};
        }
    }
    if (w == 0) TL(0, 1, 103);
}

__global__ __launch_bounds__(512) void k1_proj(const u16* __restrict__ x, const u16* __restrict__ wqkv, const u16* __restrict__ w_in,
                                               const u16* __restrict__ gate_w, char* __restrict__ ws) {
    __shared__ __attribute__((aligned(1024))) char lds[K1_LDS];
    const int b = blockIdx.x, xcd = b & 7, idx = b >> 3;
    const int mt = (xcd & 1) * 8 + (idx & 7), nt = (xcd >> 1) * 4 + (idx >> 3), p = nt >> 1;
    STAMP(0, 0);
    if (b == 255 && threadIdx.x < SY_N) *(unsigned*)(ws + WS_SYNC + threadIdx.x * 256) = 0u;
    if (!(nt & 1)) k1_body<0>(x, wqkv, w_in, gate_w, ws, lds, mt, p, threadIdx.x);
    else if (p < 6) k1_body<1>(x, wqkv, w_in, gate_w, ws, lds, mt, p, threadIdx.x);
    else if (p == 6) k1_body<2>(x, wqkv, w_in, gate_w, ws, lds, mt, p, threadIdx.x);
    else k1_body<3>(x, wqkv, w_in, gate_w, ws, lds, mt, p, threadIdx.x);
    STAMP(0, 2);
}

DI void ln_apply(char* __restrict__ ws, char* lds, int unit, int tid) {
    const int l = tid & 63, w = tid >> 6, h = l >> 5, r32 = l & 31;
    if (w >= 7) return;
    const int ttile = unit >> 2, fb = (unit & 3) * 7 + w;
    const u32x4 v0 = *(const u32x4*)(ws + WS_Z2 + ((size_t)(ttile * 56 + 2 * fb) * 1024) + l * 16);
    const u32x4 v1 = *(const u32x4*)(ws + WS_Z2 + ((size_t)(ttile * 56 + 2 * fb + 1) * 1024) + l * 16);
    const f32x4* st = (const f32x4*)(ws + WS_STATS) + (size_t)(ttile * 32 + r32) * 14 + h * 7;
    float s1 = 0.f, s2 = 0.f;
#pragma unroll
    for (int i = 0; i < 7; ++i) { const f32x4 v = st[i]; s1 += v[0] + v[2]; s2 += v[1] + v[3]; }
    s1 = swap32_sum(s1); s2 = swap32_sum(s2);
    const float mean = s1 * (1.0f / FH);
    const float var = fmaxf(s2 * (1.0f / FH) - mean * mean, 0.f);
    const float rstd = __builtin_amdgcn_rsqf(var + 1e-5f), nm = -mean * rstd;
    lds_ptr tile = (lds_ptr)lds + 49152 + w * 2048;
    const int q4 = (l & 15) >> 2, p4 = l & 3, b16 = (l >> 4) & 1;
#pragma unroll
    for (int g = 0; g < 2; ++g) {
        const u32x4 v = g ? v1 : v0;
        unsigned o[4];
#pragma unroll
        for (int e = 0; e < 4; ++e) o[e] = cvtpk(__builtin_fmaf(bf_lo(v[e]), rstd, nm), __builtin_fmaf(bf_hi(v[e]), rstd, nm));
        auto r0 = __builtin_amdgcn_permlane32_swap(o[0], o[2], false, false);
        auto r1 = __builtin_amdgcn_permlane32_swap(o[1], o[3], false, false);
        const u32x4 ov = {r0[0], r1[0], r0[1], r1[1]};
        *(LDS_AS u32x4*)(tile + r32 * 64 + g * 32 + h * 16) = ov;
    }
    asm volatile("" ::: "memory");
#pragma unroll
    for (int kk = 0; kk < 2; ++kk) {
        lds_cptr rp = (lds_cptr)tile + (16 * kk + 8 * h + q4) * 64 + b16 * 32 + p4 * 8;
        st16_wt(ws, WS_Z2N + ((size_t)(fb * 128 + ttile * 2 + kk) * 1024) + l * 16, __builtin_bit_cast(u32x4, trfrag(rp, rp + 256)));
    }
}

constexpr int AT_TS = 2304, AT_SLOT = 8 * AT_TS, AT_LDS = 4 * AT_SLOT + 64;
#define ATTN_ASM \
    "s_mov_b32 %[sv], m0\n\t" \
    "v_mov_b32 v88, 0\n\t" \
    "v_mov_b32 v89, 0\n\t" \
    "v_mov_b32 v90, 0\n\t" \
    "v_mov_b32 v91, 0\n\t" \
    "v_mov_b32 v96, 0\n\t" \
    "v_mov_b32 v97, 0\n\t" \
    "v_mov_b32 v98, 0\n\t" \
    "v_mov_b32 v99, 0\n\t" \
    "v_mov_b32 v0, 0\n\t" \
    "v_mov_b32 v1, 0\n\t" \
    "v_mov_b32 v2, 0\n\t" \
    "v_mov_b32 v3, 0\n\t" \
    "v_mov_b32 v4, 0\n\t" \
    "v_mov_b32 v5, 0\n\t" \
    "v_mov_b32 v6, 0\n\t" \
    "v_mov_b32 v7, 0\n\t" \
    "v_mov_b32 v8, 0\n\t" \
    "v_mov_b32 v9, 0\n\t" \
    "v_mov_b32 v10, 0\n\t" \
    "v_mov_b32 v11, 0\n\t" \
    "v_mov_b32 v12, 0\n\t" \
    "v_mov_b32 v13, 0\n\t" \
    "v_mov_b32 v14, 0\n\t" \
    "v_mov_b32 v15, 0\n\t" \
    "s_mov_b32 %[cnt], 0\n\t" \
    "s_waitcnt vmcnt(4) lgkmcnt(0)\n\t" \
    "s_barrier\n\t" \
    "ds_read_b128 v[68:71], %[kl] offset:0\n\t" \
    "ds_read_b128 v[72:75], %[kl] offset:2304\n\t" \
    "ds_read_b128 v[76:79], %[v0l] offset:0\n\t" \
    "ds_read_b128 v[80:83], %[v1l] offset:0\n\t" \
    "s_waitcnt lgkmcnt(0)\n\t" \
    "v_mfma_f32_32x32x16_bf16 v[32:47], v[68:71], v[64:67], v[16:31]\n\t" \
    "s_nop 7\n\t" \
    "s_nop 3\n\t" \
    "Lloop%=:\n\t" \
    "s_setprio 3\n\t" \
    "v_mfma_f32_32x32x16_bf16 v[48:63], v[72:75], v[64:67], v[16:31]\n\t" \
    "v_exp_f32 v32, v32\n\t" \
    "ds_read_b128 v[68:71], %[kl] offset:4608\n\t" \
    "v_exp_f32 v33, v33\n\t" \
    "v_exp_f32 v34, v34\n\t" \
    "v_exp_f32 v35, v35\n\t" \
    "v_exp_f32 v36, v36\n\t" \
    "v_exp_f32 v37, v37\n\t" \
    "v_exp_f32 v38, v38\n\t" \
    "v_mfma_f32_32x32x16_bf16 v[0:15], v[88:91], v[96:99], v[0:15]\n\t" \
    "v_exp_f32 v39, v39\n\t" \
    "ds_read_b128 v[84:87], %[v0l] offset:2304\n\t" \
    "v_cvt_pk_bf16_f32 v92, v32, v33\n\t" \
    "v_cvt_pk_bf16_f32 v93, v34, v35\n\t" \
    "v_cvt_pk_bf16_f32 v94, v36, v37\n\t" \
    "v_cvt_pk_bf16_f32 v95, v38, v39\n\t" \
    "v_exp_f32 v40, v40\n\t" \
    "ds_read_b128 v[88:91], %[v1l] offset:2304\n\t" \
    "v_exp_f32 v41, v41\n\t" \
    "v_exp_f32 v42, v42\n\t" \
    "v_exp_f32 v43, v43\n\t" \
    "s_waitcnt lgkmcnt(4)\n\t" \
    "v_mfma_f32_32x32x16_bf16 v[0:15], v[76:79], v[92:95], v[0:15]\n\t" \
    "v_exp_f32 v44, v44\n\t" \
    "v_exp_f32 v45, v45\n\t" \
    "v_exp_f32 v46, v46\n\t" \
    "v_exp_f32 v47, v47\n\t" \
    "s_waitcnt lgkmcnt(2)\n\t" \
    "v_cvt_pk_bf16_f32 v96, v40, v41\n\t" \
    "v_cvt_pk_bf16_f32 v97, v42, v43\n\t" \
    "v_cvt_pk_bf16_f32 v98, v44, v45\n\t" \
    "v_cvt_pk_bf16_f32 v99, v46, v47\n\t" \
    "s_setprio 2\n\t" \
    "v_mfma_f32_32x32x16_bf16 v[32:47], v[68:71], v[64:67], v[16:31]\n\t" \
    "v_exp_f32 v48, v48\n\t" \
    "ds_read_b128 v[72:75], %[kl] offset:6912\n\t" \
    "v_exp_f32 v49, v49\n\t" \
    "v_exp_f32 v50, v50\n\t" \
    "v_exp_f32 v51, v51\n\t" \
    "v_exp_f32 v52, v52\n\t" \
    "v_exp_f32 v53, v53\n\t" \
    "v_exp_f32 v54, v54\n\t" \
    "v_mfma_f32_32x32x16_bf16 v[0:15], v[80:83], v[96:99], v[0:15]\n\t" \
    "v_exp_f32 v55, v55\n\t" \
    "ds_read_b128 v[76:79], %[v0l] offset:4608\n\t" \
    "v_cvt_pk_bf16_f32 v92, v48, v49\n\t" \
    "v_cvt_pk_bf16_f32 v93, v50, v51\n\t" \
    "v_cvt_pk_bf16_f32 v94, v52, v53\n\t" \
    "v_cvt_pk_bf16_f32 v95, v54, v55\n\t" \
    "v_exp_f32 v56, v56\n\t" \
    "ds_read_b128 v[80:83], %[v1l] offset:4608\n\t" \
    "v_exp_f32 v57, v57\n\t" \
    "v_exp_f32 v58, v58\n\t" \
    "v_exp_f32 v59, v59\n\t" \
    "s_waitcnt lgkmcnt(4)\n\t" \
    "v_mfma_f32_32x32x16_bf16 v[0:15], v[84:87], v[92:95], v[0:15]\n\t" \
    "v_exp_f32 v60, v60\n\t" \
    "v_exp_f32 v61, v61\n\t" \
    "v_exp_f32 v62, v62\n\t" \
    "v_exp_f32 v63, v63\n\t" \
    "s_waitcnt lgkmcnt(2)\n\t" \
    "v_cvt_pk_bf16_f32 v96, v56, v57\n\t" \
    "v_cvt_pk_bf16_f32 v97, v58, v59\n\t" \
    "v_cvt_pk_bf16_f32 v98, v60, v61\n\t" \
    "v_cvt_pk_bf16_f32 v99, v62, v63\n\t" \
    "s_setprio 1\n\t" \
    "v_mfma_f32_32x32x16_bf16 v[48:63], v[72:75], v[64:67], v[16:31]\n\t" \
    "v_exp_f32 v32, v32\n\t" \
    "ds_read_b128 v[68:71], %[kl] offset:9216\n\t" \
    "v_exp_f32 v33, v33\n\t" \
    "v_exp_f32 v34, v34\n\t" \
    "v_exp_f32 v35, v35\n\t" \
    "v_exp_f32 v36, v36\n\t" \
    "v_exp_f32 v37, v37\n\t" \
    "v_exp_f32 v38, v38\n\t" \
    "v_mfma_f32_32x32x16_bf16 v[0:15], v[88:91], v[96:99], v[0:15]\n\t" \
    "v_exp_f32 v39, v39\n\t" \
    "ds_read_b128 v[84:87], %[v0l] offset:6912\n\t" \
    "v_cvt_pk_bf16_f32 v92, v32, v33\n\t" \
    "v_cvt_pk_bf16_f32 v93, v34, v35\n\t" \
    "v_cvt_pk_bf16_f32 v94, v36, v37\n\t" \
    "v_cvt_pk_bf16_f32 v95, v38, v39\n\t" \
    "v_exp_f32 v40, v40\n\t" \
    "ds_read_b128 v[88:91], %[v1l] offset:6912\n\t" \
    "v_exp_f32 v41, v41\n\t" \
    "v_exp_f32 v42, v42\n\t" \
    "v_exp_f32 v43, v43\n\t" \
    "s_waitcnt lgkmcnt(4)\n\t" \
    "v_mfma_f32_32x32x16_bf16 v[0:15], v[76:79], v[92:95], v[0:15]\n\t" \
    "v_exp_f32 v44, v44\n\t" \
    "v_exp_f32 v45, v45\n\t" \
    "v_exp_f32 v46, v46\n\t" \
    "v_exp_f32 v47, v47\n\t" \
    "s_waitcnt lgkmcnt(2)\n\t" \
    "v_cvt_pk_bf16_f32 v96, v40, v41\n\t" \
    "v_cvt_pk_bf16_f32 v97, v42, v43\n\t" \
    "v_cvt_pk_bf16_f32 v98, v44, v45\n\t" \
    "v_cvt_pk_bf16_f32 v99, v46, v47\n\t" \
    "s_setprio 0\n\t" \
    "v_mfma_f32_32x32x16_bf16 v[32:47], v[68:71], v[64:67], v[16:31]\n\t" \
    "v_exp_f32 v48, v48\n\t" \
    "ds_read_b128 v[72:75], %[kl] offset:11520\n\t" \
    "v_exp_f32 v49, v49\n\t" \
    "v_exp_f32 v50, v50\n\t" \
    "v_exp_f32 v51, v51\n\t" \
    "v_exp_f32 v52, v52\n\t" \
    "v_exp_f32 v53, v53\n\t" \
    "v_exp_f32 v54, v54\n\t" \
    "v_mfma_f32_32x32x16_bf16 v[0:15], v[80:83], v[96:99], v[0:15]\n\t" \
    "v_exp_f32 v55, v55\n\t" \
    "ds_read_b128 v[76:79], %[v0l] offset:9216\n\t" \
    "v_cvt_pk_bf16_f32 v92, v48, v49\n\t" \
    "v_cvt_pk_bf16_f32 v93, v50, v51\n\t" \
    "v_cvt_pk_bf16_f32 v94, v52, v53\n\t" \
    "v_cvt_pk_bf16_f32 v95, v54, v55\n\t" \
    "v_exp_f32 v56, v56\n\t" \
    "ds_read_b128 v[80:83], %[v1l] offset:9216\n\t" \
    "v_exp_f32 v57, v57\n\t" \
    "v_exp_f32 v58, v58\n\t" \
    "v_exp_f32 v59, v59\n\t" \
    "s_waitcnt lgkmcnt(4)\n\t" \
    "v_mfma_f32_32x32x16_bf16 v[0:15], v[84:87], v[92:95], v[0:15]\n\t" \
    "v_exp_f32 v60, v60\n\t" \
    "v_exp_f32 v61, v61\n\t" \
    "v_exp_f32 v62, v62\n\t" \
    "v_exp_f32 v63, v63\n\t" \
    "s_waitcnt lgkmcnt(2)\n\t" \
    "v_cvt_pk_bf16_f32 v96, v56, v57\n\t" \
    "v_cvt_pk_bf16_f32 v97, v58, v59\n\t" \
    "v_cvt_pk_bf16_f32 v98, v60, v61\n\t" \
    "v_cvt_pk_bf16_f32 v99, v62, v63\n\t" \
    "s_setprio 3\n\t" \
    "v_mfma_f32_32x32x16_bf16 v[48:63], v[72:75], v[64:67], v[16:31]\n\t" \
    "v_exp_f32 v32, v32\n\t" \
    "ds_read_b128 v[68:71], %[kl] offset:13824\n\t" \
    "v_exp_f32 v33, v33\n\t" \
    "v_exp_f32 v34, v34\n\t" \
    "v_exp_f32 v35, v35\n\t" \
    "v_exp_f32 v36, v36\n\t" \
    "v_exp_f32 v37, v37\n\t" \
    "v_exp_f32 v38, v38\n\t" \
    "v_mfma_f32_32x32x16_bf16 v[0:15], v[88:91], v[96:99], v[0:15]\n\t" \
    "v_exp_f32 v39, v39\n\t" \
    "ds_read_b128 v[84:87], %[v0l] offset:11520\n\t" \
    "v_cvt_pk_bf16_f32 v92, v32, v33\n\t" \
    "v_cvt_pk_bf16_f32 v93, v34, v35\n\t" \
    "v_cvt_pk_bf16_f32 v94, v36, v37\n\t" \
    "v_cvt_pk_bf16_f32 v95, v38, v39\n\t" \
    "v_exp_f32 v40, v40\n\t" \
    "ds_read_b128 v[88:91], %[v1l] offset:11520\n\t" \
    "v_exp_f32 v41, v41\n\t" \
    "v_exp_f32 v42, v42\n\t" \
    "v_exp_f32 v43, v43\n\t" \
    "s_waitcnt lgkmcnt(4)\n\t" \
    "v_mfma_f32_32x32x16_bf16 v[0:15], v[76:79], v[92:95], v[0:15]\n\t" \
    "v_exp_f32 v44, v44\n\t" \
    "v_exp_f32 v45, v45\n\t" \
    "v_exp_f32 v46, v46\n\t" \
    "v_exp_f32 v47, v47\n\t" \
    "s_waitcnt lgkmcnt(2)\n\t" \
    "v_cvt_pk_bf16_f32 v96, v40, v41\n\t" \
    "v_cvt_pk_bf16_f32 v97, v42, v43\n\t" \
    "v_cvt_pk_bf16_f32 v98, v44, v45\n\t" \
    "v_cvt_pk_bf16_f32 v99, v46, v47\n\t" \
    "s_setprio 2\n\t" \
    "v_mfma_f32_32x32x16_bf16 v[32:47], v[68:71], v[64:67], v[16:31]\n\t" \
    "v_exp_f32 v48, v48\n\t" \
    "ds_read_b128 v[72:75], %[kl] offset:16128\n\t" \
    "s_and_b32 %[t0], %[cnt], 3\n\t" \
    "s_cmp_eq_u32 %[t0], 3\n\t" \
    "s_mov_b32 %[dl], 18432\n\t" \
    "s_cselect_b32 %[dl], -55296, %[dl]\n\t" \
    "v_add_u32 %[kl], %[dl], %[kl]\n\t" \
    "v_exp_f32 v49, v49\n\t" \
    "v_exp_f32 v50, v50\n\t" \
    "v_exp_f32 v51, v51\n\t" \
    "v_exp_f32 v52, v52\n\t" \
    "v_exp_f32 v53, v53\n\t" \
    "v_exp_f32 v54, v54\n\t" \
    "v_mfma_f32_32x32x16_bf16 v[0:15], v[80:83], v[96:99], v[0:15]\n\t" \
    "v_exp_f32 v55, v55\n\t" \
    "ds_read_b128 v[76:79], %[v0l] offset:13824\n\t" \
    "v_cvt_pk_bf16_f32 v92, v48, v49\n\t" \
    "v_cvt_pk_bf16_f32 v93, v50, v51\n\t" \
    "v_cvt_pk_bf16_f32 v94, v52, v53\n\t" \
    "v_cvt_pk_bf16_f32 v95, v54, v55\n\t" \
    "v_exp_f32 v56, v56\n\t" \
    "ds_read_b128 v[80:83], %[v1l] offset:13824\n\t" \
    "v_exp_f32 v57, v57\n\t" \
    "v_exp_f32 v58, v58\n\t" \
    "v_exp_f32 v59, v59\n\t" \
    "s_waitcnt lgkmcnt(4)\n\t" \
    "v_mfma_f32_32x32x16_bf16 v[0:15], v[84:87], v[92:95], v[0:15]\n\t" \
    "v_exp_f32 v60, v60\n\t" \
    "v_exp_f32 v61, v61\n\t" \
    "v_exp_f32 v62, v62\n\t" \
    "v_exp_f32 v63, v63\n\t" \
    "s_waitcnt lgkmcnt(2)\n\t" \
    "v_cvt_pk_bf16_f32 v96, v56, v57\n\t" \
    "v_cvt_pk_bf16_f32 v97, v58, v59\n\t" \
    "v_cvt_pk_bf16_f32 v98, v60, v61\n\t" \
    "v_cvt_pk_bf16_f32 v99, v62, v63\n\t" \
    "s_cmp_eq_u32 %[cnt], 7\n\t" \
    "s_cbranch_scc1 Lskip%=\n\t" \
    "s_cmp_eq_u32 %[cnt], 6\n\t" \
    "s_cbranch_scc1 Lw0%=\n\t" \
    "s_waitcnt vmcnt(2)\n\t" \
    "s_barrier\n\t" \
    "s_cmp_gt_u32 %[cnt], 4\n\t" \
    "s_cbranch_scc1 Lskip%=\n\t" \
    "s_add_u32 %[t0], %[cnt], 3\n\t" \
    "s_and_b32 %[t0], %[t0], 3\n\t" \
    "s_mul_i32 %[t0], %[t0], 18432\n\t" \
    "s_add_u32 m0, %[t0], %[dst]\n\t" \
    "s_setprio 3\n\t" \
    "global_load_lds_dwordx4 %[goff], %[base]\n\t" \
    "global_load_lds_dwordx4 %[goff], %[base] offset:1024\n\t" \
    "s_setprio 2\n\t" \
    "v_add_u32 %[goff], 0x4000, %[goff]\n\t" \
    "s_branch Lskip%=\n\t" \
    "Lw0%=:\n\t" \
    "s_waitcnt vmcnt(0)\n\t" \
    "s_barrier\n\t" \
    "Lskip%=:\n\t" \
    "s_setprio 1\n\t" \
    "v_mfma_f32_32x32x16_bf16 v[48:63], v[72:75], v[64:67], v[16:31]\n\t" \
    "v_exp_f32 v32, v32\n\t" \
    "ds_read_b128 v[68:71], %[kl] offset:0\n\t" \
    "v_exp_f32 v33, v33\n\t" \
    "v_exp_f32 v34, v34\n\t" \
    "v_exp_f32 v35, v35\n\t" \
    "v_exp_f32 v36, v36\n\t" \
    "v_exp_f32 v37, v37\n\t" \
    "v_exp_f32 v38, v38\n\t" \
    "v_mfma_f32_32x32x16_bf16 v[0:15], v[88:91], v[96:99], v[0:15]\n\t" \
    "v_exp_f32 v39, v39\n\t" \
    "ds_read_b128 v[84:87], %[v0l] offset:16128\n\t" \
    "v_cvt_pk_bf16_f32 v92, v32, v33\n\t" \
    "v_cvt_pk_bf16_f32 v93, v34, v35\n\t" \
    "v_cvt_pk_bf16_f32 v94, v36, v37\n\t" \
    "v_cvt_pk_bf16_f32 v95, v38, v39\n\t" \
    "v_exp_f32 v40, v40\n\t" \
    "ds_read_b128 v[88:91], %[v1l] offset:16128\n\t" \
    "v_add_u32 %[v0l], %[dl], %[v0l]\n\t" \
    "v_add_u32 %[v1l], %[dl], %[v1l]\n\t" \
    "v_exp_f32 v41, v41\n\t" \
    "v_exp_f32 v42, v42\n\t" \
    "v_exp_f32 v43, v43\n\t" \
    "s_waitcnt lgkmcnt(4)\n\t" \
    "v_mfma_f32_32x32x16_bf16 v[0:15], v[76:79], v[92:95], v[0:15]\n\t" \
    "v_exp_f32 v44, v44\n\t" \
    "v_exp_f32 v45, v45\n\t" \
    "v_exp_f32 v46, v46\n\t" \
    "v_exp_f32 v47, v47\n\t" \
    "s_waitcnt lgkmcnt(2)\n\t" \
    "v_cvt_pk_bf16_f32 v96, v40, v41\n\t" \
    "v_cvt_pk_bf16_f32 v97, v42, v43\n\t" \
    "v_cvt_pk_bf16_f32 v98, v44, v45\n\t" \
    "v_cvt_pk_bf16_f32 v99, v46, v47\n\t" \
    "s_setprio 0\n\t" \
    "v_mfma_f32_32x32x16_bf16 v[32:47], v[68:71], v[64:67], v[16:31]\n\t" \
    "v_exp_f32 v48, v48\n\t" \
    "ds_read_b128 v[72:75], %[kl] offset:2304\n\t" \
    "v_exp_f32 v49, v49\n\t" \
    "v_exp_f32 v50, v50\n\t" \
    "v_exp_f32 v51, v51\n\t" \
    "v_exp_f32 v52, v52\n\t" \
    "v_exp_f32 v53, v53\n\t" \
    "v_exp_f32 v54, v54\n\t" \
    "v_mfma_f32_32x32x16_bf16 v[0:15], v[80:83], v[96:99], v[0:15]\n\t" \
    "v_exp_f32 v55, v55\n\t" \
    "ds_read_b128 v[76:79], %[v0l] offset:0\n\t" \
    "v_cvt_pk_bf16_f32 v92, v48, v49\n\t" \
    "v_cvt_pk_bf16_f32 v93, v50, v51\n\t" \
    "v_cvt_pk_bf16_f32 v94, v52, v53\n\t" \
    "v_cvt_pk_bf16_f32 v95, v54, v55\n\t" \
    "v_exp_f32 v56, v56\n\t" \
    "ds_read_b128 v[80:83], %[v1l] offset:0\n\t" \
    "v_exp_f32 v57, v57\n\t" \
    "v_exp_f32 v58, v58\n\t" \
    "v_exp_f32 v59, v59\n\t" \
    "s_waitcnt lgkmcnt(4)\n\t" \
    "v_mfma_f32_32x32x16_bf16 v[0:15], v[84:87], v[92:95], v[0:15]\n\t" \
    "v_exp_f32 v60, v60\n\t" \
    "v_exp_f32 v61, v61\n\t" \
    "v_exp_f32 v62, v62\n\t" \
    "v_exp_f32 v63, v63\n\t" \
    "s_waitcnt lgkmcnt(2)\n\t" \
    "v_cvt_pk_bf16_f32 v96, v56, v57\n\t" \
    "v_cvt_pk_bf16_f32 v97, v58, v59\n\t" \
    "v_cvt_pk_bf16_f32 v98, v60, v61\n\t" \
    "v_cvt_pk_bf16_f32 v99, v62, v63\n\t" \
    "s_add_u32 %[cnt], %[cnt], 1\n\t" \
    "s_cmp_lt_u32 %[cnt], 8\n\t" \
    "s_cbranch_scc1 Lloop%=\n\t" \
    "s_waitcnt lgkmcnt(0)\n\t" \
    "v_mfma_f32_32x32x16_bf16 v[0:15], v[88:91], v[96:99], v[0:15]\n\t" \
    "s_mov_b32 m0, %[sv]\n\t" \
    "s_setprio 0\n\t" \
    "s_nop 7\n\t" \
    "s_nop 7\n\t"
#define ATTN_CLOBBERS "v32", "v33", "v34", "v35", "v36", "v37", "v38", "v39", "v40", "v41", "v42", "v43", "v44", "v45", "v46", "v47", "v48", "v49", "v50", "v51", "v52", "v53", "v54", "v55", "v56", "v57", "v58", "v59", "v60", "v61", "v62", "v63", "v68", "v69", "v70", "v71", "v72", "v73", "v74", "v75", "v76", "v77", "v78", "v79", "v80", "v81", "v82", "v83", "v84", "v85", "v86", "v87", "v88", "v89", "v90", "v91", "v92", "v93", "v94", "v95", "v96", "v97", "v98", "v99"
#define ATTN_INSTR 0

DI void attn_block(char* __restrict__ ws, char* lds, int head, int chunk, int tid) {
    const int l = tid & 63, w = __builtin_amdgcn_readfirstlane(tid >> 6), h = l >> 5;
    const int qtile = chunk * 8 + w;
    const unsigned lds0 = (unsigned)(uintptr_t)lds;
    const char* kvbase = ws + WS_KV + (size_t)head * 64 * 2048;
    const unsigned dst = lds0 + w * AT_TS;
    bf16x8 qf; float km, gate;
    asm volatile("global_load_dwordx4 %0, %3, off\n\tglobal_load_dword %1, %4, off\n\tglobal_load_dword %2, %5, off"
                 : "=&v"(qf), "=&v"(km), "=&v"(gate)
                 : "v"(ws + WS_QF + ((size_t)(head * 64 + qtile) * 1024) + l * 16), "v"((const float*)(ws + WS_KMAX) + l * 32 + head),
                   "v"((const float*)(ws + WS_GATE) + qtile * 32 + (l & 31)) : "memory");
    auto issue3 = [&]() {
#pragma unroll
        for (int c = 0; c < 3; ++c) { glds16(kvbase + c * 16384 + w * 2048 + l * 16, dst + c * AT_SLOT); glds16(kvbase + c * 16384 + w * 2048 + 1024 + l * 16, dst + c * AT_SLOT + 1024); }
    };
    issue3();
    {
        const u32x4 ones = {0x3f803f80u, 0x3f803f80u, 0x3f803f80u, 0x3f803f80u};
        *(LDS_AS u32x4*)((lds_ptr)lds + (tid >> 4) * AT_TS + 2048 + (tid & 15) * 16) = ones;
        if (tid == 0) *(LDS_AS unsigned*)((lds_ptr)lds + 4 * AT_SLOT) = 0u;
    }
    asm volatile("s_waitcnt vmcnt(6)" : "+v"(qf), "+v"(km), "+v"(gate));
    float qn = 0.f;
    { const u32x4 qu = __builtin_bit_cast(u32x4, qf);
#pragma unroll
      for (int e = 0; e < 4; ++e) { qn = __builtin_fmaf(bf_lo(qu[e]), bf_lo(qu[e]), qn); qn = __builtin_fmaf(bf_hi(qu[e]), bf_hi(qu[e]), qn); } }
    qn = swap32_sum(qn);
    float ref = __builtin_amdgcn_sqrtf(qn * wave_max_fast(km));
    unsigned kl = lds0 + l * 16;
    const bool onesl = (l & 16) != 0;
    const unsigned ol = lds0 + 2048 + ((((l & 15) >= 4) && ((l & 15) < 12)) ? 64 : 0);
    unsigned v0l = onesl ? ol : lds0 + 1024 + h * 256 + (l & 15) * 16, v1l = onesl ? ol : v0l + 512;
    LDS_AS volatile unsigned* flag = (LDS_AS volatile unsigned*)((lds_ptr)lds + 4 * AT_SLOT);
    const size_t ooff = WS_HO + ((size_t)(qtile * 88 + head) * 1024) + l * 16;
#pragma unroll 1
    for (int pass = 0;; ++pass) {
        f32x16 negm, o;
#pragma unroll
        for (int r = 0; r < 16; ++r) negm[r] = -ref;
        unsigned sv, cnt, t0, dl, goff = 3 * 16384 + w * 2048 + l * 16;
        STAMP(2, 1);
        asm volatile(ATTN_ASM
                     : "=&{v[0:15]}"(o), [sv] "=&s"(sv), [cnt] "=&s"(cnt), [t0] "=&s"(t0), [dl] "=&s"(dl), [goff] "+v"(goff), [kl] "+v"(kl), [v0l] "+v"(v0l), [v1l] "+v"(v1l)
                     : "{v[16:31]}"(negm), "{v[64:67]}"(qf), [dst] "s"(dst), [base] "s"(kvbase)
                     : ATTN_CLOBBERS, "scc", "memory");
        STAMP(2, 3);
        st16_wt(ws, ooff, pack8(o, 0, gate * __builtin_amdgcn_rcpf(o[8])));
        drain_stores();
        if (pass == 1) { __syncthreads(); break; }
        const bool bad = !(o[8] >= 9.094947e-13f);
        if (__any(bad) && l == 0) *flag = 1u;
        __syncthreads();
        if (*flag == 0u) break;
        float m = -3.0e38f;
        for (int t = 0; t < 64; ++t) {
            const bf16x8 kf = *(const bf16x8*)(kvbase + (size_t)t * 2048 + l * 16);
            const f32x16 s = MFMA(kf, qf, f32x16{});
#pragma unroll
            for (int r = 0; r < 16; ++r) m = fmaxf(m, s[r]);
        }
        ref = swap32_max(m);
        asm volatile("s_waitcnt vmcnt(0)" ::: "memory");
        issue3();
    }
    if (tid == 0) __hip_atomic_fetch_add(sync_word(ws, SY_O + chunk), 1u, RLX_AGENT);
}
constexpr int SP_ST = 24576, SP_LDS = 3 * SP_ST + 256, FN_ST = 16384;

template <int N> DI void wait_vm() { asm volatile("s_waitcnt vmcnt(%0)" :: "i"(N) : "memory"); }

DI void gate_block(const u16* __restrict__ x, const u16* __restrict__ w_in, const u16* __restrict__ w_sp, const u16* __restrict__ b_sp,
                   const u16* __restrict__ wo, const u16* __restrict__ w_out, char* __restrict__ ws, u16* __restrict__ out, char* lds, int g, int tid) {
    const int l = tid & 63, w = __builtin_amdgcn_readfirstlane(tid >> 6), h = l >> 5, r32 = l & 31;
    const int tl = (g >> 3) & 7, tt = (g & 3) * 8 + tl, ft = ((g >> 2) & 1) * 4 + (g >> 6);
    const int rot = SP_ROT(tl, g >> 6);
    const unsigned lds0 = (unsigned)(uintptr_t)lds;
    const int q4 = (l & 15) >> 2, p4 = l & 3, b16 = (l >> 4) & 1;
    const unsigned lo = l * 16;
    if (tid == 0) TL(5, 0, 99);
    if (GPRIO) __builtin_amdgcn_s_setprio(GPRIO);
    const int j = w - 4, lrow = 8 * j + (l >> 3);
    const unsigned xv = (unsigned)(lrow * D_ + ((l & 7) ^ ((lrow >> 1) & 7)) * 8) * 2;
    const int wr = 4 * j + (l >> 4), wc = ((((l & 15) >> 2) ^ (wr & 3)) << 2) | (l & 3);
    const unsigned wv = (unsigned)(wr * 1792 + wc * 8) * 2;
    auto issue_z = [&](int s) {
        const char* xu = (const char*)(x + (size_t)tt * 64 * D_); const char* wu = (const char*)(w_in + ft * 128);
        const unsigned dst = lds0 + (s % 3) * SP_ST + j * 1024; const int sr = (s + rot) & 7;
        glds16s(xu + sr * 128, xv, dst); glds16s(xu + 32 * D_ * 2 + sr * 128, xv, dst + 4096);
#pragma unroll
        for (int i = 0; i < 4; ++i) glds16s(wu + (size_t)(sr * 64 + 16 * i) * 3584, wv, dst + 8192 + i * 4096);
    };
    if (w >= 4 && ft < 7) { issue_z(0); issue_z(1); }
    ln_apply(ws, lds, g, tid);
    drain_stores();
    bar_only();
    if (tid == 0) __hip_atomic_fetch_add(sync_word(ws, SY_LN + (g & 15)), 1u, RLX_AGENT);
    STAMP(5, 1);
    if (tid == 0) TL(5, 0, 100);
    if (ft < 7) {
        if (w >= 4) {
            if (LPRIO != GPRIO) __builtin_amdgcn_s_setprio(LPRIO);
            issue_z(2);
            bar_only();
#pragma unroll 1
            for (int s = 0; s < 7; ++s) {
                if (s == 0) bar_only(); else if (s + 2 < 8) wait_bar<6>(); else wait_bar<0>();
                if (s + 3 < 8) issue_z(s + 3);
            }
            bar_only();
            if (j == 3) {
                wait_words(ws, SY_LN, 16, 16u, l);
                const char* zu = ws + WS_Z2N + (size_t)(ft * 4) * 128 * 1024;
                const unsigned pv = (unsigned)((tl >> 1) * 128 * 1024 + ((tl & 1) * 16 + (l & 15)) * 128);
                auto pf = [&](int s4) { const int sr = (4 * s4 + (l >> 4) + rot) & 31; prefetch_line(zu, pv + sr * 4096, lds0 + 3 * SP_ST); };
                pf(0); pf(1); pf(2);
                bar_only();
                bar_only();
#pragma unroll 1
                for (int s = 0; s < 31; ++s) { bar_only(); if ((s & 3) == 0 && s + 12 < 32) pf((s >> 2) + 3); }
                drain_stores();
            } else {
                const unsigned a0 = (unsigned)((l >> 3) * S_ * 2 + (((l & 7) ^ ((l >> 4) & 7)) << 4)), a1 = a0 ^ 64u;
                const unsigned va = (j & 1) ? a1 : a0, vb = (j & 1) ? a0 : a1;
                const char* au = (const char*)(w_sp + (size_t)(tt * 64 + 8 * j) * S_);
                auto issue = [&](int s) {
                    const unsigned dst = lds0 + (s % 6) * 8192 + j * 1024; const int sr = (s + rot) & 31;
                    glds16s(au + sr * 128, va, dst); glds16s(au + 24 * S_ * 2 + sr * 128, vb, dst + 3072);
                    if (j < 2) glds16s(au + 48 * S_ * 2 + sr * 128, va, dst + 6144);
                };
                auto waitst = [&](int younger) {
                    if (j < 2) { if (younger >= 4) wait_vm<12>(); else if (younger == 3) wait_vm<9>(); else if (younger == 2) wait_vm<6>(); else if (younger == 1) wait_vm<3>(); else wait_vm<0>(); }
                    else { if (younger >= 4) wait_vm<8>(); else if (younger == 3) wait_vm<6>(); else if (younger == 2) wait_vm<4>(); else if (younger == 1) wait_vm<2>(); else wait_vm<0>(); }
                };
#pragma unroll 1
                for (int s = 0; s < 5; ++s) issue(s);
                bar_only();
                waitst(4); bar_only();
                issue(5);
#pragma unroll 1
                for (int s = 0; s < 31; ++s) {
                    waitst(min(s + 5, 31) - (s + 1)); bar_only();
                    if (s + 6 < 32) issue(s + 6);
                }
            }
            bar_only();
        } else {
            const int fb = w;
            int boff[4];
#pragma unroll
            for (int ks = 0; ks < 4; ++ks) { const int f = (r32 >> 1) & 7; boff[ks] = r32 * 128 + (((ks * 2 + h) ^ f) << 4); }
            const int fbg = ft * 4 + fb, fg = 2 * fbg;
            const float gt0 = ((const float*)(ws + WS_GATE))[(tt * 2) * 32 + r32], gt1 = ((const float*)(ws + WS_GATE))[(tt * 2 + 1) * 32 + r32];
            const float bs0 = __uint_as_float((unsigned)b_sp[(tt * 2) * 32 + r32] << 16), bs1 = __uint_as_float((unsigned)b_sp[(tt * 2 + 1) * 32 + r32] << 16);
            LDS_AS u32x4* z1p = (LDS_AS u32x4*)((lds_ptr)lds + 49152 + w * 4096) + l;
            {
                f32x16 acc[2]; acc[0] = f32x16{}; acc[1] = f32x16{};
                const int zoff = 8192 + (8 * h + q4) * 256 + ((fb ^ q4) << 6) + b16 * 32 + p4 * 8;
                bf16x8 aa[2], ba[2][2], ab[2], bb[2][2];
                auto rdh = [&](int s, int half, bf16x8 (&af)[2], bf16x8 (&bf)[2][2]) {
                    lds_cptr base = (lds_cptr)lds + (s % 3) * SP_ST;
#pragma unroll
                    for (int kk = 0; kk < 2; ++kk) {
                        const int ks = 2 * half + kk;
                        af[kk] = trfrag(base + zoff + ks * 4096, base + zoff + ks * 4096 + 1024);
                        bf[0][kk] = ldsfrag(base + boff[ks]); bf[1][kk] = ldsfrag(base + boff[ks] + 4096);
                    }
                };
                auto mm = [&](bf16x8 (&af)[2], bf16x8 (&bf)[2][2]) {
#pragma unroll
                    for (int kk = 0; kk < 2; ++kk) { acc[0] = MFMA(af[kk], bf[0][kk], acc[0]); acc[1] = MFMA(af[kk], bf[1][kk], acc[1]); }
                };
                bar_only();
                rdh(0, 0, aa, ba);
#pragma unroll 1
                for (int s = 0; s < 7; ++s) {
                    rdh(s, 1, ab, bb); mm(aa, ba); interleave<4, 2>(); SB();
                    bar_only();
                    rdh(s + 1, 0, aa, ba); mm(ab, bb); interleave<4, 2>(); SB();
                }
                rdh(7, 1, ab, bb); mm(aa, ba); interleave<4, 2>(); SB();
                mm(ab, bb);
                bar_only();
                if (tid == 0) TL(5, 0, 101);
#pragma unroll
                for (int tb = 0; tb < 2; ++tb) {
                    const float og = 1.0f - (tb ? gt1 : gt0);
#pragma unroll
                    for (int gq = 0; gq < 2; ++gq) {
                        u32x4 zv;
#pragma unroll
                        for (int e = 0; e < 4; ++e) zv[e] = cvtpk(gelu_tanh(acc[tb][8 * gq + 2 * e]) * og, gelu_tanh(acc[tb][8 * gq + 2 * e + 1]) * og);
                        z1p[(tb * 2 + gq) * 64] = zv;
                    }
                }
            }
            f32x16 acc[2]; acc[0] = f32x16{}; acc[1] = f32x16{};
            {
                if (tid == 0) TL(5, 0, 102);
                bar_only();
                if (tid == 0) TL(5, 0, 103);
                bf16x8 za[4], zb[4];
                const size_t zoff = WS_Z2N + (size_t)fbg * 128 * 1024 + lo;
                auto ldz = [&](int s, int ks) { const int sr = (min(s, 31) + rot) & 31; return ld16_dev(ws, zoff + (size_t)((sr * 4 + ks) * 1024)); };
#pragma unroll
                for (int ks = 0; ks < 4; ++ks) za[ks] = ldz(0, ks);
#pragma unroll
                for (int ks = 0; ks < 4; ++ks) zb[ks] = ldz(1, ks);
                bf16x8 bx[2][2], by[2][2];
                auto rdh = [&](int s, int half, bf16x8 (&bf)[2][2]) {
                    lds_cptr base = (lds_cptr)lds + (s % 6) * 8192;
#pragma unroll
                    for (int kk = 0; kk < 2; ++kk) { bf[0][kk] = ldsfrag(base + boff[2 * half + kk]); bf[1][kk] = ldsfrag(base + boff[2 * half + kk] + 4096); }
                };
                auto stage = [&](int s, bf16x8 (&z)[4], bool last) {
                    rdh(s, 1, by);
                    acc[0] = MFMA(z[0], bx[0][0], acc[0]); acc[1] = MFMA(z[0], bx[1][0], acc[1]);
                    acc[0] = MFMA(z[1], bx[0][1], acc[0]); acc[1] = MFMA(z[1], bx[1][1], acc[1]); interleave<4, 1>(); SB();
                    z[0] = ldz(s + 2, 0); z[1] = ldz(s + 2, 1); SB();
                    if (!last) { bar_only(); rdh(s + 1, 0, bx); }
                    acc[0] = MFMA(z[2], by[0][0], acc[0]); acc[1] = MFMA(z[2], by[1][0], acc[1]);
                    acc[0] = MFMA(z[3], by[0][1], acc[0]); acc[1] = MFMA(z[3], by[1][1], acc[1]); interleave<4, 1>(); SB();
                    z[2] = ldz(s + 2, 2); z[3] = ldz(s + 2, 3); SB();
                };
                bar_only();
                rdh(0, 0, bx);
#pragma unroll 1
                for (int s = 0; s < 30; s += 2) { stage(s, za, false); stage(s + 1, zb, false); }
                stage(30, za, false); stage(31, zb, true);
                if (tid == 0) TL(5, 0, 104);
            }
#pragma unroll
            for (int tb = 0; tb < 2; ++tb) {
                const float bs = tb ? bs1 : bs0;
#pragma unroll
                for (int gq = 0; gq < 2; ++gq) {
                    u32x4 ov; const u32x4 zv = z1p[(tb * 2 + gq) * 64];
#pragma unroll
                    for (int e = 0; e < 4; ++e) ov[e] = cvtpk((acc[tb][8 * gq + 2 * e] + bs) * bf_lo(zv[e]), (acc[tb][8 * gq + 2 * e + 1] + bs) * bf_hi(zv[e]));
                    st16_wt(ws, WS_HO + ((size_t)((tt * 2 + tb) * 88 + 32 + fg + gq) * 1024) + lo, ov);
                }
            }
            drain_stores();
            bar_only();
            if (tid == 0) __hip_atomic_fetch_add(sync_word(ws, SY_H + tt), 1u, RLX_AGENT);
        }
    }
    STAMP(5, 3);
    if (tid == 0) TL(5, 0, 105);
    {
        if (GPRIO) __builtin_amdgcn_s_setprio(GPRIO);
        const int tb = w & 1, kq = w >> 1;
        const int prow = 8 * w + (l >> 3);
        const unsigned pv = (unsigned)(prow * D_ + ((l & 7) ^ (((prow >> 1) & 1) << 2)) * 8) * 2;
        const char* w0 = (const char*)(w_out + ft * 64);
        const char* w1 = (const char*)(wo + ft * 64);
        auto issue = [&](int s) {
            const unsigned dst = lds0 + (s & 3) * FN_ST + w * 1024;
            const char* wp = s < 7 ? w0 + (size_t)s * 128 * D_ * 2 : w1 + (size_t)(s - 7) * 128 * D_ * 2;
            glds16s(wp, pv, dst); glds16s(wp + (size_t)64 * D_ * 2, pv, dst + 8192);
        };
        issue(0); issue(1); issue(2); issue(3);
        const int woff0 = (kq * 32 + 4 * h + q4) * 128 + (((b16 * 2 + (p4 >> 1)) ^ ((q4 >> 1) << 2)) << 4) + (p4 & 1) * 8, woff1 = woff0 ^ 64;
        const size_t hoff = WS_HO + ((size_t)((tt * 2 + tb) * 88 + 2 * kq) * 1024) + lo;
        f32x16 acc[2]; acc[0] = f32x16{}; acc[1] = f32x16{};
        bf16x8 hf[14];
        auto comp = [&](int s, const bf16x8& h0, const bf16x8& h1) {
            lds_cptr base = (lds_cptr)lds + (s & 3) * FN_ST;
            const bf16x8 w00 = trfrag(base + woff0, base + woff0 + 1024), w01 = trfrag(base + woff1, base + woff1 + 1024);
            const bf16x8 w10 = trfrag(base + woff0 + 2048, base + woff0 + 3072), w11 = trfrag(base + woff1 + 2048, base + woff1 + 3072);
            acc[0] = MFMA(w00, h0, acc[0]); acc[1] = MFMA(w01, h0, acc[1]);
            acc[0] = MFMA(w10, h1, acc[0]); acc[1] = MFMA(w11, h1, acc[1]);
            SB();
        };
        if (w == 7) wait_words(ws, SY_H + tt, 1, 7u, l);
        if (w == 0) TL(5, 1, 99);
        bar_only();
        if (w == 0) TL(5, 1, 100);
#pragma unroll
        for (int q = 0; q < 14; ++q) hf[q] = ld16_dev(ws, hoff + (size_t)((32 + 8 * (q >> 1) + (q & 1)) * 1024));
        wait_vm<20>(); bar_only();            comp(0, hf[0], hf[1]);
        wait_vm<18>(); bar_only(); issue(4);  comp(1, hf[2], hf[3]);
        wait_vm<18>(); bar_only(); issue(5);  comp(2, hf[4], hf[5]);
        wait_vm<18>(); bar_only(); issue(6);  comp(3, hf[6], hf[7]);
        wait_vm<4>();  bar_only(); issue(7);  comp(4, hf[8], hf[9]);
        wait_vm<4>();  bar_only(); issue(8);  comp(5, hf[10], hf[11]);
        wait_vm<4>();  bar_only(); issue(9);  comp(6, hf[12], hf[13]);
        if (w == 0) TL(5, 1, 101);
        if (w == 7) wait_words(ws, SY_O + (tt >> 2), 1, 32u, l);
        bar_only();
        if (w == 0) TL(5, 1, 102);
#pragma unroll
        for (int q = 0; q < 8; ++q) hf[q] = ld16_dev(ws, hoff + (size_t)((8 * (q >> 1) + (q & 1)) * 1024));
        wait_vm<12>(); bar_only(); issue(10); comp(7, hf[0], hf[1]);
        wait_vm<12>(); bar_only();            comp(8, hf[2], hf[3]);
        wait_vm<10>(); bar_only();            comp(9, hf[4], hf[5]);
        wait_vm<0>();  bar_only();            comp(10, hf[6], hf[7]);
        if (w == 0) TL(5, 1, 103);
        bar_only();
        LDS_AS f32x4* red = (LDS_AS f32x4*)lds;
#pragma unroll
        for (int db = 0; db < 2; ++db)
#pragma unroll
            for (int q = 0; q < 4; ++q) red[((w * 2 + db) * 4 + q) * 64 + l] = f32x4{acc[db][4 * q], acc[db][4 * q + 1], acc[db][4 * q + 2], acc[db][4 * q + 3]};
        bar_only();
        const int db = kq & 1, gq = kq >> 1;
        f32x4 v0 = {0.f, 0.f, 0.f, 0.f}, v1 = v0;
#pragma unroll
        for (int q = 0; q < 4; ++q) { const int ww = q * 2 + tb; v0 += red[((ww * 2 + db) * 4 + 2 * gq) * 64 + l]; v1 += red[((ww * 2 + db) * 4 + 2 * gq + 1) * 64 + l]; }
        const unsigned a0 = cvtpk(v0[0], v0[1]), a1 = cvtpk(v0[2], v0[3]), b0 = cvtpk(v1[0], v1[1]), b1 = cvtpk(v1[2], v1[3]);
        auto r0 = __builtin_amdgcn_permlane32_swap(a0, b0, false, false);
        auto r1 = __builtin_amdgcn_permlane32_swap(a1, b1, false, false);
        const u32x4 ov = {r0[0], r1[0], r0[1], r1[1]};
        *(u32x4*)(out + (size_t)(tt * 64 + tb * 32 + r32) * D_ + ft * 64 + db * 32 + gq * 16 + h * 8) = ov;
        if (w == 0) TL(5, 1, 104);
    }
}

__global__ __launch_bounds__(512, 4) void k_main(const u16* __restrict__ x, const u16* __restrict__ w_in, const u16* __restrict__ w_sp, const u16* __restrict__ b_sp,
                                                 const u16* __restrict__ wo, const u16* __restrict__ w_out, char* __restrict__ ws, u16* __restrict__ out) {
    __shared__ __attribute__((aligned(1024))) char lds[AT_LDS > SP_LDS ? AT_LDS : SP_LDS];
    const int b = blockIdx.x;
    if (b < 256) {
        STAMP(2, 0);
        attn_block(ws, lds, (b & 7) * 4 + ((b >> 3) & 3), b >> 5, threadIdx.x);
        STAMP(2, 2);
    } else {
        STAMP(5, 0);
        gate_block(x, w_in, w_sp, b_sp, wo, w_out, ws, out, lds, b - 256, threadIdx.x);
        STAMP(5, 2);
    }
}

extern "C" void kernel_launch(void* const* d_in, const int* in_sizes, int n_in, void* d_out, int out_size, void* d_ws, size_t ws_size, hipStream_t stream) {
    const u16* x = (const u16*)d_in[0];
    const u16* wqkv = (const u16*)d_in[1];
    const u16* wo = (const u16*)d_in[2];
    const u16* w_in = (const u16*)d_in[4];
    const u16* w_sp = (const u16*)d_in[5];
    const u16* b_sp = (const u16*)d_in[6];
    const u16* w_out = (const u16*)d_in[7];
    const u16* gate_w = (const u16*)d_in[8];
    char* ws = (char*)d_ws;
    k1_proj<<<256, 512, 0, stream>>>(x, wqkv, w_in, gate_w, ws);
    k_main<<<512, 512, 0, stream>>>(x, w_in, w_sp, b_sp, wo, w_out, ws, (u16*)d_out);
}
```

```cpp
#include <hip/hip_runtime.h>
#include <hip/hip_bf16.h>
#include <stdint.h>

typedef __attribute__((ext_vector_type(8))) short bf16x8;
typedef __attribute__((ext_vector_type(4))) short s16x4;
typedef __attribute__((ext_vector_type(16))) float f32x16;
typedef __attribute__((ext_vector_type(4))) unsigned u32x4;
typedef __attribute__((ext_vector_type(2))) float f32x2;
typedef __attribute__((ext_vector_type(4))) float f32x4;
typedef __attribute__((ext_vector_type(2))) __bf16 bf16x2_t;
#define LDS_AS __attribute__((address_space(3)))
typedef LDS_AS const char* lds_cptr;
typedef LDS_AS char* lds_ptr;
typedef short v4i16_t __attribute__((ext_vector_type(4)));
typedef unsigned short u16;
#define DI __device__ __forceinline__

#define MFMA(a, b, c) __builtin_amdgcn_mfma_f32_32x32x16_bf16((a), (b), (c), 0, 0, 0)
#define SB() __builtin_amdgcn_sched_barrier(0)
template <int NM, int ND> DI void interleave() {
#pragma unroll
    for (int i = 0; i < NM; ++i) { __builtin_amdgcn_sched_group_barrier(0x008, 1, 0); __builtin_amdgcn_sched_group_barrier(0x100, ND, 0); }
}
#define TL(kid, who, idx) do {} while (0)
#define WAIT_BAR(N) asm volatile("s_waitcnt vmcnt(" #N ") lgkmcnt(0)\n\ts_barrier" ::: "memory")

#define K1_ROT(mt, p) ((mt) & 7)
#define SP_ROT(tl, fl) ((tl) * 4 + (fl) * 2)
#define PACE 1
#define PACE_() do { if (PACE == 1) __builtin_amdgcn_s_sleep(1); else if (PACE == 2) asm volatile("s_nop 7"); else if (PACE == 3) asm volatile("s_nop 11"); else if (PACE == 4) asm volatile("s_nop 15"); SB(); } while (0)
#define LPRIO 0
#define CODE_PF 1
#define DATA_PF 0
#define GPRIO 2
#define FN_ROT(i, dt) ((((i) & 3) * 3 + (dt)) % 11)
constexpr int S_ = 2048, D_ = 512, FH = 896;
constexpr float QSCALE = 0.25f * 1.4426950408889634f;

constexpr size_t WS_QF    = 0;
constexpr size_t WS_KV    = WS_QF + 32u * 64 * 1024;
constexpr size_t WS_Z2    = WS_KV + 32u * 64 * 2048;
constexpr size_t WS_Z2N   = WS_Z2 + 64u * 56 * 1024;
constexpr size_t WS_HO    = WS_Z2N + 28u * 128 * 1024;
constexpr size_t WS_STATS = WS_HO + 64u * 88 * 1024;
constexpr size_t WS_KMAX  = WS_STATS + 2048u * 28 * 8;
constexpr size_t WS_GATE  = WS_KMAX + 64u * 32 * 4;
constexpr size_t WS_SYNC  = WS_GATE + 2048u * 4;
constexpr int SY_LN = 0, SY_H = 16, SY_O = 48, SY_TMO = 56, SY_N = 57;
constexpr size_t WS_END   = WS_SYNC + SY_N * 256;

#define STAMP(kid, slot) do {} while (0)
DI unsigned cvtpk(float a, float b) { f32x2 f = {a, b}; bf16x2_t r = __builtin_convertvector(f, bf16x2_t); return __builtin_bit_cast(unsigned, r); }
DI float bf_lo(unsigned u) { return __uint_as_float(u << 16); }
DI float bf_hi(unsigned u) { return __uint_as_float(u & 0xffff0000u); }
DI void glds16(const void* g, unsigned lds_base) {
    unsigned sv;
    asm volatile("s_mov_b32 %0, m0\n\ts_mov_b32 m0, %2\n\ts_nop 0\n\tglobal_load_lds_dwordx4 %1, off\n\ts_mov_b32 m0, %0"
                 : "=&s"(sv) : "v"(g), "s"(lds_base) : "memory");
}
DI void glds16s(const void* ubase, unsigned voff, unsigned lds_base) {
    asm volatile("s_mov_b32 m0, %2\n\ts_nop 0\n\tglobal_load_lds_dwordx4 %0, %1" :: "v"(voff), "s"(ubase), "s"(lds_base) : "memory");
}
#define GAS __attribute__((address_space(1)))
typedef GAS unsigned gu32;
#define RLX_AGENT __ATOMIC_RELAXED, __HIP_MEMORY_SCOPE_AGENT
DI gu32* sync_word(char* ws, int i) { return (gu32*)(ws + WS_SYNC + (size_t)i * 256); }
DI void st16_wt(char* ws, size_t off, u32x4 v) {
    __builtin_amdgcn_raw_buffer_store_b128(v, __builtin_amdgcn_make_buffer_rsrc(ws, 0, (int)WS_END, 0x00020000), (int)off, 0, 16);
}
DI bf16x8 ld16_dev(char* ws, size_t off) {
    return __builtin_bit_cast(bf16x8, __builtin_amdgcn_raw_buffer_load_b128(__builtin_amdgcn_make_buffer_rsrc(ws, 0, (int)WS_END, 0x00020000), (int)off, 0, 16));
}
DI void prefetch_line(const void* ubase, unsigned voff, unsigned lds_dump) {
    asm volatile("s_mov_b32 m0, %2\n\ts_nop 0\n\tglobal_load_lds_dword %0, %1" :: "v"(voff), "s"(ubase), "s"(lds_dump) : "memory");
}
DI void prefetch_ptr(const void* g, unsigned lds_dump) {
    asm volatile("s_mov_b32 m0, %1\n\ts_nop 0\n\tglobal_load_lds_dword %0, off" :: "v"(g), "s"(lds_dump) : "memory");
}
DI void drain_stores() { asm volatile("s_waitcnt vmcnt(0)" ::: "memory"); }
DI void wait_words(char* ws, int first, int n, unsigned want, int lane) {
    gu32* p = sync_word(ws, first + lane % n);
    for (unsigned spins = 0;; ++spins) {
        if (__all(__hip_atomic_load(p, RLX_AGENT) >= want)) break;
        if (spins > (1u << 22)) { if (lane == 0) __hip_atomic_store(sync_word(ws, SY_TMO), 1u, RLX_AGENT); break; }
        __builtin_amdgcn_s_sleep(2);
    }
    asm volatile("" ::: "memory");
}
DI void bar_only() { asm volatile("" ::: "memory"); __builtin_amdgcn_s_waitcnt(0xC07F); __builtin_amdgcn_s_barrier(); asm volatile("" ::: "memory"); }
template <int N> DI void wait_bar() { asm volatile("s_waitcnt vmcnt(%0)" :: "i"(N) : "memory"); bar_only(); }
DI s16x4 vtr(lds_cptr p) { return __builtin_bit_cast(s16x4, __builtin_amdgcn_ds_read_tr16_b64_v4i16((LDS_AS v4i16_t*)p)); }
DI bf16x8 trfrag(lds_cptr p0, lds_cptr p1) { s16x4 lo = vtr(p0), hi = vtr(p1); return __builtin_shufflevector(lo, hi, 0, 1, 2, 3, 4, 5, 6, 7); }
DI bf16x8 ldsfrag(lds_cptr p) { return *(const LDS_AS bf16x8*)p; }
DI u32x4 pack8(const f32x16& a, int o, float sc) {
    u32x4 r; r[0] = cvtpk(a[o] * sc, a[o + 1] * sc); r[1] = cvtpk(a[o + 2] * sc, a[o + 3] * sc);
    r[2] = cvtpk(a[o + 4] * sc, a[o + 5] * sc); r[3] = cvtpk(a[o + 6] * sc, a[o + 7] * sc); return r;
}
DI float swap32_sum(float v) { auto r = __builtin_amdgcn_permlane32_swap(__float_as_uint(v), __float_as_uint(v), false, false); return __uint_as_float(r[0]) + __uint_as_float(r[1]); }
DI float swap32_max(float v) { auto r = __builtin_amdgcn_permlane32_swap(__float_as_uint(v), __float_as_uint(v), false, false); return fmaxf(__uint_as_float(r[0]), __uint_as_float(r[1])); }
DI float dpp_max16(float v) {
    v = fmaxf(v, __uint_as_float(__builtin_amdgcn_update_dpp(0u, __float_as_uint(v), 0x128, 0xf, 0xf, false)));
    v = fmaxf(v, __uint_as_float(__builtin_amdgcn_update_dpp(0u, __float_as_uint(v), 0x124, 0xf, 0xf, false)));
    v = fmaxf(v, __uint_as_float(__builtin_amdgcn_update_dpp(0u, __float_as_uint(v), 0x122, 0xf, 0xf, false)));
    v = fmaxf(v, __uint_as_float(__builtin_amdgcn_update_dpp(0u, __float_as_uint(v), 0x121, 0xf, 0xf, false)));
    return v;
}
DI float wave_max_fast(float v) {
    v = dpp_max16(v);
    auto r = __builtin_amdgcn_permlane16_swap(__float_as_uint(v), __float_as_uint(v), false, false);
    v = fmaxf(__uint_as_float(r[0]), __uint_as_float(r[1]));
    return swap32_max(v);
}

DI float gelu_tanh(float x) {
    const float c1 = -2.0f * 0.7978845608028654f * 1.4426950408889634f, c2 = c1 * 0.044715f;
    const float a = x * __builtin_fmaf(x * x, c2, c1);
    return x * __builtin_amdgcn_rcpf(1.0f + __builtin_amdgcn_exp2f(a));
}

constexpr int K1_ST = 40960, K1_LDS = 3 * K1_ST + 1024 + 256;
extern "C" __global__ void k_main(const u16* __restrict__ x, const u16* __restrict__ w_in, const u16* __restrict__ w_sp, const u16* __restrict__ b_sp,
                                  const u16* __restrict__ wo, const u16* __restrict__ w_out, char* __restrict__ ws, u16* __restrict__ out);

template <int TYPE>
DI void k1_body(const u16* __restrict__ x, const u16* __restrict__ wqkv, const u16* __restrict__ w_in, const u16* __restrict__ gate_w,
                const u16* __restrict__ w_sp, const u16* __restrict__ wo, const u16* __restrict__ w_out,
                char* __restrict__ ws, char* lds, int mt, int p, int tid) {
    if (tid == 0) TL(0, 1, 99);
    constexpr int NG = TYPE == 0 ? 3 : (TYPE == 1 ? 2 : 1);
    constexpr int NA = TYPE == 2 ? 1 : (TYPE == 3 ? 2 : NG);
    constexpr int P = 4 + 2 * NG;
    const int l = tid & 63, w = __builtin_amdgcn_readfirstlane(tid >> 6);
    const int rot = K1_ROT(mt, p);
    if (w >= 4) {
        const int j = w - 4;
        const unsigned lds0 = (unsigned)(uintptr_t)lds;
        const int row = 8 * j + (l >> 3);
        const unsigned xv = (unsigned)(row * D_ + ((l & 7) ^ ((row >> 1) & 7)) * 8) * 2;
        const int wch = ((l & 7) ^ (((row >> 1) & 1) << 2)) * 8;
        const unsigned qv = (unsigned)(row * 1536 + wch) * 2, zv = (unsigned)(row * 1792 + wch) * 2;
        const char* xu = (const char*)(x + (size_t)mt * 128 * D_);
        const char* g0 = (const char*)(wqkv + p * 64 + (TYPE == 0 ? 0 : 1024));
        const char* g1 = TYPE == 0 ? (const char*)(wqkv + p * 64 + 512) : (const char*)(w_in + FH + (8 + p) * 64);
        const char* g2 = (const char*)(w_in + FH + p * 64);
        auto issue = [&](int s) {
            const unsigned dst = lds0 + (s % 3) * K1_ST + j * 1024;
            const int sr = (s + rot) & 7;
#pragma unroll
            for (int i = 0; i < 4; ++i) glds16s(xu + (size_t)i * 32 * D_ * 2 + sr * 128, xv, dst + i * 4096);
            glds16s(g0 + (size_t)sr * 64 * 3072, qv, dst + 16384); glds16s(g0 + (size_t)(sr * 64 + 32) * 3072, qv, dst + 16384 + 4096);
            if (NG >= 2) { constexpr int ld = TYPE == 0 ? 3072 : 3584; const unsigned gv = TYPE == 0 ? qv : zv;
                glds16s(g1 + (size_t)sr * 64 * ld, gv, dst + 16384 + 8192); glds16s(g1 + (size_t)(sr * 64 + 32) * ld, gv, dst + 16384 + 8192 + 4096); }
            if (NG >= 3) { glds16s(g2 + (size_t)sr * 64 * 3584, zv, dst + 32768); glds16s(g2 + (size_t)(sr * 64 + 32) * 3584, zv, dst + 32768 + 4096); }
        };
        if (TYPE == 3 && j == 0) glds16s(gate_w, l * 16, lds0 + 3 * K1_ST);
        issue(0); issue(1); issue(2);
        wait_bar<2 * P>();
        if (j == 3) {
            unsigned clo, chi;
            asm volatile("s_getpc_b64 s[70:71]\n\ts_add_u32 s70, s70, k_main@rel32@lo+4\n\ts_addc_u32 s71, s71, k_main@rel32@hi+12\n\ts_mov_b32 %0, s70\n\ts_mov_b32 %1, s71"
                         : "=s"(clo), "=s"(chi) :: "s70", "s71", "scc");
            const char* cm = (const char*)(((unsigned long long)chi << 32) | clo);
#pragma unroll
            for (int i = 0; i < 2; ++i) prefetch_line(cm, (unsigned)(i * 64 + l) * 128, lds0 + 3 * K1_ST + 1024);
        }
#pragma unroll 1
        for (int s = 0; s < 7; ++s) {
            if (j == 0) TL(0, 0, 3 * s);
            if (s + 2 < 8) asm volatile("s_waitcnt vmcnt(%0)" :: "i"(P) : "memory"); else asm volatile("s_waitcnt vmcnt(0)" ::: "memory");
            if (j == 0) TL(0, 0, 3 * s + 1);
            bar_only();
            if (j == 0) TL(0, 0, 3 * s + 2);
            if (s + 3 < 8) issue(s + 3);
        }
        return;
    }
    const int rh = w & 1, ch = w >> 1, h = l >> 5, r32 = l & 31;
    int xo[4];
    { const int row = rh * 64 + r32, f = (row >> 1) & 7;
#pragma unroll
      for (int c = 0; c < 4; ++c) xo[c] = row * 128 + (((2 * c + h) ^ f) << 4); }
    const int q4 = (l & 15) >> 2, p4 = l & 3, b16 = (l >> 4) & 1;
    const int wl = 16384 + (8 * h + q4) * 128 + (((ch * 4 + b16 * 2 + (p4 >> 1)) ^ ((q4 >> 1) << 2)) << 4) + (p4 & 1) * 8;
    f32x16 acc[NA][2];
#pragma unroll
    for (int g = 0; g < NA; ++g) { acc[g][0] = f32x16{}; acc[g][1] = f32x16{}; }
    bf16x8 xa[2][2], wa[NG][2], xb[2][2], wb[NG][2], ga[2], gb[2];
    auto rdh = [&](int s, int half, bf16x8 (&xf)[2][2], bf16x8 (&wf)[NG][2], bf16x8 (&gf)[2]) {
        lds_cptr base = (lds_cptr)lds + (s % 3) * K1_ST;
#pragma unroll
        for (int kk = 0; kk < 2; ++kk) {
            const int ks = 2 * half + kk;
            xf[0][kk] = ldsfrag(base + xo[ks]); xf[1][kk] = ldsfrag(base + xo[ks] + 4096);
#pragma unroll
            for (int g = 0; g < NG; ++g) wf[g][kk] = trfrag(base + wl + g * 8192 + ks * 2048, base + wl + g * 8192 + ks * 2048 + 512);
            if (TYPE == 3) gf[kk] = ldsfrag((lds_cptr)lds + 3 * K1_ST + (((s + rot) & 7) * 4 + ks) * 32 + h * 16);
        }
    };
    auto mm = [&](bf16x8 (&xf)[2][2], bf16x8 (&wf)[NG][2], bf16x8 (&gf)[2]) {
#pragma unroll
        for (int kk = 0; kk < 2; ++kk)
#pragma unroll
            for (int rb = 0; rb < 2; ++rb) {
                if (TYPE == 0) {
#pragma unroll
                    for (int g = 0; g < 3; ++g) acc[g % NA][rb] = MFMA(wf[g % NG][kk], xf[rb][kk], acc[g % NA][rb]);
                } else {
                    acc[0][rb] = MFMA(xf[rb][kk], wf[0][kk], acc[0][rb]);
                    if (TYPE == 1) acc[NA - 1][rb] = MFMA(wf[NG - 1][kk], xf[rb][kk], acc[NA - 1][rb]);
                    if (TYPE == 3) acc[NA - 1][rb] = MFMA(gf[kk], xf[rb][kk], acc[NA - 1][rb]);
                }
            }
    };
    if (w == 0) TL(0, 1, 100);
    bar_only();
    if (w == 0) TL(0, 1, 101);
    rdh(0, 0, xa, wa, ga);
#pragma unroll 1
    for (int s = 0; s < 7; ++s) {
        rdh(s, 1, xb, wb, gb); mm(xa, wa, ga); interleave<4 * NA, 2>(); SB();
        if (w == 0) TL(0, 1, 2 * s);
        bar_only();
        if (w == 0) TL(0, 1, 2 * s + 1);
        rdh(s + 1, 0, xa, wa, ga); mm(xb, wb, gb); interleave<4 * NA, 2>(); SB();
    }
    rdh(7, 1, xb, wb, gb); mm(xa, wa, ga); interleave<4 * NA, 2>(); SB();
    mm(xb, wb, gb);
    STAMP(0, 1);
    if (w == 0) TL(0, 1, 102);
    char* kv = ws + WS_KV;
#pragma unroll
    for (int rb = 0; rb < 2; ++rb) {
        const int ttile = mt * 4 + rh * 2 + rb;
        if (TYPE == 0) {
            const int hd0 = 4 * p + 2 * ch;
            char* qf = ws + WS_QF;
            const f32x16 &q = acc[0][rb], &k = acc[1 % NA][rb];
            *(u32x4*)(qf + ((size_t)(hd0 * 64 + ttile) * 1024) + l * 16) = pack8(q, 0, QSCALE);
            *(u32x4*)(qf + ((size_t)((hd0 + 1) * 64 + ttile) * 1024) + l * 16) = pack8(q, 8, QSCALE);
            *(u32x4*)(kv + ((size_t)(hd0 * 64 + ttile) * 2048) + l * 16) = pack8(k, 0, 1.0f);
            *(u32x4*)(kv + ((size_t)((hd0 + 1) * 64 + ttile) * 2048) + l * 16) = pack8(k, 8, 1.0f);
            float n0 = 0.f, n1 = 0.f;
#pragma unroll
            for (int r = 0; r < 8; ++r) { n0 = __builtin_fmaf(k[r], k[r], n0); n1 = __builtin_fmaf(k[8 + r], k[8 + r], n1); }
            n0 = wave_max_fast(swap32_sum(n0)); n1 = wave_max_fast(swap32_sum(n1));
            if (l == 0) { float* km = (float*)(ws + WS_KMAX) + ttile * 32 + hd0; km[0] = n0 * 1.01f; km[1] = n1 * 1.01f; }
        } else {
            const int hd = 4 * p + 2 * ch + (r32 >> 4), d = r32 & 15;
            char* vp = kv + ((size_t)(hd * 64 + ttile) * 2048) + 1024 + h * 256 + d * 16;
            *(u32x4*)(vp) = pack8(acc[0][rb], 0, 1.0f); *(u32x4*)(vp + 512) = pack8(acc[0][rb], 8, 1.0f);
            if (TYPE == 3 && ch == 0 && h == 0)
                ((float*)(ws + WS_GATE))[ttile * 32 + r32] = __builtin_amdgcn_rcpf(1.0f + __builtin_amdgcn_exp2f(-1.4426950408889634f * acc[NA - 1][rb][0]));
        }
        if (TYPE <= 1) {
            f32x16& z = acc[NA - 1][rb];
            const int zg = TYPE == 0 ? p : 8 + p;
            float s1 = 0.f, s2 = 0.f;
#pragma unroll
            for (int r = 0; r < 16; ++r) { const float v = gelu_tanh(z[r]); z[r] = v; s1 += v; s2 = __builtin_fmaf(v, v, s2); }
            const int fb = 2 * zg + ch;
            char* zp = ws + WS_Z2 + ((size_t)(ttile * 56 + 2 * fb) * 1024);
            *(u32x4*)(zp + l * 16) = pack8(z, 0, 1.0f); *(u32x4*)(zp + 1024 + l * 16) = pack8(z, 8, 1.0f);
            s1 = swap32_sum(s1); s2 = swap32_sum(s2);
            if (h == 0) ((f32x2*)(ws + WS_STATS))[(size_t)(ttile * 32 + r32) * 28 + fb] = f32x2{s1, s2};
        }
    }
    if (w == 0) TL(0, 1, 103);
}

__global__ __launch_bounds__(512) void k1_proj(const u16* __restrict__ x, const u16* __restrict__ wqkv, const u16* __restrict__ w_in, const u16* __restrict__ gate_w,
                                               const u16* __restrict__ w_sp, const u16* __restrict__ wo, const u16* __restrict__ w_out, char* __restrict__ ws) {
    __shared__ __attribute__((aligned(1024))) char lds[K1_LDS];
    const int b = blockIdx.x, xcd = b & 7, idx = b >> 3;
    const int mt = (xcd & 1) * 8 + (idx & 7), nt = (xcd >> 1) * 4 + (idx >> 3), p = nt >> 1;
    STAMP(0, 0);
    if (b == 255 && threadIdx.x < SY_N) *(unsigned*)(ws + WS_SYNC + threadIdx.x * 256) = 0u;
    if (!(nt & 1)) k1_body<0>(x, wqkv, w_in, gate_w, w_sp, wo, w_out, ws, lds, mt, p, threadIdx.x);
    else if (p < 6) k1_body<1>(x, wqkv, w_in, gate_w, w_sp, wo, w_out, ws, lds, mt, p, threadIdx.x);
    else if (p == 6) k1_body<2>(x, wqkv, w_in, gate_w, w_sp, wo, w_out, ws, lds, mt, p, threadIdx.x);
    else k1_body<3>(x, wqkv, w_in, gate_w, w_sp, wo, w_out, ws, lds, mt, p, threadIdx.x);
    STAMP(0, 2);
}

DI void ln_apply(char* __restrict__ ws, char* lds, int unit, int tid) {
    const int l = tid & 63, w = tid >> 6, h = l >> 5, r32 = l & 31;
    if (w >= 7) return;
    const int ttile = unit >> 2, fb = (unit & 3) * 7 + w;
    const u32x4 v0 = *(const u32x4*)(ws + WS_Z2 + ((size_t)(ttile * 56 + 2 * fb) * 1024) + l * 16);
    const u32x4 v1 = *(const u32x4*)(ws + WS_Z2 + ((size_t)(ttile * 56 + 2 * fb + 1) * 1024) + l * 16);
    const f32x4* st = (const f32x4*)(ws + WS_STATS) + (size_t)(ttile * 32 + r32) * 14 + h * 7;
    float s1 = 0.f, s2 = 0.f;
#pragma unroll
    for (int i = 0; i < 7; ++i) { const f32x4 v = st[i]; s1 += v[0] + v[2]; s2 += v[1] + v[3]; }
    s1 = swap32_sum(s1); s2 = swap32_sum(s2);
    const float mean = s1 * (1.0f / FH);
    const float var = fmaxf(s2 * (1.0f / FH) - mean * mean, 0.f);
    const float rstd = __builtin_amdgcn_rsqf(var + 1e-5f), nm = -mean * rstd;
    lds_ptr tile = (lds_ptr)lds + 49152 + w * 2048;
    const int q4 = (l & 15) >> 2, p4 = l & 3, b16 = (l >> 4) & 1;
#pragma unroll
    for (int g = 0; g < 2; ++g) {
        const u32x4 v = g ? v1 : v0;
        unsigned o[4];
#pragma unroll
        for (int e = 0; e < 4; ++e) o[e] = cvtpk(__builtin_fmaf(bf_lo(v[e]), rstd, nm), __builtin_fmaf(bf_hi(v[e]), rstd, nm));
        auto r0 = __builtin_amdgcn_permlane32_swap(o[0], o[2], false, false);
        auto r1 = __builtin_amdgcn_permlane32_swap(o[1], o[3], false, false);
        const u32x4 ov = {r0[0], r1[0], r0[1], r1[1]};
        *(LDS_AS u32x4*)(tile + r32 * 64 + g * 32 + h * 16) = ov;
    }
    asm volatile("" ::: "memory");
#pragma unroll
    for (int kk = 0; kk < 2; ++kk) {
        lds_cptr rp = (lds_cptr)tile + (16 * kk + 8 * h + q4) * 64 + b16 * 32 + p4 * 8;
        st16_wt(ws, WS_Z2N + ((size_t)(fb * 128 + ttile * 2 + kk) * 1024) + l * 16, __builtin_bit_cast(u32x4, trfrag(rp, rp + 256)));
    }
}

constexpr int AT_TS = 2304, AT_SLOT = 8 * AT_TS, AT_LDS = 4 * AT_SLOT + 64;
#define ATTN_ASM \
    "s_mov_b32 %[sv], m0\n\t" \
    "v_mov_b32 v88, 0\n\t" \
    "v_mov_b32 v89, 0\n\t" \
    "v_mov_b32 v90, 0\n\t" \
    "v_mov_b32 v91, 0\n\t" \
    "v_mov_b32 v96, 0\n\t" \
    "v_mov_b32 v97, 0\n\t" \
    "v_mov_b32 v98, 0\n\t" \
    "v_mov_b32 v99, 0\n\t" \
    "v_mov_b32 v0, 0\n\t" \
    "v_mov_b32 v1, 0\n\t" \
    "v_mov_b32 v2, 0\n\t" \
    "v_mov_b32 v3, 0\n\t" \
    "v_mov_b32 v4, 0\n\t" \
    "v_mov_b32 v5, 0\n\t" \
    "v_mov_b32 v6, 0\n\t" \
    "v_mov_b32 v7, 0\n\t" \
    "v_mov_b32 v8, 0\n\t" \
    "v_mov_b32 v9, 0\n\t" \
    "v_mov_b32 v10, 0\n\t" \
    "v_mov_b32 v11, 0\n\t" \
    "v_mov_b32 v12, 0\n\t" \
    "v_mov_b32 v13, 0\n\t" \
    "v_mov_b32 v14, 0\n\t" \
    "v_mov_b32 v15, 0\n\t" \
    "s_mov_b32 %[cnt], 0\n\t" \
    "s_waitcnt vmcnt(4) lgkmcnt(0)\n\t" \
    "s_barrier\n\t" \
    "ds_read_b128 v[68:71], %[kl] offset:0\n\t" \
    "ds_read_b128 v[72:75], %[kl] offset:2304\n\t" \
    "ds_read_b128 v[76:79], %[v0l] offset:0\n\t" \
    "ds_read_b128 v[80:83], %[v1l] offset:0\n\t" \
    "s_waitcnt lgkmcnt(0)\n\t" \
    "v_mfma_f32_32x32x16_bf16 v[32:47], v[68:71], v[64:67], v[16:31]\n\t" \
    "s_nop 7\n\t" \
    "s_nop 3\n\t" \
    "Lloop%=:\n\t" \
    "s_setprio 3\n\t" \
    "v_mfma_f32_32x32x16_bf16 v[48:63], v[72:75], v[64:67], v[16:31]\n\t" \
    "v_exp_f32 v32, v32\n\t" \
    "ds_read_b128 v[68:71], %[kl] offset:4608\n\t" \
    "v_exp_f32 v33, v33\n\t" \
    "v_exp_f32 v34, v34\n\t" \
    "v_exp_f32 v35, v35\n\t" \
    "v_exp_f32 v36, v36\n\t" \
    "v_exp_f32 v37, v37\n\t" \
    "v_exp_f32 v38, v38\n\t" \
    "v_mfma_f32_32x32x16_bf16 v[0:15], v[88:91], v[96:99], v[0:15]\n\t" \
    "v_exp_f32 v39, v39\n\t" \
    "ds_read_b128 v[84:87], %[v0l] offset:2304\n\t" \
    "v_cvt_pk_bf16_f32 v92, v32, v33\n\t" \
    "v_cvt_pk_bf16_f32 v93, v34, v35\n\t" \
    "v_cvt_pk_bf16_f32 v94, v36, v37\n\t" \
    "v_cvt_pk_bf16_f32 v95, v38, v39\n\t" \
    "v_exp_f32 v40, v40\n\t" \
    "ds_read_b128 v[88:91], %[v1l] offset:2304\n\t" \
    "v_exp_f32 v41, v41\n\t" \
    "v_exp_f32 v42, v42\n\t" \
    "v_exp_f32 v43, v43\n\t" \
    "s_waitcnt lgkmcnt(4)\n\t" \
    "v_mfma_f32_32x32x16_bf16 v[0:15], v[76:79], v[92:95], v[0:15]\n\t" \
    "v_exp_f32 v44, v44\n\t" \
    "v_exp_f32 v45, v45\n\t" \
    "v_exp_f32 v46, v46\n\t" \
    "v_exp_f32 v47, v47\n\t" \
    "s_waitcnt lgkmcnt(2)\n\t" \
    "v_cvt_pk_bf16_f32 v96, v40, v41\n\t" \
    "v_cvt_pk_bf16_f32 v97, v42, v43\n\t" \
    "v_cvt_pk_bf16_f32 v98, v44, v45\n\t" \
    "v_cvt_pk_bf16_f32 v99, v46, v47\n\t" \
    "s_setprio 2\n\t" \
    "v_mfma_f32_32x32x16_bf16 v[32:47], v[68:71], v[64:67], v[16:31]\n\t" \
    "v_exp_f32 v48, v48\n\t" \
    "ds_read_b128 v[72:75], %[kl] offset:6912\n\t" \
    "v_exp_f32 v49, v49\n\t" \
    "v_exp_f32 v50, v50\n\t" \
    "v_exp_f32 v51, v51\n\t" \
    "v_exp_f32 v52, v52\n\t" \
    "v_exp_f32 v53, v53\n\t" \
    "v_exp_f32 v54, v54\n\t" \
    "v_mfma_f32_32x32x16_bf16 v[0:15], v[80:83], v[96:99], v[0:15]\n\t" \
    "v_exp_f32 v55, v55\n\t" \
    "ds_read_b128 v[76:79], %[v0l] offset:4608\n\t" \
    "v_cvt_pk_bf16_f32 v92, v48, v49\n\t" \
    "v_cvt_pk_bf16_f32 v93, v50, v51\n\t" \
    "v_cvt_pk_bf16_f32 v94, v52, v53\n\t" \
    "v_cvt_pk_bf16_f32 v95, v54, v55\n\t" \
    "v_exp_f32 v56, v56\n\t" \
    "ds_read_b128 v[80:83], %[v1l] offset:4608\n\t" \
    "v_exp_f32 v57, v57\n\t" \
    "v_exp_f32 v58, v58\n\t" \
    "v_exp_f32 v59, v59\n\t" \
    "s_waitcnt lgkmcnt(4)\n\t" \
    "v_mfma_f32_32x32x16_bf16 v[0:15], v[84:87], v[92:95], v[0:15]\n\t" \
    "v_exp_f32 v60, v60\n\t" \
    "v_exp_f32 v61, v61\n\t" \
    "v_exp_f32 v62, v62\n\t" \
    "v_exp_f32 v63, v63\n\t" \
    "s_waitcnt lgkmcnt(2)\n\t" \
    "v_cvt_pk_bf16_f32 v96, v56, v57\n\t" \
    "v_cvt_pk_bf16_f32 v97, v58, v59\n\t" \
    "v_cvt_pk_bf16_f32 v98, v60, v61\n\t" \
    "v_cvt_pk_bf16_f32 v99, v62, v63\n\t" \
    "s_setprio 1\n\t" \
    "v_mfma_f32_32x32x16_bf16 v[48:63], v[72:75], v[64:67], v[16:31]\n\t" \
    "v_exp_f32 v32, v32\n\t" \
    "ds_read_b128 v[68:71], %[kl] offset:9216\n\t" \
    "v_exp_f32 v33, v33\n\t" \
    "v_exp_f32 v34, v34\n\t" \
    "v_exp_f32 v35, v35\n\t" \
    "v_exp_f32 v36, v36\n\t" \
    "v_exp_f32 v37, v37\n\t" \
    "v_exp_f32 v38, v38\n\t" \
    "v_mfma_f32_32x32x16_bf16 v[0:15], v[88:91], v[96:99], v[0:15]\n\t" \
    "v_exp_f32 v39, v39\n\t" \
    "ds_read_b128 v[84:87], %[v0l] offset:6912\n\t" \
    "v_cvt_pk_bf16_f32 v92, v32, v33\n\t" \
    "v_cvt_pk_bf16_f32 v93, v34, v35\n\t" \
    "v_cvt_pk_bf16_f32 v94, v36, v37\n\t" \
    "v_cvt_pk_bf16_f32 v95, v38, v39\n\t" \
    "v_exp_f32 v40, v40\n\t" \
    "ds_read_b128 v[88:91], %[v1l] offset:6912\n\t" \
    "v_exp_f32 v41, v41\n\t" \
    "v_exp_f32 v42, v42\n\t" \
    "v_exp_f32 v43, v43\n\t" \
    "s_waitcnt lgkmcnt(4)\n\t" \
    "v_mfma_f32_32x32x16_bf16 v[0:15], v[76:79], v[92:95], v[0:15]\n\t" \
    "v_exp_f32 v44, v44\n\t" \
    "v_exp_f32 v45, v45\n\t" \
    "v_exp_f32 v46, v46\n\t" \
    "v_exp_f32 v47, v47\n\t" \
    "s_waitcnt lgkmcnt(2)\n\t" \
    "v_cvt_pk_bf16_f32 v96, v40, v41\n\t" \
    "v_cvt_pk_bf16_f32 v97, v42, v43\n\t" \
    "v_cvt_pk_bf16_f32 v98, v44, v45\n\t" \
    "v_cvt_pk_bf16_f32 v99, v46, v47\n\t" \
    "s_setprio 0\n\t" \
    "v_mfma_f32_32x32x16_bf16 v[32:47], v[68:71], v[64:67], v[16:31]\n\t" \
    "v_exp_f32 v48, v48\n\t" \
    "ds_read_b128 v[72:75], %[kl] offset:11520\n\t" \
    "v_exp_f32 v49, v49\n\t" \
    "v_exp_f32 v50, v50\n\t" \
    "v_exp_f32 v51, v51\n\t" \
    "v_exp_f32 v52, v52\n\t" \
    "v_exp_f32 v53, v53\n\t" \
    "v_exp_f32 v54, v54\n\t" \
    "v_mfma_f32_32x32x16_bf16 v[0:15], v[80:83], v[96:99], v[0:15]\n\t" \
    "v_exp_f32 v55, v55\n\t" \
    "ds_read_b128 v[76:79], %[v0l] offset:9216\n\t" \
    "v_cvt_pk_bf16_f32 v92, v48, v49\n\t" \
    "v_cvt_pk_bf16_f32 v93, v50, v51\n\t" \
    "v_cvt_pk_bf16_f32 v94, v52, v53\n\t" \
    "v_cvt_pk_bf16_f32 v95, v54, v55\n\t" \
    "v_exp_f32 v56, v56\n\t" \
    "ds_read_b128 v[80:83], %[v1l] offset:9216\n\t" \
    "v_exp_f32 v57, v57\n\t" \
    "v_exp_f32 v58, v58\n\t" \
    "v_exp_f32 v59, v59\n\t" \
    "s_waitcnt lgkmcnt(4)\n\t" \
    "v_mfma_f32_32x32x16_bf16 v[0:15], v[84:87], v[92:95], v[0:15]\n\t" \
    "v_exp_f32 v60, v60\n\t" \
    "v_exp_f32 v61, v61\n\t" \
    "v_exp_f32 v62, v62\n\t" \
    "v_exp_f32 v63, v63\n\t" \
    "s_waitcnt lgkmcnt(2)\n\t" \
    "v_cvt_pk_bf16_f32 v96, v56, v57\n\t" \
    "v_cvt_pk_bf16_f32 v97, v58, v59\n\t" \
    "v_cvt_pk_bf16_f32 v98, v60, v61\n\t" \
    "v_cvt_pk_bf16_f32 v99, v62, v63\n\t" \
    "s_setprio 3\n\t" \
    "v_mfma_f32_32x32x16_bf16 v[48:63], v[72:75], v[64:67], v[16:31]\n\t" \
    "v_exp_f32 v32, v32\n\t" \
    "ds_read_b128 v[68:71], %[kl] offset:13824\n\t" \
    "v_exp_f32 v33, v33\n\t" \
    "v_exp_f32 v34, v34\n\t" \
    "v_exp_f32 v35, v35\n\t" \
    "v_exp_f32 v36, v36\n\t" \
    "v_exp_f32 v37, v37\n\t" \
    "v_exp_f32 v38, v38\n\t" \
    "v_mfma_f32_32x32x16_bf16 v[0:15], v[88:91], v[96:99], v[0:15]\n\t" \
    "v_exp_f32 v39, v39\n\t" \
    "ds_read_b128 v[84:87], %[v0l] offset:11520\n\t" \
    "v_cvt_pk_bf16_f32 v92, v32, v33\n\t" \
    "v_cvt_pk_bf16_f32 v93, v34, v35\n\t" \
    "v_cvt_pk_bf16_f32 v94, v36, v37\n\t" \
    "v_cvt_pk_bf16_f32 v95, v38, v39\n\t" \
    "v_exp_f32 v40, v40\n\t" \
    "ds_read_b128 v[88:91], %[v1l] offset:11520\n\t" \
    "v_exp_f32 v41, v41\n\t" \
    "v_exp_f32 v42, v42\n\t" \
    "v_exp_f32 v43, v43\n\t" \
    "s_waitcnt lgkmcnt(4)\n\t" \
    "v_mfma_f32_32x32x16_bf16 v[0:15], v[76:79], v[92:95], v[0:15]\n\t" \
    "v_exp_f32 v44, v44\n\t" \
    "v_exp_f32 v45, v45\n\t" \
    "v_exp_f32 v46, v46\n\t" \
    "v_exp_f32 v47, v47\n\t" \
    "s_waitcnt lgkmcnt(2)\n\t" \
    "v_cvt_pk_bf16_f32 v96, v40, v41\n\t" \
    "v_cvt_pk_bf16_f32 v97, v42, v43\n\t" \
    "v_cvt_pk_bf16_f32 v98, v44, v45\n\t" \
    "v_cvt_pk_bf16_f32 v99, v46, v47\n\t" \
    "s_setprio 2\n\t" \
    "v_mfma_f32_32x32x16_bf16 v[32:47], v[68:71], v[64:67], v[16:31]\n\t" \
    "v_exp_f32 v48, v48\n\t" \
    "ds_read_b128 v[72:75], %[kl] offset:16128\n\t" \
    "s_and_b32 %[t0], %[cnt], 3\n\t" \
    "s_cmp_eq_u32 %[t0], 3\n\t" \
    "s_mov_b32 %[dl], 18432\n\t" \
    "s_cselect_b32 %[dl], -55296, %[dl]\n\t" \
    "v_add_u32 %[kl], %[dl], %[kl]\n\t" \
    "v_exp_f32 v49, v49\n\t" \
    "v_exp_f32 v50, v50\n\t" \
    "v_exp_f32 v51, v51\n\t" \
    "v_exp_f32 v52, v52\n\t" \
    "v_exp_f32 v53, v53\n\t" \
    "v_exp_f32 v54, v54\n\t" \
    "v_mfma_f32_32x32x16_bf16 v[0:15], v[80:83], v[96:99], v[0:15]\n\t" \
    "v_exp_f32 v55, v55\n\t" \
    "ds_read_b128 v[76:79], %[v0l] offset:13824\n\t" \
    "v_cvt_pk_bf16_f32 v92, v48, v49\n\t" \
    "v_cvt_pk_bf16_f32 v93, v50, v51\n\t" \
    "v_cvt_pk_bf16_f32 v94, v52, v53\n\t" \
    "v_cvt_pk_bf16_f32 v95, v54, v55\n\t" \
    "v_exp_f32 v56, v56\n\t" \
    "ds_read_b128 v[80:83], %[v1l] offset:13824\n\t" \
    "v_exp_f32 v57, v57\n\t" \
    "v_exp_f32 v58, v58\n\t" \
    "v_exp_f32 v59, v59\n\t" \
    "s_waitcnt lgkmcnt(4)\n\t" \
    "v_mfma_f32_32x32x16_bf16 v[0:15], v[84:87], v[92:95], v[0:15]\n\t" \
    "v_exp_f32 v60, v60\n\t" \
    "v_exp_f32 v61, v61\n\t" \
    "v_exp_f32 v62, v62\n\t" \
    "v_exp_f32 v63, v63\n\t" \
    "s_waitcnt lgkmcnt(2)\n\t" \
    "v_cvt_pk_bf16_f32 v96, v56, v57\n\t" \
    "v_cvt_pk_bf16_f32 v97, v58, v59\n\t" \
    "v_cvt_pk_bf16_f32 v98, v60, v61\n\t" \
    "v_cvt_pk_bf16_f32 v99, v62, v63\n\t" \
    "s_cmp_eq_u32 %[cnt], 7\n\t" \
    "s_cbranch_scc1 Lskip%=\n\t" \
    "s_cmp_eq_u32 %[cnt], 6\n\t" \
    "s_cbranch_scc1 Lw0%=\n\t" \
    "s_waitcnt vmcnt(2)\n\t" \
    "s_barrier\n\t" \
    "s_cmp_gt_u32 %[cnt], 4\n\t" \
    "s_cbranch_scc1 Lskip%=\n\t" \
    "s_add_u32 %[t0], %[cnt], 3\n\t" \
    "s_and_b32 %[t0], %[t0], 3\n\t" \
    "s_mul_i32 %[t0], %[t0], 18432\n\t" \
    "s_add_u32 m0, %[t0], %[dst]\n\t" \
    "s_setprio 3\n\t" \
    "global_load_lds_dwordx4 %[goff], %[base]\n\t" \
    "global_load_lds_dwordx4 %[goff], %[base] offset:1024\n\t" \
    "s_setprio 2\n\t" \
    "v_add_u32 %[goff], 0x4000, %[goff]\n\t" \
    "s_branch Lskip%=\n\t" \
    "Lw0%=:\n\t" \
    "s_waitcnt vmcnt(0)\n\t" \
    "s_barrier\n\t" \
    "Lskip%=:\n\t" \
    "s_setprio 1\n\t" \
    "v_mfma_f32_32x32x16_bf16 v[48:63], v[72:75], v[64:67], v[16:31]\n\t" \
    "v_exp_f32 v32, v32\n\t" \
    "ds_read_b128 v[68:71], %[kl] offset:0\n\t" \
    "v_exp_f32 v33, v33\n\t" \
    "v_exp_f32 v34, v34\n\t" \
    "v_exp_f32 v35, v35\n\t" \
    "v_exp_f32 v36, v36\n\t" \
    "v_exp_f32 v37, v37\n\t" \
    "v_exp_f32 v38, v38\n\t" \
    "v_mfma_f32_32x32x16_bf16 v[0:15], v[88:91], v[96:99], v[0:15]\n\t" \
    "v_exp_f32 v39, v39\n\t" \
    "ds_read_b128 v[84:87], %[v0l] offset:16128\n\t" \
    "v_cvt_pk_bf16_f32 v92, v32, v33\n\t" \
    "v_cvt_pk_bf16_f32 v93, v34, v35\n\t" \
    "v_cvt_pk_bf16_f32 v94, v36, v37\n\t" \
    "v_cvt_pk_bf16_f32 v95, v38, v39\n\t" \
    "v_exp_f32 v40, v40\n\t" \
    "ds_read_b128 v[88:91], %[v1l] offset:16128\n\t" \
    "v_add_u32 %[v0l], %[dl], %[v0l]\n\t" \
    "v_add_u32 %[v1l], %[dl], %[v1l]\n\t" \
    "v_exp_f32 v41, v41\n\t" \
    "v_exp_f32 v42, v42\n\t" \
    "v_exp_f32 v43, v43\n\t" \
    "s_waitcnt lgkmcnt(4)\n\t" \
    "v_mfma_f32_32x32x16_bf16 v[0:15], v[76:79], v[92:95], v[0:15]\n\t" \
    "v_exp_f32 v44, v44\n\t" \
    "v_exp_f32 v45, v45\n\t" \
    "v_exp_f32 v46, v46\n\t" \
    "v_exp_f32 v47, v47\n\t" \
    "s_waitcnt lgkmcnt(2)\n\t" \
    "v_cvt_pk_bf16_f32 v96, v40, v41\n\t" \
    "v_cvt_pk_bf16_f32 v97, v42, v43\n\t" \
    "v_cvt_pk_bf16_f32 v98, v44, v45\n\t" \
    "v_cvt_pk_bf16_f32 v99, v46, v47\n\t" \
    "s_setprio 0\n\t" \
    "v_mfma_f32_32x32x16_bf16 v[32:47], v[68:71], v[64:67], v[16:31]\n\t" \
    "v_exp_f32 v48, v48\n\t" \
    "ds_read_b128 v[72:75], %[kl] offset:2304\n\t" \
    "v_exp_f32 v49, v49\n\t" \
    "v_exp_f32 v50, v50\n\t" \
    "v_exp_f32 v51, v51\n\t" \
    "v_exp_f32 v52, v52\n\t" \
    "v_exp_f32 v53, v53\n\t" \
    "v_exp_f32 v54, v54\n\t" \
    "v_mfma_f32_32x32x16_bf16 v[0:15], v[80:83], v[96:99], v[0:15]\n\t" \
    "v_exp_f32 v55, v55\n\t" \
    "ds_read_b128 v[76:79], %[v0l] offset:0\n\t" \
    "v_cvt_pk_bf16_f32 v92, v48, v49\n\t" \
    "v_cvt_pk_bf16_f32 v93, v50, v51\n\t" \
    "v_cvt_pk_bf16_f32 v94, v52, v53\n\t" \
    "v_cvt_pk_bf16_f32 v95, v54, v55\n\t" \
    "v_exp_f32 v56, v56\n\t" \
    "ds_read_b128 v[80:83], %[v1l] offset:0\n\t" \
    "v_exp_f32 v57, v57\n\t" \
    "v_exp_f32 v58, v58\n\t" \
    "v_exp_f32 v59, v59\n\t" \
    "s_waitcnt lgkmcnt(4)\n\t" \
    "v_mfma_f32_32x32x16_bf16 v[0:15], v[84:87], v[92:95], v[0:15]\n\t" \
    "v_exp_f32 v60, v60\n\t" \
    "v_exp_f32 v61, v61\n\t" \
    "v_exp_f32 v62, v62\n\t" \
    "v_exp_f32 v63, v63\n\t" \
    "s_waitcnt lgkmcnt(2)\n\t" \
    "v_cvt_pk_bf16_f32 v96, v56, v57\n\t" \
    "v_cvt_pk_bf16_f32 v97, v58, v59\n\t" \
    "v_cvt_pk_bf16_f32 v98, v60, v61\n\t" \
    "v_cvt_pk_bf16_f32 v99, v62, v63\n\t" \
    "s_add_u32 %[cnt], %[cnt], 1\n\t" \
    "s_cmp_lt_u32 %[cnt], 8\n\t" \
    "s_cbranch_scc1 Lloop%=\n\t" \
    "s_waitcnt lgkmcnt(0)\n\t" \
    "v_mfma_f32_32x32x16_bf16 v[0:15], v[88:91], v[96:99], v[0:15]\n\t" \
    "s_mov_b32 m0, %[sv]\n\t" \
    "s_setprio 0\n\t" \
    "s_nop 7\n\t" \
    "s_nop 7\n\t"
#define ATTN_CLOBBERS "v32", "v33", "v34", "v35", "v36", "v37", "v38", "v39", "v40", "v41", "v42", "v43", "v44", "v45", "v46", "v47", "v48", "v49", "v50", "v51", "v52", "v53", "v54", "v55", "v56", "v57", "v58", "v59", "v60", "v61", "v62", "v63", "v68", "v69", "v70", "v71", "v72", "v73", "v74", "v75", "v76", "v77", "v78", "v79", "v80", "v81", "v82", "v83", "v84", "v85", "v86", "v87", "v88", "v89", "v90", "v91", "v92", "v93", "v94", "v95", "v96", "v97", "v98", "v99"
#define ATTN_INSTR 0

DI void attn_block(char* __restrict__ ws, char* lds, int head, int chunk, int tid) {
    const int l = tid & 63, w = __builtin_amdgcn_readfirstlane(tid >> 6), h = l >> 5;
    const int qtile = chunk * 8 + w;
    const unsigned lds0 = (unsigned)(uintptr_t)lds;
    const char* kvbase = ws + WS_KV + (size_t)head * 64 * 2048;
    const unsigned dst = lds0 + w * AT_TS;
    bf16x8 qf; float km, gate;
    asm volatile("global_load_dwordx4 %0, %3, off\n\tglobal_load_dword %1, %4, off\n\tglobal_load_dword %2, %5, off"
                 : "=&v"(qf), "=&v"(km), "=&v"(gate)
                 : "v"(ws + WS_QF + ((size_t)(head * 64 + qtile) * 1024) + l * 16), "v"((const float*)(ws + WS_KMAX) + l * 32 + head),
                   "v"((const float*)(ws + WS_GATE) + qtile * 32 + (l & 31)) : "memory");
    auto issue3 = [&]() {
#pragma unroll
        for (int c = 0; c < 3; ++c) { glds16(kvbase + c * 16384 + w * 2048 + l * 16, dst + c * AT_SLOT); glds16(kvbase + c * 16384 + w * 2048 + 1024 + l * 16, dst + c * AT_SLOT + 1024); }
    };
    issue3();
    {
        const u32x4 ones = {0x3f803f80u, 0x3f803f80u, 0x3f803f80u, 0x3f803f80u};
        *(LDS_AS u32x4*)((lds_ptr)lds + (tid >> 4) * AT_TS + 2048 + (tid & 15) * 16) = ones;
        if (tid == 0) *(LDS_AS unsigned*)((lds_ptr)lds + 4 * AT_SLOT) = 0u;
    }
    asm volatile("s_waitcnt vmcnt(6)" : "+v"(qf), "+v"(km), "+v"(gate));
    float qn = 0.f;
    { const u32x4 qu = __builtin_bit_cast(u32x4, qf);
#pragma unroll
      for (int e = 0; e < 4; ++e) { qn = __builtin_fmaf(bf_lo(qu[e]), bf_lo(qu[e]), qn); qn = __builtin_fmaf(bf_hi(qu[e]), bf_hi(qu[e]), qn); } }
    qn = swap32_sum(qn);
    float ref = __builtin_amdgcn_sqrtf(qn * wave_max_fast(km));
    unsigned kl = lds0 + l * 16;
    const bool onesl = (l & 16) != 0;
    const unsigned ol = lds0 + 2048 + ((((l & 15) >= 4) && ((l & 15) < 12)) ? 64 : 0);
    unsigned v0l = onesl ? ol : lds0 + 1024 + h * 256 + (l & 15) * 16, v1l = onesl ? ol : v0l + 512;
    LDS_AS volatile unsigned* flag = (LDS_AS volatile unsigned*)((lds_ptr)lds + 4 * AT_SLOT);
    const size_t ooff = WS_HO + ((size_t)(qtile * 88 + head) * 1024) + l * 16;
#pragma unroll 1
    for (int pass = 0;; ++pass) {
        f32x16 negm, o;
#pragma unroll
        for (int r = 0; r < 16; ++r) negm[r] = -ref;
        unsigned sv, cnt, t0, dl, goff = 3 * 16384 + w * 2048 + l * 16;
        STAMP(2, 1);
        asm volatile(ATTN_ASM
                     : "=&{v[0:15]}"(o), [sv] "=&s"(sv), [cnt] "=&s"(cnt), [t0] "=&s"(t0), [dl] "=&s"(dl), [goff] "+v"(goff), [kl] "+v"(kl), [v0l] "+v"(v0l), [v1l] "+v"(v1l)
                     : "{v[16:31]}"(negm), "{v[64:67]}"(qf), [dst] "s"(dst), [base] "s"(kvbase)
                     : ATTN_CLOBBERS, "scc", "memory");
        STAMP(2, 3);
        st16_wt(ws, ooff, pack8(o, 0, gate * __builtin_amdgcn_rcpf(o[8])));
        drain_stores();
        if (pass == 1) { __syncthreads(); break; }
        const bool bad = !(o[8] >= 9.094947e-13f);
        if (__any(bad) && l == 0) *flag = 1u;
        __syncthreads();
        if (*flag == 0u) break;
        float m = -3.0e38f;
        for (int t = 0; t < 64; ++t) {
            const bf16x8 kf = *(const bf16x8*)(kvbase + (size_t)t * 2048 + l * 16);
            const f32x16 s = MFMA(kf, qf, f32x16{});
#pragma unroll
            for (int r = 0; r < 16; ++r) m = fmaxf(m, s[r]);
        }
        ref = swap32_max(m);
        asm volatile("s_waitcnt vmcnt(0)" ::: "memory");
        issue3();
    }
    if (tid == 0) __hip_atomic_fetch_add(sync_word(ws, SY_O + chunk), 1u, RLX_AGENT);
}
constexpr int SP_ST = 24576, SP_LDS = 3 * SP_ST + 256, FN_ST = 16384;

template <int N> DI void wait_vm() { asm volatile("s_waitcnt vmcnt(%0)" :: "i"(N) : "memory"); }

DI void gate_block(const u16* __restrict__ x, const u16* __restrict__ w_in, const u16* __restrict__ w_sp, const u16* __restrict__ b_sp,
                   const u16* __restrict__ wo, const u16* __restrict__ w_out, char* __restrict__ ws, u16* __restrict__ out, char* lds, int g, int tid) {
    const int l = tid & 63, w = __builtin_amdgcn_readfirstlane(tid >> 6), h = l >> 5, r32 = l & 31;
    const int tl = (g >> 3) & 7, tt = (g & 3) * 8 + tl, ft = ((g >> 2) & 1) * 4 + (g >> 6);
    const int rot = SP_ROT(tl, g >> 6);
    const unsigned lds0 = (unsigned)(uintptr_t)lds;
    const int q4 = (l & 15) >> 2, p4 = l & 3, b16 = (l >> 4) & 1;
    const unsigned lo = l * 16;
    if (tid == 0) TL(5, 0, 99);
    if (GPRIO) __builtin_amdgcn_s_setprio(GPRIO);
    const int j = w - 4, lrow = 8 * j + (l >> 3);
    const unsigned xv = (unsigned)(lrow * D_ + ((l & 7) ^ ((lrow >> 1) & 7)) * 8) * 2;
    const int wr = 4 * j + (l >> 4), wc = ((((l & 15) >> 2) ^ (wr & 3)) << 2) | (l & 3);
    const unsigned wv = (unsigned)(wr * 1792 + wc * 8) * 2;
    auto issue_z = [&](int s) {
        const char* xu = (const char*)(x + (size_t)tt * 64 * D_); const char* wu = (const char*)(w_in + ft * 128);
        const unsigned dst = lds0 + (s % 3) * SP_ST + j * 1024; const int sr = (s + rot) & 7;
        glds16s(xu + sr * 128, xv, dst); glds16s(xu + 32 * D_ * 2 + sr * 128, xv, dst + 4096);
#pragma unroll
        for (int i = 0; i < 4; ++i) glds16s(wu + (size_t)(sr * 64 + 16 * i) * 3584, wv, dst + 8192 + i * 4096);
    };
    if (w >= 4 && ft < 7) { issue_z(0); issue_z(1); }
    ln_apply(ws, lds, g, tid);
    drain_stores();
    bar_only();
    if (tid == 0) __hip_atomic_fetch_add(sync_word(ws, SY_LN + (g & 15)), 1u, RLX_AGENT);
    STAMP(5, 1);
    if (tid == 0) TL(5, 0, 100);
    if (ft < 7) {
        if (w >= 4) {
            if (LPRIO != GPRIO) __builtin_amdgcn_s_setprio(LPRIO);
            issue_z(2);
            bar_only();
#pragma unroll 1
            for (int s = 0; s < 7; ++s) {
                if (s == 0) bar_only(); else if (s + 2 < 8) wait_bar<6>(); else wait_bar<0>();
                if (s + 3 < 8) issue_z(s + 3);
            }
            bar_only();
            if (j == 3) {
                wait_words(ws, SY_LN, 16, 16u, l);
                const char* zu = ws + WS_Z2N + (size_t)(ft * 4) * 128 * 1024;
                const unsigned pv = (unsigned)((tl >> 1) * 128 * 1024 + ((tl & 1) * 16 + (l & 15)) * 128);
                auto pf = [&](int s4) { const int sr = (4 * s4 + (l >> 4) + rot) & 31; prefetch_line(zu, pv + sr * 4096, lds0 + 3 * SP_ST); };
                pf(0); pf(1); pf(2);
                bar_only();
                bar_only();
#pragma unroll 1
                for (int s = 0; s < 31; ++s) { bar_only(); if ((s & 3) == 0 && s + 12 < 32) pf((s >> 2) + 3); }
                drain_stores();
            } else {
                const unsigned a0 = (unsigned)((l >> 3) * S_ * 2 + (((l & 7) ^ ((l >> 4) & 7)) << 4)), a1 = a0 ^ 64u;
                const unsigned va = (j & 1) ? a1 : a0, vb = (j & 1) ? a0 : a1;
                const char* au = (const char*)(w_sp + (size_t)(tt * 64 + 8 * j) * S_);
                auto issue = [&](int s) {
                    const unsigned dst = lds0 + (s % 6) * 8192 + j * 1024; const int sr = (s + rot) & 31;
                    glds16s(au + sr * 128, va, dst); glds16s(au + 24 * S_ * 2 + sr * 128, vb, dst + 3072);
                    if (j < 2) glds16s(au + 48 * S_ * 2 + sr * 128, va, dst + 6144);
                };
                auto waitst = [&](int younger) {
                    if (j < 2) { if (younger >= 4) wait_vm<12>(); else if (younger == 3) wait_vm<9>(); else if (younger == 2) wait_vm<6>(); else if (younger == 1) wait_vm<3>(); else wait_vm<0>(); }
                    else { if (younger >= 4) wait_vm<8>(); else if (younger == 3) wait_vm<6>(); else if (younger == 2) wait_vm<4>(); else if (younger == 1) wait_vm<2>(); else wait_vm<0>(); }
                };
#pragma unroll 1
                for (int s = 0; s < 5; ++s) issue(s);
                bar_only();
                waitst(4); bar_only();
                issue(5);
#pragma unroll 1
                for (int s = 0; s < 31; ++s) {
                    waitst(min(s + 5, 31) - (s + 1)); bar_only();
                    if (s + 6 < 32) issue(s + 6);
                }
            }
            bar_only();
        } else {
            const int fb = w;
            int boff[4];
#pragma unroll
            for (int ks = 0; ks < 4; ++ks) { const int f = (r32 >> 1) & 7; boff[ks] = r32 * 128 + (((ks * 2 + h) ^ f) << 4); }
            const int fbg = ft * 4 + fb, fg = 2 * fbg;
            const float gt0 = ((const float*)(ws + WS_GATE))[(tt * 2) * 32 + r32], gt1 = ((const float*)(ws + WS_GATE))[(tt * 2 + 1) * 32 + r32];
            const float bs0 = __uint_as_float((unsigned)b_sp[(tt * 2) * 32 + r32] << 16), bs1 = __uint_as_float((unsigned)b_sp[(tt * 2 + 1) * 32 + r32] << 16);
            LDS_AS u32x4* z1p = (LDS_AS u32x4*)((lds_ptr)lds + 49152 + w * 4096) + l;
            {
                f32x16 acc[2]; acc[0] = f32x16{}; acc[1] = f32x16{};
                const int zoff = 8192 + (8 * h + q4) * 256 + ((fb ^ q4) << 6) + b16 * 32 + p4 * 8;
                bf16x8 aa[2], ba[2][2], ab[2], bb[2][2];
                auto rdh = [&](int s, int half, bf16x8 (&af)[2], bf16x8 (&bf)[2][2]) {
                    lds_cptr base = (lds_cptr)lds + (s % 3) * SP_ST;
#pragma unroll
                    for (int kk = 0; kk < 2; ++kk) {
                        const int ks = 2 * half + kk;
                        af[kk] = trfrag(base + zoff + ks * 4096, base + zoff + ks * 4096 + 1024);
                        bf[0][kk] = ldsfrag(base + boff[ks]); bf[1][kk] = ldsfrag(base + boff[ks] + 4096);
                    }
                };
                auto mm = [&](bf16x8 (&af)[2], bf16x8 (&bf)[2][2]) {
#pragma unroll
                    for (int kk = 0; kk < 2; ++kk) { acc[0] = MFMA(af[kk], bf[0][kk], acc[0]); acc[1] = MFMA(af[kk], bf[1][kk], acc[1]); }
                };
                bar_only();
                rdh(0, 0, aa, ba);
#pragma unroll 1
                for (int s = 0; s < 7; ++s) {
                    rdh(s, 1, ab, bb); mm(aa, ba); interleave<4, 2>(); SB();
                    bar_only();
                    rdh(s + 1, 0, aa, ba); mm(ab, bb); interleave<4, 2>(); SB();
                }
                rdh(7, 1, ab, bb); mm(aa, ba); interleave<4, 2>(); SB();
                mm(ab, bb);
                bar_only();
                if (tid == 0) TL(5, 0, 101);
#pragma unroll
                for (int tb = 0; tb < 2; ++tb) {
                    const float og = 1.0f - (tb ? gt1 : gt0);
#pragma unroll
                    for (int gq = 0; gq < 2; ++gq) {
                        u32x4 zv;
#pragma unroll
                        for (int e = 0; e < 4; ++e) zv[e] = cvtpk(gelu_tanh(acc[tb][8 * gq + 2 * e]) * og, gelu_tanh(acc[tb][8 * gq + 2 * e + 1]) * og);
                        z1p[(tb * 2 + gq) * 64] = zv;
                    }
                }
            }
            f32x16 acc[2]; acc[0] = f32x16{}; acc[1] = f32x16{};
            {
                if (tid == 0) TL(5, 0, 102);
                bar_only();
                if (tid == 0) TL(5, 0, 103);
                bf16x8 za[4], zb[4];
                const size_t zoff = WS_Z2N + (size_t)fbg * 128 * 1024 + lo;
                auto ldz = [&](int s, int ks) { const int sr = (min(s, 31) + rot) & 31; return ld16_dev(ws, zoff + (size_t)((sr * 4 + ks) * 1024)); };
#pragma unroll
                for (int ks = 0; ks < 4; ++ks) za[ks] = ldz(0, ks);
#pragma unroll
                for (int ks = 0; ks < 4; ++ks) zb[ks] = ldz(1, ks);
                bf16x8 bx[2][2], by[2][2];
                auto rdh = [&](int s, int half, bf16x8 (&bf)[2][2]) {
                    lds_cptr base = (lds_cptr)lds + (s % 6) * 8192;
#pragma unroll
                    for (int kk = 0; kk < 2; ++kk) { bf[0][kk] = ldsfrag(base + boff[2 * half + kk]); bf[1][kk] = ldsfrag(base + boff[2 * half + kk] + 4096); }
                };
                auto stage = [&](int s, bf16x8 (&z)[4], bool last) {
                    rdh(s, 1, by);
                    acc[0] = MFMA(z[0], bx[0][0], acc[0]); acc[1] = MFMA(z[0], bx[1][0], acc[1]);
                    acc[0] = MFMA(z[1], bx[0][1], acc[0]); acc[1] = MFMA(z[1], bx[1][1], acc[1]); interleave<4, 1>(); SB();
                    z[0] = ldz(s + 2, 0); z[1] = ldz(s + 2, 1); SB();
                    if (!last) { bar_only(); rdh(s + 1, 0, bx); }
                    acc[0] = MFMA(z[2], by[0][0], acc[0]); acc[1] = MFMA(z[2], by[1][0], acc[1]);
                    acc[0] = MFMA(z[3], by[0][1], acc[0]); acc[1] = MFMA(z[3], by[1][1], acc[1]); interleave<4, 1>(); SB();
                    z[2] = ldz(s + 2, 2); z[3] = ldz(s + 2, 3); SB();
                };
                bar_only();
                rdh(0, 0, bx);
#pragma unroll 1
                for (int s = 0; s < 30; s += 2) { stage(s, za, false); stage(s + 1, zb, false); }
                stage(30, za, false); stage(31, zb, true);
                if (tid == 0) TL(5, 0, 104);
            }
#pragma unroll
            for (int tb = 0; tb < 2; ++tb) {
                const float bs = tb ? bs1 : bs0;
#pragma unroll
                for (int gq = 0; gq < 2; ++gq) {
                    u32x4 ov; const u32x4 zv = z1p[(tb * 2 + gq) * 64];
#pragma unroll
                    for (int e = 0; e < 4; ++e) ov[e] = cvtpk((acc[tb][8 * gq + 2 * e] + bs) * bf_lo(zv[e]), (acc[tb][8 * gq + 2 * e + 1] + bs) * bf_hi(zv[e]));
                    st16_wt(ws, WS_HO + ((size_t)((tt * 2 + tb) * 88 + 32 + fg + gq) * 1024) + lo, ov);
                }
            }
            drain_stores();
            bar_only();
            if (tid == 0) __hip_atomic_fetch_add(sync_word(ws, SY_H + tt), 1u, RLX_AGENT);
        }
    }
    STAMP(5, 3);
    if (tid == 0) TL(5, 0, 105);
    {
        if (GPRIO) __builtin_amdgcn_s_setprio(GPRIO);
        const int tb = w & 1, kq = w >> 1;
        const int prow = 8 * w + (l >> 3);
        const unsigned pv = (unsigned)(prow * D_ + ((l & 7) ^ (((prow >> 1) & 1) << 2)) * 8) * 2;
        const char* w0 = (const char*)(w_out + ft * 64);
        const char* w1 = (const char*)(wo + ft * 64);
        auto issue = [&](int s) {
            const unsigned dst = lds0 + (s & 3) * FN_ST + w * 1024;
            const char* wp = s < 7 ? w0 + (size_t)s * 128 * D_ * 2 : w1 + (size_t)(s - 7) * 128 * D_ * 2;
            glds16s(wp, pv, dst); glds16s(wp + (size_t)64 * D_ * 2, pv, dst + 8192);
        };
        issue(0); issue(1); issue(2); issue(3);
        const int woff0 = (kq * 32 + 4 * h + q4) * 128 + (((b16 * 2 + (p4 >> 1)) ^ ((q4 >> 1) << 2)) << 4) + (p4 & 1) * 8, woff1 = woff0 ^ 64;
        const size_t hoff = WS_HO + ((size_t)((tt * 2 + tb) * 88 + 2 * kq) * 1024) + lo;
        f32x16 acc[2]; acc[0] = f32x16{}; acc[1] = f32x16{};
        bf16x8 hf[14];
        auto comp = [&](int s, const bf16x8& h0, const bf16x8& h1) {
            lds_cptr base = (lds_cptr)lds + (s & 3) * FN_ST;
            const bf16x8 w00 = trfrag(base + woff0, base + woff0 + 1024), w01 = trfrag(base + woff1, base + woff1 + 1024);
            const bf16x8 w10 = trfrag(base + woff0 + 2048, base + woff0 + 3072), w11 = trfrag(base + woff1 + 2048, base + woff1 + 3072);
            acc[0] = MFMA(w00, h0, acc[0]); acc[1] = MFMA(w01, h0, acc[1]);
            acc[0] = MFMA(w10, h1, acc[0]); acc[1] = MFMA(w11, h1, acc[1]);
            SB();
        };
        if (w == 7) wait_words(ws, SY_H + tt, 1, 7u, l);
        if (w == 0) TL(5, 1, 99);
        bar_only();
        if (w == 0) TL(5, 1, 100);
#pragma unroll
        for (int q = 0; q < 14; ++q) hf[q] = ld16_dev(ws, hoff + (size_t)((32 + 8 * (q >> 1) + (q & 1)) * 1024));
        wait_vm<20>(); bar_only();            comp(0, hf[0], hf[1]);
        wait_vm<18>(); bar_only(); issue(4);  comp(1, hf[2], hf[3]);
        wait_vm<18>(); bar_only(); issue(5);  comp(2, hf[4], hf[5]);
        wait_vm<18>(); bar_only(); issue(6);  comp(3, hf[6], hf[7]);
        wait_vm<4>();  bar_only(); issue(7);  comp(4, hf[8], hf[9]);
        wait_vm<4>();  bar_only(); issue(8);  comp(5, hf[10], hf[11]);
        wait_vm<4>();  bar_only(); issue(9);  comp(6, hf[12], hf[13]);
        if (w == 0) TL(5, 1, 101);
        if (w == 7) wait_words(ws, SY_O + (tt >> 2), 1, 32u, l);
        bar_only();
        if (w == 0) TL(5, 1, 102);
#pragma unroll
        for (int q = 0; q < 8; ++q) hf[q] = ld16_dev(ws, hoff + (size_t)((8 * (q >> 1) + (q & 1)) * 1024));
        wait_vm<12>(); bar_only(); issue(10); comp(7, hf[0], hf[1]);
        wait_vm<12>(); bar_only();            comp(8, hf[2], hf[3]);
        wait_vm<10>(); bar_only();            comp(9, hf[4], hf[5]);
        wait_vm<0>();  bar_only();            comp(10, hf[6], hf[7]);
        if (w == 0) TL(5, 1, 103);
        bar_only();
        LDS_AS f32x4* red = (LDS_AS f32x4*)lds;
#pragma unroll
        for (int db = 0; db < 2; ++db)
#pragma unroll
            for (int q = 0; q < 4; ++q) red[((w * 2 + db) * 4 + q) * 64 + l] = f32x4{acc[db][4 * q], acc[db][4 * q + 1], acc[db][4 * q + 2], acc[db][4 * q + 3]};
        bar_only();
        const int db = kq & 1, gq = kq >> 1;
        f32x4 v0 = {0.f, 0.f, 0.f, 0.f}, v1 = v0;
#pragma unroll
        for (int q = 0; q < 4; ++q) { const int ww = q * 2 + tb; v0 += red[((ww * 2 + db) * 4 + 2 * gq) * 64 + l]; v1 += red[((ww * 2 + db) * 4 + 2 * gq + 1) * 64 + l]; }
        const unsigned a0 = cvtpk(v0[0], v0[1]), a1 = cvtpk(v0[2], v0[3]), b0 = cvtpk(v1[0], v1[1]), b1 = cvtpk(v1[2], v1[3]);
        auto r0 = __builtin_amdgcn_permlane32_swap(a0, b0, false, false);
        auto r1 = __builtin_amdgcn_permlane32_swap(a1, b1, false, false);
        const u32x4 ov = {r0[0], r1[0], r0[1], r1[1]};
        *(u32x4*)(out + (size_t)(tt * 64 + tb * 32 + r32) * D_ + ft * 64 + db * 32 + gq * 16 + h * 8) = ov;
        if (w == 0) TL(5, 1, 104);
    }
}

extern "C" __global__ __launch_bounds__(512, 4) void k_main(const u16* __restrict__ x, const u16* __restrict__ w_in, const u16* __restrict__ w_sp, const u16* __restrict__ b_sp,
                                                 const u16* __restrict__ wo, const u16* __restrict__ w_out, char* __restrict__ ws, u16* __restrict__ out) {
    __shared__ __attribute__((aligned(1024))) char lds[AT_LDS > SP_LDS ? AT_LDS : SP_LDS];
    const int b = blockIdx.x;
    if (b < 256) {
        STAMP(2, 0);
        attn_block(ws, lds, (b & 7) * 4 + ((b >> 3) & 3), b >> 5, threadIdx.x);
        STAMP(2, 2);
    } else {
        STAMP(5, 0);
        gate_block(x, w_in, w_sp, b_sp, wo, w_out, ws, out, lds, b - 256, threadIdx.x);
        STAMP(5, 2);
    }
}

extern "C" void kernel_launch(void* const* d_in, const int* in_sizes, int n_in, void* d_out, int out_size, void* d_ws, size_t ws_size, hipStream_t stream) {
    const u16* x = (const u16*)d_in[0];
    const u16* wqkv = (const u16*)d_in[1];
    const u16* wo = (const u16*)d_in[2];
    const u16* w_in = (const u16*)d_in[4];
    const u16* w_sp = (const u16*)d_in[5];
    const u16* b_sp = (const u16*)d_in[6];
    const u16* w_out = (const u16*)d_in[7];
    const u16* gate_w = (const u16*)d_in[8];
    char* ws = (char*)d_ws;
    k1_proj<<<256, 512, 0, stream>>>(x, wqkv, w_in, gate_w, w_sp, wo, w_out, ws);
    k_main<<<512, 512, 0, stream>>>(x, w_in, w_sp, b_sp, wo, w_out, ws, (u16*)d_out);
}
```

```cpp
#include <hip/hip_runtime.h>
#include <hip/hip_bf16.h>
#include <stdint.h>

typedef __attribute__((ext_vector_type(8))) short bf16x8;
typedef __attribute__((ext_vector_type(4))) short s16x4;
typedef __attribute__((ext_vector_type(16))) float f32x16;
typedef __attribute__((ext_vector_type(4))) unsigned u32x4;
typedef __attribute__((ext_vector_type(2))) float f32x2;
typedef __attribute__((ext_vector_type(4))) float f32x4;
typedef __attribute__((ext_vector_type(2))) __bf16 bf16x2_t;
#define LDS_AS __attribute__((address_space(3)))
typedef LDS_AS const char* lds_cptr;
typedef LDS_AS char* lds_ptr;
typedef short v4i16_t __attribute__((ext_vector_type(4)));
typedef unsigned short u16;
#define DI __device__ __forceinline__

#define MFMA(a, b, c) __builtin_amdgcn_mfma_f32_32x32x16_bf16((a), (b), (c), 0, 0, 0)
#define SB() __builtin_amdgcn_sched_barrier(0)
template <int NM, int ND> DI void interleave() {
#pragma unroll
    for (int i = 0; i < NM; ++i) { __builtin_amdgcn_sched_group_barrier(0x008, 1, 0); __builtin_amdgcn_sched_group_barrier(0x100, ND, 0); }
}
#define TL(kid, who, idx) do {} while (0)
#define WAIT_BAR(N) asm volatile("s_waitcnt vmcnt(" #N ") lgkmcnt(0)\n\ts_barrier" ::: "memory")

#define K1_ROT(mt, p) 0
#define SP_ROT(tl, fl) ((tl) * 4 + (fl) * 2)
#define PACE 1
#define PACE_() do { if (PACE == 1) __builtin_amdgcn_s_sleep(1); else if (PACE == 2) asm volatile("s_nop 7"); else if (PACE == 3) asm volatile("s_nop 11"); else if (PACE == 4) asm volatile("s_nop 15"); SB(); } while (0)
#define LPRIO 0
#define K1_WT 1
#define K1_ST16(off, v) st16_wt(ws, (off), (v))
#define TLB_PF 1
#define TLB_STRIDE 65536
#define TLB_NWG 1
#define CODE_PF 1
#define DATA_PF 0
#define GPRIO 2
#define FN_ROT(i, dt) ((((i) & 3) * 3 + (dt)) % 11)
constexpr int S_ = 2048, D_ = 512, FH = 896;
constexpr float QSCALE = 0.25f * 1.4426950408889634f;

constexpr size_t WS_QF    = 0;
constexpr size_t WS_KV    = WS_QF + 32u * 64 * 1024;
constexpr size_t WS_Z2    = WS_KV + 32u * 64 * 2048;
constexpr size_t WS_Z2N   = WS_Z2 + 64u * 56 * 1024;
constexpr size_t WS_HO    = WS_Z2N + 28u * 128 * 1024;
constexpr size_t WS_STATS = WS_HO + 64u * 88 * 1024;
constexpr size_t WS_KMAX  = WS_STATS + 2048u * 28 * 8;
constexpr size_t WS_GATE  = WS_KMAX + 64u * 32 * 4;
constexpr size_t WS_SYNC  = WS_GATE + 2048u * 4;
constexpr int SY_LN = 0, SY_H = 16, SY_O = 48, SY_TMO = 56, SY_N = 57;
constexpr size_t WS_END   = WS_SYNC + SY_N * 256;

#define STAMP(kid, slot) do {} while (0)
DI unsigned cvtpk(float a, float b) { f32x2 f = {a, b}; bf16x2_t r = __builtin_convertvector(f, bf16x2_t); return __builtin_bit_cast(unsigned, r); }
DI float bf_lo(unsigned u) { return __uint_as_float(u << 16); }
DI float bf_hi(unsigned u) { return __uint_as_float(u & 0xffff0000u); }
DI void glds16(const void* g, unsigned lds_base) {
    unsigned sv;
    asm volatile("s_mov_b32 %0, m0\n\ts_mov_b32 m0, %2\n\ts_nop 0\n\tglobal_load_lds_dwordx4 %1, off\n\ts_mov_b32 m0, %0"
                 : "=&s"(sv) : "v"(g), "s"(lds_base) : "memory");
}
DI void glds16s(const void* ubase, unsigned voff, unsigned lds_base) {
    asm volatile("s_mov_b32 m0, %2\n\ts_nop 0\n\tglobal_load_lds_dwordx4 %0, %1" :: "v"(voff), "s"(ubase), "s"(lds_base) : "memory");
}
#define GAS __attribute__((address_space(1)))
typedef GAS unsigned gu32;
#define RLX_AGENT __ATOMIC_RELAXED, __HIP_MEMORY_SCOPE_AGENT
DI gu32* sync_word(char* ws, int i) { return (gu32*)(ws + WS_SYNC + (size_t)i * 256); }
DI void st16_wt(char* ws, size_t off, u32x4 v) {
    __builtin_amdgcn_raw_buffer_store_b128(v, __builtin_amdgcn_make_buffer_rsrc(ws, 0, (int)WS_END, 0x00020000), (int)off, 0, 16);
}
DI bf16x8 ld16_dev(char* ws, size_t off) {
    return __builtin_bit_cast(bf16x8, __builtin_amdgcn_raw_buffer_load_b128(__builtin_amdgcn_make_buffer_rsrc(ws, 0, (int)WS_END, 0x00020000), (int)off, 0, 16));
}
DI void prefetch_line(const void* ubase, unsigned voff, unsigned lds_dump) {
    asm volatile("s_mov_b32 m0, %2\n\ts_nop 0\n\tglobal_load_lds_dword %0, %1" :: "v"(voff), "s"(ubase), "s"(lds_dump) : "memory");
}
DI void prefetch_ptr(const void* g, unsigned lds_dump) {
    asm volatile("s_mov_b32 m0, %1\n\ts_nop 0\n\tglobal_load_lds_dword %0, off" :: "v"(g), "s"(lds_dump) : "memory");
}
DI void drain_stores() { asm volatile("s_waitcnt vmcnt(0)" ::: "memory"); }
DI void wait_words(char* ws, int first, int n, unsigned want, int lane) {
    gu32* p = sync_word(ws, first + lane % n);
    for (unsigned spins = 0;; ++spins) {
        if (__all(__hip_atomic_load(p, RLX_AGENT) >= want)) break;
        if (spins > (1u << 22)) { if (lane == 0) __hip_atomic_store(sync_word(ws, SY_TMO), 1u, RLX_AGENT); break; }
        __builtin_amdgcn_s_sleep(2);
    }
    asm volatile("" ::: "memory");
}
DI void bar_only() { asm volatile("" ::: "memory"); __builtin_amdgcn_s_waitcnt(0xC07F); __builtin_amdgcn_s_barrier(); asm volatile("" ::: "memory"); }
template <int N> DI void wait_bar() { asm volatile("s_waitcnt vmcnt(%0)" :: "i"(N) : "memory"); bar_only(); }
DI s16x4 vtr(lds_cptr p) { return __builtin_bit_cast(s16x4, __builtin_amdgcn_ds_read_tr16_b64_v4i16((LDS_AS v4i16_t*)p)); }
DI bf16x8 trfrag(lds_cptr p0, lds_cptr p1) { s16x4 lo = vtr(p0), hi = vtr(p1); return __builtin_shufflevector(lo, hi, 0, 1, 2, 3, 4, 5, 6, 7); }
DI bf16x8 ldsfrag(lds_cptr p) { return *(const LDS_AS bf16x8*)p; }
DI u32x4 pack8(const f32x16& a, int o, float sc) {
    u32x4 r; r[0] = cvtpk(a[o] * sc, a[o + 1] * sc); r[1] = cvtpk(a[o + 2] * sc, a[o + 3] * sc);
    r[2] = cvtpk(a[o + 4] * sc, a[o + 5] * sc); r[3] = cvtpk(a[o + 6] * sc, a[o + 7] * sc); return r;
}
DI float swap32_sum(float v) { auto r = __builtin_amdgcn_permlane32_swap(__float_as_uint(v), __float_as_uint(v), false, false); return __uint_as_float(r[0]) + __uint_as_float(r[1]); }
DI float swap32_max(float v) { auto r = __builtin_amdgcn_permlane32_swap(__float_as_uint(v), __float_as_uint(v), false, false); return fmaxf(__uint_as_float(r[0]), __uint_as_float(r[1])); }
DI float dpp_max16(float v) {
    v = fmaxf(v, __uint_as_float(__builtin_amdgcn_update_dpp(0u, __float_as_uint(v), 0x128, 0xf, 0xf, false)));
    v = fmaxf(v, __uint_as_float(__builtin_amdgcn_update_dpp(0u, __float_as_uint(v), 0x124, 0xf, 0xf, false)));
    v = fmaxf(v, __uint_as_float(__builtin_amdgcn_update_dpp(0u, __float_as_uint(v), 0x122, 0xf, 0xf, false)));
    v = fmaxf(v, __uint_as_float(__builtin_amdgcn_update_dpp(0u, __float_as_uint(v), 0x121, 0xf, 0xf, false)));
    return v;
}
DI float wave_max_fast(float v) {
    v = dpp_max16(v);
    auto r = __builtin_amdgcn_permlane16_swap(__float_as_uint(v), __float_as_uint(v), false, false);
    v = fmaxf(__uint_as_float(r[0]), __uint_as_float(r[1]));
    return swap32_max(v);
}

DI float gelu_tanh(float x) {
    const float c1 = -2.0f * 0.7978845608028654f * 1.4426950408889634f, c2 = c1 * 0.044715f;
    const float a = x * __builtin_fmaf(x * x, c2, c1);
    return x * __builtin_amdgcn_rcpf(1.0f + __builtin_amdgcn_exp2f(a));
}

constexpr int K1_ST = 40960, K1_LDS = 3 * K1_ST + 1024 + 256;
extern "C" __global__ void k_main(const u16* __restrict__ x, const u16* __restrict__ w_in, const u16* __restrict__ w_sp, const u16* __restrict__ b_sp,
                                  const u16* __restrict__ wo, const u16* __restrict__ w_out, char* __restrict__ ws, u16* __restrict__ out);

template <int TYPE>
DI void k1_body(const u16* __restrict__ x, const u16* __restrict__ wqkv, const u16* __restrict__ w_in, const u16* __restrict__ gate_w,
                const u16* __restrict__ w_sp, const u16* __restrict__ wo, const u16* __restrict__ w_out, const u16* __restrict__ out,
                char* __restrict__ ws, char* lds, int mt, int p, int tid) {
    if (tid == 0) TL(0, 1, 99);
    constexpr int NG = TYPE == 0 ? 3 : (TYPE == 1 ? 2 : 1);
    constexpr int NA = TYPE == 2 ? 1 : (TYPE == 3 ? 2 : NG);
    constexpr int P = 4 + 2 * NG;
    const int l = tid & 63, w = __builtin_amdgcn_readfirstlane(tid >> 6);
    const int rot = K1_ROT(mt, p);
    if (w >= 4) {
        const int j = w - 4;
        const unsigned lds0 = (unsigned)(uintptr_t)lds;
        const int row = 8 * j + (l >> 3);
        const unsigned xv = (unsigned)(row * D_ + ((l & 7) ^ ((row >> 1) & 7)) * 8) * 2;
        const int wch = ((l & 7) ^ (((row >> 1) & 1) << 2)) * 8;
        const unsigned qv = (unsigned)(row * 1536 + wch) * 2, zv = (unsigned)(row * 1792 + wch) * 2;
        const char* xu = (const char*)(x + (size_t)mt * 128 * D_);
        const char* g0 = (const char*)(wqkv + p * 64 + (TYPE == 0 ? 0 : 1024));
        const char* g1 = TYPE == 0 ? (const char*)(wqkv + p * 64 + 512) : (const char*)(w_in + FH + (8 + p) * 64);
        const char* g2 = (const char*)(w_in + FH + p * 64);
        auto issue = [&](int s) {
            const unsigned dst = lds0 + (s % 3) * K1_ST + j * 1024;
            const int sr = (s + rot) & 7;
#pragma unroll
            for (int i = 0; i < 4; ++i) glds16s(xu + (size_t)i * 32 * D_ * 2 + sr * 128, xv, dst + i * 4096);
            glds16s(g0 + (size_t)sr * 64 * 3072, qv, dst + 16384); glds16s(g0 + (size_t)(sr * 64 + 32) * 3072, qv, dst + 16384 + 4096);
            if (NG >= 2) { constexpr int ld = TYPE == 0 ? 3072 : 3584; const unsigned gv = TYPE == 0 ? qv : zv;
                glds16s(g1 + (size_t)sr * 64 * ld, gv, dst + 16384 + 8192); glds16s(g1 + (size_t)(sr * 64 + 32) * ld, gv, dst + 16384 + 8192 + 4096); }
            if (NG >= 3) { glds16s(g2 + (size_t)sr * 64 * 3584, zv, dst + 32768); glds16s(g2 + (size_t)(sr * 64 + 32) * 3584, zv, dst + 32768 + 4096); }
        };
        if (TYPE == 3 && j == 0) glds16s(gate_w, l * 16, lds0 + 3 * K1_ST);
        issue(0); issue(1); issue(2);
        wait_bar<2 * P>();
#pragma unroll 1
        for (int s = 0; s < 7; ++s) {
            if (j == 0) TL(0, 0, 3 * s);
            if (s + 2 < 8) asm volatile("s_waitcnt vmcnt(%0)" :: "i"(P) : "memory"); else asm volatile("s_waitcnt vmcnt(0)" ::: "memory");
            if (j == 0) TL(0, 0, 3 * s + 1);
            bar_only();
            if (j == 0) TL(0, 0, 3 * s + 2);
            if (s + 3 < 8) issue(s + 3);
        }
        return;
    }
    if (w == 3) {
        const unsigned dump = (unsigned)(uintptr_t)lds + 3 * K1_ST + 1024;
        {
            unsigned clo, chi;
            asm volatile("s_getpc_b64 s[70:71]\n\ts_add_u32 s70, s70, k_main@rel32@lo+4\n\ts_addc_u32 s71, s71, k_main@rel32@hi+12\n\ts_mov_b32 %0, s70\n\ts_mov_b32 %1, s71"
                         : "=s"(clo), "=s"(chi) :: "s70", "s71", "scc");
            const char* cm = (const char*)(((unsigned long long)chi << 32) | clo);
#pragma unroll
            for (int i = 0; i < 3; ++i) prefetch_line(cm, min((unsigned)(i * 64 + l) * 128, 18176u), dump);
        }
        if ((blockIdx.x >> 3) < TLB_NWG) {
            constexpr unsigned ST = TLB_STRIDE;
#pragma unroll 1
            for (unsigned o = l * ST; o < (unsigned)WS_END; o += 64 * ST) prefetch_ptr(ws + o, dump);
#pragma unroll 1
            for (unsigned o = l * ST; o < 8388608u; o += 64 * ST) prefetch_ptr((const char*)w_sp + o, dump);
            if (l * ST < 2097152u) prefetch_ptr((const char*)out + l * ST, dump);
            if (l * ST < 1835008u) prefetch_ptr((const char*)w_in + l * ST, dump);
            if (l * ST < 917504u) prefetch_ptr((const char*)w_out + l * ST, dump);
            if (l * ST < 524288u) prefetch_ptr((const char*)wo + l * ST, dump);
        }
    }
    const int rh = w & 1, ch = w >> 1, h = l >> 5, r32 = l & 31;
    int xo[4];
    { const int row = rh * 64 + r32, f = (row >> 1) & 7;
#pragma unroll
      for (int c = 0; c < 4; ++c) xo[c] = row * 128 + (((2 * c + h) ^ f) << 4); }
    const int q4 = (l & 15) >> 2, p4 = l & 3, b16 = (l >> 4) & 1;
    const int wl = 16384 + (8 * h + q4) * 128 + (((ch * 4 + b16 * 2 + (p4 >> 1)) ^ ((q4 >> 1) << 2)) << 4) + (p4 & 1) * 8;
    f32x16 acc[NA][2];
#pragma unroll
    for (int g = 0; g < NA; ++g) { acc[g][0] = f32x16{}; acc[g][1] = f32x16{}; }
    bf16x8 xa[2][2], wa[NG][2], xb[2][2], wb[NG][2], ga[2], gb[2];
    auto rdh = [&](int s, int half, bf16x8 (&xf)[2][2], bf16x8 (&wf)[NG][2], bf16x8 (&gf)[2]) {
        lds_cptr base = (lds_cptr)lds + (s % 3) * K1_ST;
#pragma unroll
        for (int kk = 0; kk < 2; ++kk) {
            const int ks = 2 * half + kk;
            xf[0][kk] = ldsfrag(base + xo[ks]); xf[1][kk] = ldsfrag(base + xo[ks] + 4096);
#pragma unroll
            for (int g = 0; g < NG; ++g) wf[g][kk] = trfrag(base + wl + g * 8192 + ks * 2048, base + wl + g * 8192 + ks * 2048 + 512);
            if (TYPE == 3) gf[kk] = ldsfrag((lds_cptr)lds + 3 * K1_ST + (((s + rot) & 7) * 4 + ks) * 32 + h * 16);
        }
    };
    auto mm = [&](bf16x8 (&xf)[2][2], bf16x8 (&wf)[NG][2], bf16x8 (&gf)[2]) {
#pragma unroll
        for (int kk = 0; kk < 2; ++kk)
#pragma unroll
            for (int rb = 0; rb < 2; ++rb) {
                if (TYPE == 0) {
#pragma unroll
                    for (int g = 0; g < 3; ++g) acc[g % NA][rb] = MFMA(wf[g % NG][kk], xf[rb][kk], acc[g % NA][rb]);
                } else {
                    acc[0][rb] = MFMA(xf[rb][kk], wf[0][kk], acc[0][rb]);
                    if (TYPE == 1) acc[NA - 1][rb] = MFMA(wf[NG - 1][kk], xf[rb][kk], acc[NA - 1][rb]);
                    if (TYPE == 3) acc[NA - 1][rb] = MFMA(gf[kk], xf[rb][kk], acc[NA - 1][rb]);
                }
            }
    };
    if (w == 0) TL(0, 1, 100);
    bar_only();
    if (w == 0) TL(0, 1, 101);
    rdh(0, 0, xa, wa, ga);
#pragma unroll 1
    for (int s = 0; s < 7; ++s) {
        rdh(s, 1, xb, wb, gb); mm(xa, wa, ga); interleave<4 * NA, 2>(); SB();
        if (w == 0) TL(0, 1, 2 * s);
        bar_only();
        if (w == 0) TL(0, 1, 2 * s + 1);
        rdh(s + 1, 0, xa, wa, ga); mm(xb, wb, gb); interleave<4 * NA, 2>(); SB();
    }
    rdh(7, 1, xb, wb, gb); mm(xa, wa, ga); interleave<4 * NA, 2>(); SB();
    mm(xb, wb, gb);
    STAMP(0, 1);
    if (w == 0) TL(0, 1, 102);
#pragma unroll
    for (int rb = 0; rb < 2; ++rb) {
        const int ttile = mt * 4 + rh * 2 + rb;
        if (TYPE == 0) {
            const int hd0 = 4 * p + 2 * ch;
            const f32x16 &q = acc[0][rb], &k = acc[1 % NA][rb];
            K1_ST16(WS_QF + ((size_t)(hd0 * 64 + ttile) * 1024) + l * 16, pack8(q, 0, QSCALE));
            K1_ST16(WS_QF + ((size_t)((hd0 + 1) * 64 + ttile) * 1024) + l * 16, pack8(q, 8, QSCALE));
            K1_ST16(WS_KV + ((size_t)(hd0 * 64 + ttile) * 2048) + l * 16, pack8(k, 0, 1.0f));
            K1_ST16(WS_KV + ((size_t)((hd0 + 1) * 64 + ttile) * 2048) + l * 16, pack8(k, 8, 1.0f));
            float n0 = 0.f, n1 = 0.f;
#pragma unroll
            for (int r = 0; r < 8; ++r) { n0 = __builtin_fmaf(k[r], k[r], n0); n1 = __builtin_fmaf(k[8 + r], k[8 + r], n1); }
            n0 = wave_max_fast(swap32_sum(n0)); n1 = wave_max_fast(swap32_sum(n1));
            if (l == 0) { float* km = (float*)(ws + WS_KMAX) + ttile * 32 + hd0; km[0] = n0 * 1.01f; km[1] = n1 * 1.01f; }
        } else {
            const int hd = 4 * p + 2 * ch + (r32 >> 4), d = r32 & 15;
            const size_t vp = WS_KV + ((size_t)(hd * 64 + ttile) * 2048) + 1024 + h * 256 + d * 16;
            K1_ST16(vp, pack8(acc[0][rb], 0, 1.0f)); K1_ST16(vp + 512, pack8(acc[0][rb], 8, 1.0f));
            if (TYPE == 3 && ch == 0 && h == 0)
                ((float*)(ws + WS_GATE))[ttile * 32 + r32] = __builtin_amdgcn_rcpf(1.0f + __builtin_amdgcn_exp2f(-1.4426950408889634f * acc[NA - 1][rb][0]));
        }
        if (TYPE <= 1) {
            f32x16& z = acc[NA - 1][rb];
            const int zg = TYPE == 0 ? p : 8 + p;
            float s1 = 0.f, s2 = 0.f;
#pragma unroll
            for (int r = 0; r < 16; ++r) { const float v = gelu_tanh(z[r]); z[r] = v; s1 += v; s2 = __builtin_fmaf(v, v, s2); }
            const int fb = 2 * zg + ch;
            const size_t zp = WS_Z2 + ((size_t)(ttile * 56 + 2 * fb) * 1024) + l * 16;
            K1_ST16(zp, pack8(z, 0, 1.0f)); K1_ST16(zp + 1024, pack8(z, 8, 1.0f));
            s1 = swap32_sum(s1); s2 = swap32_sum(s2);
            if (h == 0) ((f32x2*)(ws + WS_STATS))[(size_t)(ttile * 32 + r32) * 28 + fb] = f32x2{s1, s2};
        }
    }
    if (w == 3) drain_stores();
    if (w == 0) TL(0, 1, 103);
}

__global__ __launch_bounds__(512) void k1_proj(const u16* __restrict__ x, const u16* __restrict__ wqkv, const u16* __restrict__ w_in, const u16* __restrict__ gate_w,
                                               const u16* __restrict__ w_sp, const u16* __restrict__ wo, const u16* __restrict__ w_out, const u16* __restrict__ out, char* __restrict__ ws) {
    __shared__ __attribute__((aligned(1024))) char lds[K1_LDS];
    const int b = blockIdx.x, xcd = b & 7, idx = b >> 3;
    const int mt = (xcd & 1) * 8 + (idx & 7), nt = (xcd >> 1) * 4 + (idx >> 3), p = nt >> 1;
    STAMP(0, 0);
    if (b == 255 && threadIdx.x < SY_N) *(unsigned*)(ws + WS_SYNC + threadIdx.x * 256) = 0u;
    if (!(nt & 1)) k1_body<0>(x, wqkv, w_in, gate_w, w_sp, wo, w_out, out, ws, lds, mt, p, threadIdx.x);
    else if (p < 6) k1_body<1>(x, wqkv, w_in, gate_w, w_sp, wo, w_out, out, ws, lds, mt, p, threadIdx.x);
    else if (p == 6) k1_body<2>(x, wqkv, w_in, gate_w, w_sp, wo, w_out, out, ws, lds, mt, p, threadIdx.x);
    else k1_body<3>(x, wqkv, w_in, gate_w, w_sp, wo, w_out, out, ws, lds, mt, p, threadIdx.x);
    STAMP(0, 2);
}

struct LnStat { float rstd, nm; };
DI LnStat ln_stats(const f32x4 (&pv)[7]) {
    float s1 = 0.f, s2 = 0.f;
#pragma unroll
    for (int i = 0; i < 7; ++i) { const f32x4 v = pv[i]; s1 += v[0] + v[2]; s2 += v[1] + v[3]; }
    s1 = swap32_sum(s1); s2 = swap32_sum(s2);
    const float mean = s1 * (1.0f / FH);
    const float var = fmaxf(s2 * (1.0f / FH) - mean * mean, 0.f);
    LnStat r; r.rstd = __builtin_amdgcn_rsqf(var + 1e-5f); r.nm = -mean * r.rstd; return r;
}
DI void ln_block(char* __restrict__ ws, char* scratch, int ttile, int fb, const u32x4& v0, const u32x4& v1, LnStat st, int l) {
    const int h = l >> 5, r32 = l & 31, q4 = (l & 15) >> 2, p4 = l & 3, b16 = (l >> 4) & 1;
    u32x4 ov[2];
#pragma unroll
    for (int g = 0; g < 2; ++g) {
        const u32x4 v = g ? v1 : v0;
        unsigned o[4];
#pragma unroll
        for (int e = 0; e < 4; ++e) o[e] = cvtpk(__builtin_fmaf(bf_lo(v[e]), st.rstd, st.nm), __builtin_fmaf(bf_hi(v[e]), st.rstd, st.nm));
        auto r0 = __builtin_amdgcn_permlane32_swap(o[0], o[2], false, false);
        auto r1 = __builtin_amdgcn_permlane32_swap(o[1], o[3], false, false);
        ov[g] = u32x4{r0[0], r1[0], r0[1], r1[1]};
    }
    lds_ptr tile = (lds_ptr)scratch;
#pragma unroll
    for (int kk = 0; kk < 2; ++kk) {
        if ((r32 >> 4) == kk) {
            *(LDS_AS u32x4*)(tile + (r32 & 15) * 64 + h * 16) = ov[0]; *(LDS_AS u32x4*)(tile + (r32 & 15) * 64 + 32 + h * 16) = ov[1]; }
        asm volatile("" ::: "memory");
        lds_cptr rp = (lds_cptr)tile + (8 * h + q4) * 64 + b16 * 32 + p4 * 8;
        const bf16x8 fr = trfrag(rp, rp + 256);
        asm volatile("" ::: "memory");
        st16_wt(ws, WS_Z2N + ((size_t)(fb * 128 + ttile * 2 + kk) * 1024) + l * 16, __builtin_bit_cast(u32x4, fr));
    }
}

constexpr int AT_TS = 2304, AT_SLOT = 8 * AT_TS, AT_LDS = 4 * AT_SLOT + 64;
#define ATTN_ASM \
    "s_mov_b32 %[sv], m0\n\t" \
    "v_mov_b32 v88, 0\n\t" \
    "v_mov_b32 v89, 0\n\t" \
    "v_mov_b32 v90, 0\n\t" \
    "v_mov_b32 v91, 0\n\t" \
    "v_mov_b32 v96, 0\n\t" \
    "v_mov_b32 v97, 0\n\t" \
    "v_mov_b32 v98, 0\n\t" \
    "v_mov_b32 v99, 0\n\t" \
    "v_mov_b32 v0, 0\n\t" \
    "v_mov_b32 v1, 0\n\t" \
    "v_mov_b32 v2, 0\n\t" \
    "v_mov_b32 v3, 0\n\t" \
    "v_mov_b32 v4, 0\n\t" \
    "v_mov_b32 v5, 0\n\t" \
    "v_mov_b32 v6, 0\n\t" \
    "v_mov_b32 v7, 0\n\t" \
    "v_mov_b32 v8, 0\n\t" \
    "v_mov_b32 v9, 0\n\t" \
    "v_mov_b32 v10, 0\n\t" \
    "v_mov_b32 v11, 0\n\t" \
    "v_mov_b32 v12, 0\n\t" \
    "v_mov_b32 v13, 0\n\t" \
    "v_mov_b32 v14, 0\n\t" \
    "v_mov_b32 v15, 0\n\t" \
    "s_mov_b32 %[cnt], 0\n\t" \
    "s_waitcnt vmcnt(4) lgkmcnt(0)\n\t" \
    "s_barrier\n\t" \
    "ds_read_b128 v[68:71], %[kl] offset:0\n\t" \
    "ds_read_b128 v[72:75], %[kl] offset:2304\n\t" \
    "ds_read_b128 v[76:79], %[v0l] offset:0\n\t" \
    "ds_read_b128 v[80:83], %[v1l] offset:0\n\t" \
    "s_waitcnt lgkmcnt(0)\n\t" \
    "s_setprio 3\n\t" \
    "v_mfma_f32_32x32x16_bf16 v[32:47], v[68:71], v[64:67], v[16:31]\n\t" \
    "s_setprio 0\n\t" \
    "s_nop 7\n\t" \
    "s_nop 3\n\t" \
    "Lloop%=:\n\t" \
    "s_setprio 3\n\t" \
    "v_mfma_f32_32x32x16_bf16 v[48:63], v[72:75], v[64:67], v[16:31]\n\t" \
    "s_setprio 2\n\t" \
    "v_exp_f32 v32, v32\n\t" \
    "ds_read_b128 v[68:71], %[kl] offset:4608\n\t" \
    "v_exp_f32 v33, v33\n\t" \
    "v_exp_f32 v34, v34\n\t" \
    "v_exp_f32 v35, v35\n\t" \
    "v_exp_f32 v36, v36\n\t" \
    "v_exp_f32 v37, v37\n\t" \
    "v_exp_f32 v38, v38\n\t" \
    "s_setprio 3\n\t" \
    "v_mfma_f32_32x32x16_bf16 v[0:15], v[88:91], v[96:99], v[0:15]\n\t" \
    "s_setprio 2\n\t" \
    "v_exp_f32 v39, v39\n\t" \
    "ds_read_b128 v[84:87], %[v0l] offset:2304\n\t" \
    "v_cvt_pk_bf16_f32 v92, v32, v33\n\t" \
    "v_cvt_pk_bf16_f32 v93, v34, v35\n\t" \
    "v_cvt_pk_bf16_f32 v94, v36, v37\n\t" \
    "v_cvt_pk_bf16_f32 v95, v38, v39\n\t" \
    "v_exp_f32 v40, v40\n\t" \
    "ds_read_b128 v[88:91], %[v1l] offset:2304\n\t" \
    "v_exp_f32 v41, v41\n\t" \
    "v_exp_f32 v42, v42\n\t" \
    "v_exp_f32 v43, v43\n\t" \
    "s_waitcnt lgkmcnt(4)\n\t" \
    "s_setprio 3\n\t" \
    "v_mfma_f32_32x32x16_bf16 v[0:15], v[76:79], v[92:95], v[0:15]\n\t" \
    "s_setprio 2\n\t" \
    "v_exp_f32 v44, v44\n\t" \
    "v_exp_f32 v45, v45\n\t" \
    "v_exp_f32 v46, v46\n\t" \
    "v_exp_f32 v47, v47\n\t" \
    "s_waitcnt lgkmcnt(2)\n\t" \
    "v_cvt_pk_bf16_f32 v96, v40, v41\n\t" \
    "v_cvt_pk_bf16_f32 v97, v42, v43\n\t" \
    "v_cvt_pk_bf16_f32 v98, v44, v45\n\t" \
    "v_cvt_pk_bf16_f32 v99, v46, v47\n\t" \
    "s_setprio 3\n\t" \
    "v_mfma_f32_32x32x16_bf16 v[32:47], v[68:71], v[64:67], v[16:31]\n\t" \
    "s_setprio 1\n\t" \
    "v_exp_f32 v48, v48\n\t" \
    "ds_read_b128 v[72:75], %[kl] offset:6912\n\t" \
    "v_exp_f32 v49, v49\n\t" \
    "v_exp_f32 v50, v50\n\t" \
    "v_exp_f32 v51, v51\n\t" \
    "v_exp_f32 v52, v52\n\t" \
    "v_exp_f32 v53, v53\n\t" \
    "v_exp_f32 v54, v54\n\t" \
    "s_setprio 3\n\t" \
    "v_mfma_f32_32x32x16_bf16 v[0:15], v[80:83], v[96:99], v[0:15]\n\t" \
    "s_setprio 1\n\t" \
    "v_exp_f32 v55, v55\n\t" \
    "ds_read_b128 v[76:79], %[v0l] offset:4608\n\t" \
    "v_cvt_pk_bf16_f32 v92, v48, v49\n\t" \
    "v_cvt_pk_bf16_f32 v93, v50, v51\n\t" \
    "v_cvt_pk_bf16_f32 v94, v52, v53\n\t" \
    "v_cvt_pk_bf16_f32 v95, v54, v55\n\t" \
    "v_exp_f32 v56, v56\n\t" \
    "ds_read_b128 v[80:83], %[v1l] offset:4608\n\t" \
    "v_exp_f32 v57, v57\n\t" \
    "v_exp_f32 v58, v58\n\t" \
    "v_exp_f32 v59, v59\n\t" \
    "s_waitcnt lgkmcnt(4)\n\t" \
    "s_setprio 3\n\t" \
    "v_mfma_f32_32x32x16_bf16 v[0:15], v[84:87], v[92:95], v[0:15]\n\t" \
    "s_setprio 1\n\t" \
    "v_exp_f32 v60, v60\n\t" \
    "v_exp_f32 v61, v61\n\t" \
    "v_exp_f32 v62, v62\n\t" \
    "v_exp_f32 v63, v63\n\t" \
    "s_waitcnt lgkmcnt(2)\n\t" \
    "v_cvt_pk_bf16_f32 v96, v56, v57\n\t" \
    "v_cvt_pk_bf16_f32 v97, v58, v59\n\t" \
    "v_cvt_pk_bf16_f32 v98, v60, v61\n\t" \
    "v_cvt_pk_bf16_f32 v99, v62, v63\n\t" \
    "s_setprio 3\n\t" \
    "v_mfma_f32_32x32x16_bf16 v[48:63], v[72:75], v[64:67], v[16:31]\n\t" \
    "s_setprio 0\n\t" \
    "v_exp_f32 v32, v32\n\t" \
    "ds_read_b128 v[68:71], %[kl] offset:9216\n\t" \
    "v_exp_f32 v33, v33\n\t" \
    "v_exp_f32 v34, v34\n\t" \
    "v_exp_f32 v35, v35\n\t" \
    "v_exp_f32 v36, v36\n\t" \
    "v_exp_f32 v37, v37\n\t" \
    "v_exp_f32 v38, v38\n\t" \
    "s_setprio 3\n\t" \
    "v_mfma_f32_32x32x16_bf16 v[0:15], v[88:91], v[96:99], v[0:15]\n\t" \
    "s_setprio 0\n\t" \
    "v_exp_f32 v39, v39\n\t" \
    "ds_read_b128 v[84:87], %[v0l] offset:6912\n\t" \
    "v_cvt_pk_bf16_f32 v92, v32, v33\n\t" \
    "v_cvt_pk_bf16_f32 v93, v34, v35\n\t" \
    "v_cvt_pk_bf16_f32 v94, v36, v37\n\t" \
    "v_cvt_pk_bf16_f32 v95, v38, v39\n\t" \
    "v_exp_f32 v40, v40\n\t" \
    "ds_read_b128 v[88:91], %[v1l] offset:6912\n\t" \
    "v_exp_f32 v41, v41\n\t" \
    "v_exp_f32 v42, v42\n\t" \
    "v_exp_f32 v43, v43\n\t" \
    "s_waitcnt lgkmcnt(4)\n\t" \
    "s_setprio 3\n\t" \
    "v_mfma_f32_32x32x16_bf16 v[0:15], v[76:79], v[92:95], v[0:15]\n\t" \
    "s_setprio 0\n\t" \
    "v_exp_f32 v44, v44\n\t" \
    "v_exp_f32 v45, v45\n\t" \
    "v_exp_f32 v46, v46\n\t" \
    "v_exp_f32 v47, v47\n\t" \
    "s_waitcnt lgkmcnt(2)\n\t" \
    "v_cvt_pk_bf16_f32 v96, v40, v41\n\t" \
    "v_cvt_pk_bf16_f32 v97, v42, v43\n\t" \
    "v_cvt_pk_bf16_f32 v98, v44, v45\n\t" \
    "v_cvt_pk_bf16_f32 v99, v46, v47\n\t" \
    "s_setprio 3\n\t" \
    "v_mfma_f32_32x32x16_bf16 v[32:47], v[68:71], v[64:67], v[16:31]\n\t" \
    "s_setprio 0\n\t" \
    "v_exp_f32 v48, v48\n\t" \
    "ds_read_b128 v[72:75], %[kl] offset:11520\n\t" \
    "v_exp_f32 v49, v49\n\t" \
    "v_exp_f32 v50, v50\n\t" \
    "v_exp_f32 v51, v51\n\t" \
    "v_exp_f32 v52, v52\n\t" \
    "v_exp_f32 v53, v53\n\t" \
    "v_exp_f32 v54, v54\n\t" \
    "s_setprio 3\n\t" \
    "v_mfma_f32_32x32x16_bf16 v[0:15], v[80:83], v[96:99], v[0:15]\n\t" \
    "s_setprio 0\n\t" \
    "v_exp_f32 v55, v55\n\t" \
    "ds_read_b128 v[76:79], %[v0l] offset:9216\n\t" \
    "v_cvt_pk_bf16_f32 v92, v48, v49\n\t" \
    "v_cvt_pk_bf16_f32 v93, v50, v51\n\t" \
    "v_cvt_pk_bf16_f32 v94, v52, v53\n\t" \
    "v_cvt_pk_bf16_f32 v95, v54, v55\n\t" \
    "v_exp_f32 v56, v56\n\t" \
    "ds_read_b128 v[80:83], %[v1l] offset:9216\n\t" \
    "v_exp_f32 v57, v57\n\t" \
    "v_exp_f32 v58, v58\n\t" \
    "v_exp_f32 v59, v59\n\t" \
    "s_waitcnt lgkmcnt(4)\n\t" \
    "s_setprio 3\n\t" \
    "v_mfma_f32_32x32x16_bf16 v[0:15], v[84:87], v[92:95], v[0:15]\n\t" \
    "s_setprio 0\n\t" \
    "v_exp_f32 v60, v60\n\t" \
    "v_exp_f32 v61, v61\n\t" \
    "v_exp_f32 v62, v62\n\t" \
    "v_exp_f32 v63, v63\n\t" \
    "s_waitcnt lgkmcnt(2)\n\t" \
    "v_cvt_pk_bf16_f32 v96, v56, v57\n\t" \
    "v_cvt_pk_bf16_f32 v97, v58, v59\n\t" \
    "v_cvt_pk_bf16_f32 v98, v60, v61\n\t" \
    "v_cvt_pk_bf16_f32 v99, v62, v63\n\t" \
    "s_setprio 3\n\t" \
    "v_mfma_f32_32x32x16_bf16 v[48:63], v[72:75], v[64:67], v[16:31]\n\t" \
    "s_setprio 2\n\t" \
    "v_exp_f32 v32, v32\n\t" \
    "ds_read_b128 v[68:71], %[kl] offset:13824\n\t" \
    "v_exp_f32 v33, v33\n\t" \
    "v_exp_f32 v34, v34\n\t" \
    "v_exp_f32 v35, v35\n\t" \
    "v_exp_f32 v36, v36\n\t" \
    "v_exp_f32 v37, v37\n\t" \
    "v_exp_f32 v38, v38\n\t" \
    "s_setprio 3\n\t" \
    "v_mfma_f32_32x32x16_bf16 v[0:15], v[88:91], v[96:99], v[0:15]\n\t" \
    "s_setprio 2\n\t" \
    "v_exp_f32 v39, v39\n\t" \
    "ds_read_b128 v[84:87], %[v0l] offset:11520\n\t" \
    "v_cvt_pk_bf16_f32 v92, v32, v33\n\t" \
    "v_cvt_pk_bf16_f32 v93, v34, v35\n\t" \
    "v_cvt_pk_bf16_f32 v94, v36, v37\n\t" \
    "v_cvt_pk_bf16_f32 v95, v38, v39\n\t" \
    "v_exp_f32 v40, v40\n\t" \
    "ds_read_b128 v[88:91], %[v1l] offset:11520\n\t" \
    "v_exp_f32 v41, v41\n\t" \
    "v_exp_f32 v42, v42\n\t" \
    "v_exp_f32 v43, v43\n\t" \
    "s_waitcnt lgkmcnt(4)\n\t" \
    "s_setprio 3\n\t" \
    "v_mfma_f32_32x32x16_bf16 v[0:15], v[76:79], v[92:95], v[0:15]\n\t" \
    "s_setprio 2\n\t" \
    "v_exp_f32 v44, v44\n\t" \
    "v_exp_f32 v45, v45\n\t" \
    "v_exp_f32 v46, v46\n\t" \
    "v_exp_f32 v47, v47\n\t" \
    "s_waitcnt lgkmcnt(2)\n\t" \
    "v_cvt_pk_bf16_f32 v96, v40, v41\n\t" \
    "v_cvt_pk_bf16_f32 v97, v42, v43\n\t" \
    "v_cvt_pk_bf16_f32 v98, v44, v45\n\t" \
    "v_cvt_pk_bf16_f32 v99, v46, v47\n\t" \
    "s_setprio 3\n\t" \
    "v_mfma_f32_32x32x16_bf16 v[32:47], v[68:71], v[64:67], v[16:31]\n\t" \
    "s_setprio 1\n\t" \
    "v_exp_f32 v48, v48\n\t" \
    "ds_read_b128 v[72:75], %[kl] offset:16128\n\t" \
    "s_and_b32 %[t0], %[cnt], 3\n\t" \
    "s_cmp_eq_u32 %[t0], 3\n\t" \
    "s_mov_b32 %[dl], 18432\n\t" \
    "s_cselect_b32 %[dl], -55296, %[dl]\n\t" \
    "v_add_u32 %[kl], %[dl], %[kl]\n\t" \
    "v_exp_f32 v49, v49\n\t" \
    "v_exp_f32 v50, v50\n\t" \
    "v_exp_f32 v51, v51\n\t" \
    "v_exp_f32 v52, v52\n\t" \
    "v_exp_f32 v53, v53\n\t" \
    "v_exp_f32 v54, v54\n\t" \
    "s_setprio 3\n\t" \
    "v_mfma_f32_32x32x16_bf16 v[0:15], v[80:83], v[96:99], v[0:15]\n\t" \
    "s_setprio 1\n\t" \
    "v_exp_f32 v55, v55\n\t" \
    "ds_read_b128 v[76:79], %[v0l] offset:13824\n\t" \
    "v_cvt_pk_bf16_f32 v92, v48, v49\n\t" \
    "v_cvt_pk_bf16_f32 v93, v50, v51\n\t" \
    "v_cvt_pk_bf16_f32 v94, v52, v53\n\t" \
    "v_cvt_pk_bf16_f32 v95, v54, v55\n\t" \
    "v_exp_f32 v56, v56\n\t" \
    "ds_read_b128 v[80:83], %[v1l] offset:13824\n\t" \
    "v_exp_f32 v57, v57\n\t" \
    "v_exp_f32 v58, v58\n\t" \
    "v_exp_f32 v59, v59\n\t" \
    "s_waitcnt lgkmcnt(4)\n\t" \
    "s_setprio 3\n\t" \
    "v_mfma_f32_32x32x16_bf16 v[0:15], v[84:87], v[92:95], v[0:15]\n\t" \
    "s_setprio 1\n\t" \
    "v_exp_f32 v60, v60\n\t" \
    "v_exp_f32 v61, v61\n\t" \
    "v_exp_f32 v62, v62\n\t" \
    "v_exp_f32 v63, v63\n\t" \
    "s_waitcnt lgkmcnt(2)\n\t" \
    "v_cvt_pk_bf16_f32 v96, v56, v57\n\t" \
    "v_cvt_pk_bf16_f32 v97, v58, v59\n\t" \
    "v_cvt_pk_bf16_f32 v98, v60, v61\n\t" \
    "v_cvt_pk_bf16_f32 v99, v62, v63\n\t" \
    "s_cmp_eq_u32 %[cnt], 7\n\t" \
    "s_cbranch_scc1 Lskip%=\n\t" \
    "s_cmp_eq_u32 %[cnt], 6\n\t" \
    "s_cbranch_scc1 Lw0%=\n\t" \
    "s_waitcnt vmcnt(2)\n\t" \
    "s_barrier\n\t" \
    "s_cmp_gt_u32 %[cnt], 4\n\t" \
    "s_cbranch_scc1 Lskip%=\n\t" \
    "s_add_u32 %[t0], %[cnt], 3\n\t" \
    "s_and_b32 %[t0], %[t0], 3\n\t" \
    "s_mul_i32 %[t0], %[t0], 18432\n\t" \
    "s_add_u32 m0, %[t0], %[dst]\n\t" \
    "s_setprio 3\n\t" \
    "global_load_lds_dwordx4 %[goff], %[base]\n\t" \
    "global_load_lds_dwordx4 %[goff], %[base] offset:1024\n\t" \
    "s_setprio 1\n\t" \
    "v_add_u32 %[goff], 0x4000, %[goff]\n\t" \
    "s_branch Lskip%=\n\t" \
    "Lw0%=:\n\t" \
    "s_waitcnt vmcnt(0)\n\t" \
    "s_barrier\n\t" \
    "Lskip%=:\n\t" \
    "s_setprio 3\n\t" \
    "v_mfma_f32_32x32x16_bf16 v[48:63], v[72:75], v[64:67], v[16:31]\n\t" \
    "s_setprio 0\n\t" \
    "v_exp_f32 v32, v32\n\t" \
    "ds_read_b128 v[68:71], %[kl] offset:0\n\t" \
    "v_exp_f32 v33, v33\n\t" \
    "v_exp_f32 v34, v34\n\t" \
    "v_exp_f32 v35, v35\n\t" \
    "v_exp_f32 v36, v36\n\t" \
    "v_exp_f32 v37, v37\n\t" \
    "v_exp_f32 v38, v38\n\t" \
    "s_setprio 3\n\t" \
    "v_mfma_f32_32x32x16_bf16 v[0:15], v[88:91], v[96:99], v[0:15]\n\t" \
    "s_setprio 0\n\t" \
    "v_exp_f32 v39, v39\n\t" \
    "ds_read_b128 v[84:87], %[v0l] offset:16128\n\t" \
    "v_cvt_pk_bf16_f32 v92, v32, v33\n\t" \
    "v_cvt_pk_bf16_f32 v93, v34, v35\n\t" \
    "v_cvt_pk_bf16_f32 v94, v36, v37\n\t" \
    "v_cvt_pk_bf16_f32 v95, v38, v39\n\t" \
    "v_exp_f32 v40, v40\n\t" \
    "ds_read_b128 v[88:91], %[v1l] offset:16128\n\t" \
    "v_add_u32 %[v0l], %[dl], %[v0l]\n\t" \
    "v_add_u32 %[v1l], %[dl], %[v1l]\n\t" \
    "v_exp_f32 v41, v41\n\t" \
    "v_exp_f32 v42, v42\n\t" \
    "v_exp_f32 v43, v43\n\t" \
    "s_waitcnt lgkmcnt(4)\n\t" \
    "s_setprio 3\n\t" \
    "v_mfma_f32_32x32x16_bf16 v[0:15], v[76:79], v[92:95], v[0:15]\n\t" \
    "s_setprio 0\n\t" \
    "v_exp_f32 v44, v44\n\t" \
    "v_exp_f32 v45, v45\n\t" \
    "v_exp_f32 v46, v46\n\t" \
    "v_exp_f32 v47, v47\n\t" \
    "s_waitcnt lgkmcnt(2)\n\t" \
    "v_cvt_pk_bf16_f32 v96, v40, v41\n\t" \
    "v_cvt_pk_bf16_f32 v97, v42, v43\n\t" \
    "v_cvt_pk_bf16_f32 v98, v44, v45\n\t" \
    "v_cvt_pk_bf16_f32 v99, v46, v47\n\t" \
    "s_setprio 3\n\t" \
    "v_mfma_f32_32x32x16_bf16 v[32:47], v[68:71], v[64:67], v[16:31]\n\t" \
    "s_setprio 0\n\t" \
    "v_exp_f32 v48, v48\n\t" \
    "ds_read_b128 v[72:75], %[kl] offset:2304\n\t" \
    "v_exp_f32 v49, v49\n\t" \
    "v_exp_f32 v50, v50\n\t" \
    "v_exp_f32 v51, v51\n\t" \
    "v_exp_f32 v52, v52\n\t" \
    "v_exp_f32 v53, v53\n\t" \
    "v_exp_f32 v54, v54\n\t" \
    "s_setprio 3\n\t" \
    "v_mfma_f32_32x32x16_bf16 v[0:15], v[80:83], v[96:99], v[0:15]\n\t" \
    "s_setprio 0\n\t" \
    "v_exp_f32 v55, v55\n\t" \
    "ds_read_b128 v[76:79], %[v0l] offset:0\n\t" \
    "v_cvt_pk_bf16_f32 v92, v48, v49\n\t" \
    "v_cvt_pk_bf16_f32 v93, v50, v51\n\t" \
    "v_cvt_pk_bf16_f32 v94, v52, v53\n\t" \
    "v_cvt_pk_bf16_f32 v95, v54, v55\n\t" \
    "v_exp_f32 v56, v56\n\t" \
    "ds_read_b128 v[80:83], %[v1l] offset:0\n\t" \
    "v_exp_f32 v57, v57\n\t" \
    "v_exp_f32 v58, v58\n\t" \
    "v_exp_f32 v59, v59\n\t" \
    "s_waitcnt lgkmcnt(4)\n\t" \
    "s_setprio 3\n\t" \
    "v_mfma_f32_32x32x16_bf16 v[0:15], v[84:87], v[92:95], v[0:15]\n\t" \
    "s_setprio 0\n\t" \
    "v_exp_f32 v60, v60\n\t" \
    "v_exp_f32 v61, v61\n\t" \
    "v_exp_f32 v62, v62\n\t" \
    "v_exp_f32 v63, v63\n\t" \
    "s_waitcnt lgkmcnt(2)\n\t" \
    "v_cvt_pk_bf16_f32 v96, v56, v57\n\t" \
    "v_cvt_pk_bf16_f32 v97, v58, v59\n\t" \
    "v_cvt_pk_bf16_f32 v98, v60, v61\n\t" \
    "v_cvt_pk_bf16_f32 v99, v62, v63\n\t" \
    "s_add_u32 %[cnt], %[cnt], 1\n\t" \
    "s_cmp_lt_u32 %[cnt], 8\n\t" \
    "s_cbranch_scc1 Lloop%=\n\t" \
    "s_waitcnt lgkmcnt(0)\n\t" \
    "s_setprio 3\n\t" \
    "v_mfma_f32_32x32x16_bf16 v[0:15], v[88:91], v[96:99], v[0:15]\n\t" \
    "s_setprio 0\n\t" \
    "s_mov_b32 m0, %[sv]\n\t" \
    "s_setprio 0\n\t" \
    "s_nop 7\n\t" \
    "s_nop 7\n\t"
#define ATTN_CLOBBERS "v32", "v33", "v34", "v35", "v36", "v37", "v38", "v39", "v40", "v41", "v42", "v43", "v44", "v45", "v46", "v47", "v48", "v49", "v50", "v51", "v52", "v53", "v54", "v55", "v56", "v57", "v58", "v59", "v60", "v61", "v62", "v63", "v68", "v69", "v70", "v71", "v72", "v73", "v74", "v75", "v76", "v77", "v78", "v79", "v80", "v81", "v82", "v83", "v84", "v85", "v86", "v87", "v88", "v89", "v90", "v91", "v92", "v93", "v94", "v95", "v96", "v97", "v98", "v99"
#define ATTN_INSTR 0

DI void attn_block(char* __restrict__ ws, char* lds, int head, int chunk, int tid) {
    const int l = tid & 63, w = __builtin_amdgcn_readfirstlane(tid >> 6), h = l >> 5;
    const int qtile = chunk * 8 + w;
    const unsigned lds0 = (unsigned)(uintptr_t)lds;
    const char* kvbase = ws + WS_KV + (size_t)head * 64 * 2048;
    const unsigned dst = lds0 + w * AT_TS;
    __builtin_amdgcn_s_setprio(3);
    bf16x8 qf; float km, gate;
    asm volatile("global_load_dwordx4 %0, %3, off\n\tglobal_load_dword %1, %4, off\n\tglobal_load_dword %2, %5, off"
                 : "=&v"(qf), "=&v"(km), "=&v"(gate)
                 : "v"(ws + WS_QF + ((size_t)(head * 64 + qtile) * 1024) + l * 16), "v"((const float*)(ws + WS_KMAX) + l * 32 + head),
                   "v"((const float*)(ws + WS_GATE) + qtile * 32 + (l & 31)) : "memory");
    auto issue3 = [&]() {
#pragma unroll
        for (int c = 0; c < 3; ++c) { glds16(kvbase + c * 16384 + w * 2048 + l * 16, dst + c * AT_SLOT); glds16(kvbase + c * 16384 + w * 2048 + 1024 + l * 16, dst + c * AT_SLOT + 1024); }
    };
    issue3();
    {
        const u32x4 ones = {0x3f803f80u, 0x3f803f80u, 0x3f803f80u, 0x3f803f80u};
        *(LDS_AS u32x4*)((lds_ptr)lds + (tid >> 4) * AT_TS + 2048 + (tid & 15) * 16) = ones;
        if (tid == 0) *(LDS_AS unsigned*)((lds_ptr)lds + 4 * AT_SLOT) = 0u;
    }
    asm volatile("s_waitcnt vmcnt(6)" : "+v"(qf), "+v"(km), "+v"(gate));
    float qn = 0.f;
    { const u32x4 qu = __builtin_bit_cast(u32x4, qf);
#pragma unroll
      for (int e = 0; e < 4; ++e) { qn = __builtin_fmaf(bf_lo(qu[e]), bf_lo(qu[e]), qn); qn = __builtin_fmaf(bf_hi(qu[e]), bf_hi(qu[e]), qn); } }
    qn = swap32_sum(qn);
    float ref = __builtin_amdgcn_sqrtf(qn * wave_max_fast(km));
    unsigned kl = lds0 + l * 16;
    const bool onesl = (l & 16) != 0;
    const unsigned ol = lds0 + 2048 + ((((l & 15) >= 4) && ((l & 15) < 12)) ? 64 : 0);
    unsigned v0l = onesl ? ol : lds0 + 1024 + h * 256 + (l & 15) * 16, v1l = onesl ? ol : v0l + 512;
    LDS_AS volatile unsigned* flag = (LDS_AS volatile unsigned*)((lds_ptr)lds + 4 * AT_SLOT);
    const size_t ooff = WS_HO + ((size_t)(qtile * 88 + head) * 1024) + l * 16;
#pragma unroll 1
    for (int pass = 0;; ++pass) {
        f32x16 negm, o;
#pragma unroll
        for (int r = 0; r < 16; ++r) negm[r] = -ref;
        unsigned sv, cnt, t0, dl, goff = 3 * 16384 + w * 2048 + l * 16;
        STAMP(2, 1);
        asm volatile(ATTN_ASM
                     : "=&{v[0:15]}"(o), [sv] "=&s"(sv), [cnt] "=&s"(cnt), [t0] "=&s"(t0), [dl] "=&s"(dl), [goff] "+v"(goff), [kl] "+v"(kl), [v0l] "+v"(v0l), [v1l] "+v"(v1l)
                     : "{v[16:31]}"(negm), "{v[64:67]}"(qf), [dst] "s"(dst), [base] "s"(kvbase)
                     : ATTN_CLOBBERS, "scc", "memory");
        STAMP(2, 3);
        st16_wt(ws, ooff, pack8(o, 0, gate * __builtin_amdgcn_rcpf(o[8])));
        drain_stores();
        if (pass == 1) { __syncthreads(); break; }
        const bool bad = !(o[8] >= 9.094947e-13f);
        if (__any(bad) && l == 0) *flag = 1u;
        __syncthreads();
        if (*flag == 0u) break;
        float m = -3.0e38f;
        for (int t = 0; t < 64; ++t) {
            const bf16x8 kf = *(const bf16x8*)(kvbase + (size_t)t * 2048 + l * 16);
            const f32x16 s = MFMA(kf, qf, f32x16{});
#pragma unroll
            for (int r = 0; r < 16; ++r) m = fmaxf(m, s[r]);
        }
        ref = swap32_max(m);
        asm volatile("s_waitcnt vmcnt(0)" ::: "memory");
        issue3();
    }
    if (tid == 0) __hip_atomic_fetch_add(sync_word(ws, SY_O + chunk), 1u, RLX_AGENT);
}
constexpr int SP_ST = 24576, SP_LDS = 3 * SP_ST + 256 + 4096, FN_ST = 16384;

template <int N> DI void wait_vm() { asm volatile("s_waitcnt vmcnt(%0)" :: "i"(N) : "memory"); }

DI void gate_block(const u16* __restrict__ x, const u16* __restrict__ w_in, const u16* __restrict__ w_sp, const u16* __restrict__ b_sp,
                   const u16* __restrict__ wo, const u16* __restrict__ w_out, char* __restrict__ ws, u16* __restrict__ out, char* lds, int g, int tid) {
    const int l = tid & 63, w = __builtin_amdgcn_readfirstlane(tid >> 6), h = l >> 5, r32 = l & 31;
    const int tl = (g >> 3) & 7, tt = (g & 3) * 8 + tl, ft = ((g >> 2) & 1) * 4 + (g >> 6);
    const int rot = SP_ROT(tl, g >> 6);
    const unsigned lds0 = (unsigned)(uintptr_t)lds;
    const int q4 = (l & 15) >> 2, p4 = l & 3, b16 = (l >> 4) & 1;
    const unsigned lo = l * 16;
    if (tid == 0) TL(5, 0, 99);
    if (GPRIO) __builtin_amdgcn_s_setprio(GPRIO);
    const int j = w - 4, lrow = 8 * j + (l >> 3);
    const unsigned xv = (unsigned)(lrow * D_ + ((l & 7) ^ ((lrow >> 1) & 7)) * 8) * 2;
    const int wr = 4 * j + (l >> 4), wc = ((((l & 15) >> 2) ^ (wr & 3)) << 2) | (l & 3);
    const unsigned wv = (unsigned)(wr * 1792 + wc * 8) * 2;
    auto issue_z = [&](int s) {
        const char* xu = (const char*)(x + (size_t)tt * 64 * D_); const char* wu = (const char*)(w_in + ft * 128);
        const unsigned dst = lds0 + (s % 3) * SP_ST + j * 1024; const int sr = (s + rot) & 7;
        glds16s(xu + sr * 128, xv, dst); glds16s(xu + 32 * D_ * 2 + sr * 128, xv, dst + 4096);
#pragma unroll
        for (int i = 0; i < 4; ++i) glds16s(wu + (size_t)(sr * 64 + 16 * i) * 3584, wv, dst + 8192 + i * 4096);
    };
    STAMP(5, 1);
    if (w >= 4) {
        if (LPRIO != GPRIO) __builtin_amdgcn_s_setprio(LPRIO);
        const int ttile = g >> 2, fb0 = (g & 3) * 7 + j, fb1 = fb0 + (j < 3 ? 4 : 0);
        if (ft < 7) { issue_z(0); issue_z(1); issue_z(2); }
        const char* z2 = ws + WS_Z2 + (size_t)ttile * 56 * 1024 + lo;
        const u32x4 za0 = *(const u32x4*)(z2 + (size_t)(2 * fb0) * 1024), za1 = *(const u32x4*)(z2 + (size_t)(2 * fb0 + 1) * 1024);
        const u32x4 zb0 = *(const u32x4*)(z2 + (size_t)(2 * fb1) * 1024), zb1 = *(const u32x4*)(z2 + (size_t)(2 * fb1 + 1) * 1024);
        f32x4 pv[7];
        { const f32x4* sp = (const f32x4*)(ws + WS_STATS) + (size_t)(ttile * 32 + r32) * 14 + h * 7;
#pragma unroll
          for (int i = 0; i < 7; ++i) pv[i] = sp[i]; }
        char* scr = lds + 3 * SP_ST + 256 + j * 1024;
        SB();
        if (ft < 7) {
            LnStat st;
            wait_bar<23>();
#pragma unroll 1
            for (int s = 0; s < 7; ++s) {
                if (s == 0) wait_bar<17>(); else if (s + 2 < 8) wait_bar<6>(); else wait_bar<0>();
                if (s == 4 && tid == 256) __hip_atomic_fetch_add(sync_word(ws, SY_LN + (g & 15)), 1u, RLX_AGENT);
                if (s + 3 < 8) issue_z(s + 3);
                if (s == 1) { st = ln_stats(pv); ln_block(ws, scr, ttile, fb0, za0, za1, st, l); }
                if (s == 2 && j < 3) ln_block(ws, scr, ttile, fb1, zb0, zb1, st, l);
            }
        } else {
            const LnStat st = ln_stats(pv);
            ln_block(ws, scr, ttile, fb0, za0, za1, st, l);
            if (j < 3) ln_block(ws, scr, ttile, fb1, zb0, zb1, st, l);
        }
        drain_stores();
    }
    if (ft >= 7) { bar_only(); if (tid == 256) __hip_atomic_fetch_add(sync_word(ws, SY_LN + (g & 15)), 1u, RLX_AGENT); }
    if (ft < 7) {
        if (w >= 4) {
            bar_only();
            if (j == 3) {
                wait_words(ws, SY_LN, 16, 16u, l);
                const char* zu = ws + WS_Z2N + (size_t)(ft * 4) * 128 * 1024;
                const unsigned pv = (unsigned)((tl >> 1) * 128 * 1024 + ((tl & 1) * 16 + (l & 15)) * 128);
                auto pf = [&](int s4) { const int sr = (4 * s4 + (l >> 4) + rot) & 31; prefetch_line(zu, pv + sr * 4096, lds0 + 3 * SP_ST); };
                pf(0); pf(1); pf(2);
                bar_only();
                bar_only();
#pragma unroll 1
                for (int s = 0; s < 31; ++s) { bar_only(); if ((s & 3) == 0 && s + 12 < 32) pf((s >> 2) + 3); }
                drain_stores();
            } else {
                const unsigned a0 = (unsigned)((l >> 3) * S_ * 2 + (((l & 7) ^ ((l >> 4) & 7)) << 4)), a1 = a0 ^ 64u;
                const unsigned va = (j & 1) ? a1 : a0, vb = (j & 1) ? a0 : a1;
                const char* au = (const char*)(w_sp + (size_t)(tt * 64 + 8 * j) * S_);
                auto issue = [&](int s) {
                    const unsigned dst = lds0 + (s % 6) * 8192 + j * 1024; const int sr = (s + rot) & 31;
                    glds16s(au + sr * 128, va, dst); glds16s(au + 24 * S_ * 2 + sr * 128, vb, dst + 3072);
                    if (j < 2) glds16s(au + 48 * S_ * 2 + sr * 128, va, dst + 6144);
                };
                auto waitst = [&](int younger) {
                    if (j < 2) { if (younger >= 4) wait_vm<12>(); else if (younger == 3) wait_vm<9>(); else if (younger == 2) wait_vm<6>(); else if (younger == 1) wait_vm<3>(); else wait_vm<0>(); }
                    else { if (younger >= 4) wait_vm<8>(); else if (younger == 3) wait_vm<6>(); else if (younger == 2) wait_vm<4>(); else if (younger == 1) wait_vm<2>(); else wait_vm<0>(); }
                };
#pragma unroll 1
                for (int s = 0; s < 5; ++s) issue(s);
                bar_only();
                waitst(4); bar_only();
                issue(5);
#pragma unroll 1
                for (int s = 0; s < 31; ++s) {
                    waitst(min(s + 5, 31) - (s + 1)); bar_only();
                    if (s + 6 < 32) issue(s + 6);
                }
            }
            bar_only();
        } else {
            const int fb = w;
            int boff[4];
#pragma unroll
            for (int ks = 0; ks < 4; ++ks) { const int f = (r32 >> 1) & 7; boff[ks] = r32 * 128 + (((ks * 2 + h) ^ f) << 4); }
            const int fbg = ft * 4 + fb, fg = 2 * fbg;
            const float gt0 = ((const float*)(ws + WS_GATE))[(tt * 2) * 32 + r32], gt1 = ((const float*)(ws + WS_GATE))[(tt * 2 + 1) * 32 + r32];
            const float bs0 = __uint_as_float((unsigned)b_sp[(tt * 2) * 32 + r32] << 16), bs1 = __uint_as_float((unsigned)b_sp[(tt * 2 + 1) * 32 + r32] << 16);
            LDS_AS u32x4* z1p = (LDS_AS u32x4*)((lds_ptr)lds + 49152 + w * 4096) + l;
            {
                f32x16 acc[2]; acc[0] = f32x16{}; acc[1] = f32x16{};
                const int zoff = 8192 + (8 * h + q4) * 256 + ((fb ^ q4) << 6) + b16 * 32 + p4 * 8;
                bf16x8 aa[2], ba[2][2], ab[2], bb[2][2];
                auto rdh = [&](int s, int half, bf16x8 (&af)[2], bf16x8 (&bf)[2][2]) {
                    lds_cptr base = (lds_cptr)lds + (s % 3) * SP_ST;
#pragma unroll
                    for (int kk = 0; kk < 2; ++kk) {
                        const int ks = 2 * half + kk;
                        af[kk] = trfrag(base + zoff + ks * 4096, base + zoff + ks * 4096 + 1024);
                        bf[0][kk] = ldsfrag(base + boff[ks]); bf[1][kk] = ldsfrag(base + boff[ks] + 4096);
                    }
                };
                auto mm = [&](bf16x8 (&af)[2], bf16x8 (&bf)[2][2]) {
#pragma unroll
                    for (int kk = 0; kk < 2; ++kk) { acc[0] = MFMA(af[kk], bf[0][kk], acc[0]); acc[1] = MFMA(af[kk], bf[1][kk], acc[1]); }
                };
                bar_only();
                rdh(0, 0, aa, ba);
#pragma unroll 1
                for (int s = 0; s < 7; ++s) {
                    rdh(s, 1, ab, bb); mm(aa, ba); interleave<4, 2>(); SB();
                    bar_only();
                    rdh(s + 1, 0, aa, ba); mm(ab, bb); interleave<4, 2>(); SB();
                }
                rdh(7, 1, ab, bb); mm(aa, ba); interleave<4, 2>(); SB();
                mm(ab, bb);
                bar_only();
                if (tid == 0) TL(5, 0, 101);
#pragma unroll
                for (int tb = 0; tb < 2; ++tb) {
                    const float og = 1.0f - (tb ? gt1 : gt0);
#pragma unroll
                    for (int gq = 0; gq < 2; ++gq) {
                        u32x4 zv;
#pragma unroll
                        for (int e = 0; e < 4; ++e) zv[e] = cvtpk(gelu_tanh(acc[tb][8 * gq + 2 * e]) * og, gelu_tanh(acc[tb][8 * gq + 2 * e + 1]) * og);
                        z1p[(tb * 2 + gq) * 64] = zv;
                    }
                }
            }
            f32x16 acc[2]; acc[0] = f32x16{}; acc[1] = f32x16{};
            {
                if (tid == 0) TL(5, 0, 102);
                bar_only();
                if (tid == 0) TL(5, 0, 103);
                bf16x8 za[4], zb[4];
                const size_t zoff = WS_Z2N + (size_t)fbg * 128 * 1024 + lo;
                auto ldz = [&](int s, int ks) { const int sr = (min(s, 31) + rot) & 31; return ld16_dev(ws, zoff + (size_t)((sr * 4 + ks) * 1024)); };
#pragma unroll
                for (int ks = 0; ks < 4; ++ks) za[ks] = ldz(0, ks);
#pragma unroll
                for (int ks = 0; ks < 4; ++ks) zb[ks] = ldz(1, ks);
                bf16x8 bx[2][2], by[2][2];
                auto rdh = [&](int s, int half, bf16x8 (&bf)[2][2]) {
                    lds_cptr base = (lds_cptr)lds + (s % 6) * 8192;
#pragma unroll
                    for (int kk = 0; kk < 2; ++kk) { bf[0][kk] = ldsfrag(base + boff[2 * half + kk]); bf[1][kk] = ldsfrag(base + boff[2 * half + kk] + 4096); }
                };
                auto stage = [&](int s, bf16x8 (&z)[4], bool last) {
                    rdh(s, 1, by);
                    acc[0] = MFMA(z[0], bx[0][0], acc[0]); acc[1] = MFMA(z[0], bx[1][0], acc[1]);
                    acc[0] = MFMA(z[1], bx[0][1], acc[0]); acc[1] = MFMA(z[1], bx[1][1], acc[1]); interleave<4, 1>(); SB();
                    z[0] = ldz(s + 2, 0); z[1] = ldz(s + 2, 1); SB();
                    if (!last) { bar_only(); rdh(s + 1, 0, bx); }
                    acc[0] = MFMA(z[2], by[0][0], acc[0]); acc[1] = MFMA(z[2], by[1][0], acc[1]);
                    acc[0] = MFMA(z[3], by[0][1], acc[0]); acc[1] = MFMA(z[3], by[1][1], acc[1]); interleave<4, 1>(); SB();
                    z[2] = ldz(s + 2, 2); z[3] = ldz(s + 2, 3); SB();
                };
                bar_only();
                rdh(0, 0, bx);
#pragma unroll 1
                for (int s = 0; s < 30; s += 2) { stage(s, za, false); stage(s + 1, zb, false); }
                stage(30, za, false); stage(31, zb, true);
                if (tid == 0) TL(5, 0, 104);
            }
#pragma unroll
            for (int tb = 0; tb < 2; ++tb) {
                const float bs = tb ? bs1 : bs0;
#pragma unroll
                for (int gq = 0; gq < 2; ++gq) {
                    u32x4 ov; const u32x4 zv = z1p[(tb * 2 + gq) * 64];
#pragma unroll
                    for (int e = 0; e < 4; ++e) ov[e] = cvtpk((acc[tb][8 * gq + 2 * e] + bs) * bf_lo(zv[e]), (acc[tb][8 * gq + 2 * e + 1] + bs) * bf_hi(zv[e]));
                    st16_wt(ws, WS_HO + ((size_t)((tt * 2 + tb) * 88 + 32 + fg + gq) * 1024) + lo, ov);
                }
            }
            drain_stores();
            bar_only();
            if (tid == 0) __hip_atomic_fetch_add(sync_word(ws, SY_H + tt), 1u, RLX_AGENT);
        }
    }
    STAMP(5, 3);
    if (tid == 0) TL(5, 0, 105);
    {
        if (GPRIO) __builtin_amdgcn_s_setprio(GPRIO);
        const int tb = w & 1, kq = w >> 1;
        const int prow = 8 * w + (l >> 3);
        const unsigned pv = (unsigned)(prow * D_ + ((l & 7) ^ (((prow >> 1) & 1) << 2)) * 8) * 2;
        const char* w0 = (const char*)(w_out + ft * 64);
        const char* w1 = (const char*)(wo + ft * 64);
        auto issue = [&](int s) {
            const unsigned dst = lds0 + (s & 3) * FN_ST + w * 1024;
            const char* wp = s < 7 ? w0 + (size_t)s * 128 * D_ * 2 : w1 + (size_t)(s - 7) * 128 * D_ * 2;
            glds16s(wp, pv, dst); glds16s(wp + (size_t)64 * D_ * 2, pv, dst + 8192);
        };
        issue(0); issue(1); issue(2); issue(3);
        const int woff0 = (kq * 32 + 4 * h + q4) * 128 + (((b16 * 2 + (p4 >> 1)) ^ ((q4 >> 1) << 2)) << 4) + (p4 & 1) * 8, woff1 = woff0 ^ 64;
        const size_t hoff = WS_HO + ((size_t)((tt * 2 + tb) * 88 + 2 * kq) * 1024) + lo;
        f32x16 acc[2]; acc[0] = f32x16{}; acc[1] = f32x16{};
        bf16x8 hf[14];
        auto comp = [&](int s, const bf16x8& h0, const bf16x8& h1) {
            lds_cptr base = (lds_cptr)lds + (s & 3) * FN_ST;
            const bf16x8 w00 = trfrag(base + woff0, base + woff0 + 1024), w01 = trfrag(base + woff1, base + woff1 + 1024);
            const bf16x8 w10 = trfrag(base + woff0 + 2048, base + woff0 + 3072), w11 = trfrag(base + woff1 + 2048, base + woff1 + 3072);
            acc[0] = MFMA(w00, h0, acc[0]); acc[1] = MFMA(w01, h0, acc[1]);
            acc[0] = MFMA(w10, h1, acc[0]); acc[1] = MFMA(w11, h1, acc[1]);
            SB();
        };
        if (w == 7) wait_words(ws, SY_H + tt, 1, 7u, l);
        if (w == 0) TL(5, 1, 99);
        bar_only();
        if (w == 0) TL(5, 1, 100);
#pragma unroll
        for (int q = 0; q < 14; ++q) hf[q] = ld16_dev(ws, hoff + (size_t)((32 + 8 * (q >> 1) + (q & 1)) * 1024));
        wait_vm<20>(); bar_only();            comp(0, hf[0], hf[1]);
        wait_vm<18>(); bar_only(); issue(4);  comp(1, hf[2], hf[3]);
        wait_vm<18>(); bar_only(); issue(5);  comp(2, hf[4], hf[5]);
        wait_vm<18>(); bar_only(); issue(6);  comp(3, hf[6], hf[7]);
        wait_vm<4>();  bar_only(); issue(7);  comp(4, hf[8], hf[9]);
        wait_vm<4>();  bar_only(); issue(8);  comp(5, hf[10], hf[11]);
        wait_vm<4>();  bar_only(); issue(9);  comp(6, hf[12], hf[13]);
        if (w == 0) TL(5, 1, 101);
        if (w == 7) wait_words(ws, SY_O + (tt >> 2), 1, 32u, l);
        bar_only();
        if (w == 0) TL(5, 1, 102);
#pragma unroll
        for (int q = 0; q < 8; ++q) hf[q] = ld16_dev(ws, hoff + (size_t)((8 * (q >> 1) + (q & 1)) * 1024));
        wait_vm<12>(); bar_only(); issue(10); comp(7, hf[0], hf[1]);
        wait_vm<12>(); bar_only();            comp(8, hf[2], hf[3]);
        wait_vm<10>(); bar_only();            comp(9, hf[4], hf[5]);
        wait_vm<0>();  bar_only();            comp(10, hf[6], hf[7]);
        if (w == 0) TL(5, 1, 103);
        bar_only();
        LDS_AS f32x4* red = (LDS_AS f32x4*)lds;
#pragma unroll
        for (int db = 0; db < 2; ++db)
#pragma unroll
            for (int q = 0; q < 4; ++q) red[((w * 2 + db) * 4 + q) * 64 + l] = f32x4{acc[db][4 * q], acc[db][4 * q + 1], acc[db][4 * q + 2], acc[db][4 * q + 3]};
        bar_only();
        const int db = kq & 1, gq = kq >> 1;
        f32x4 v0 = {0.f, 0.f, 0.f, 0.f}, v1 = v0;
#pragma unroll
        for (int q = 0; q < 4; ++q) { const int ww = q * 2 + tb; v0 += red[((ww * 2 + db) * 4 + 2 * gq) * 64 + l]; v1 += red[((ww * 2 + db) * 4 + 2 * gq + 1) * 64 + l]; }
        const unsigned a0 = cvtpk(v0[0], v0[1]), a1 = cvtpk(v0[2], v0[3]), b0 = cvtpk(v1[0], v1[1]), b1 = cvtpk(v1[2], v1[3]);
        auto r0 = __builtin_amdgcn_permlane32_swap(a0, b0, false, false);
        auto r1 = __builtin_amdgcn_permlane32_swap(a1, b1, false, false);
        const u32x4 ov = {r0[0], r1[0], r0[1], r1[1]};
        *(u32x4*)(out + (size_t)(tt * 64 + tb * 32 + r32) * D_ + ft * 64 + db * 32 + gq * 16 + h * 8) = ov;
        if (w == 0) TL(5, 1, 104);
    }
}

extern "C" __global__ __launch_bounds__(512, 4) void k_main(const u16* __restrict__ x, const u16* __restrict__ w_in, const u16* __restrict__ w_sp, const u16* __restrict__ b_sp,
                                                 const u16* __restrict__ wo, const u16* __restrict__ w_out, char* __restrict__ ws, u16* __restrict__ out) {
    __shared__ __attribute__((aligned(1024))) char lds[AT_LDS > SP_LDS ? AT_LDS : SP_LDS];
    const int b = blockIdx.x;
    if (b < 256) {
        STAMP(2, 0);
        attn_block(ws, lds, (b & 7) * 4 + ((b >> 3) & 3), b >> 5, threadIdx.x);
        STAMP(2, 2);
    } else {
        STAMP(5, 0);
        gate_block(x, w_in, w_sp, b_sp, wo, w_out, ws, out, lds, b - 256, threadIdx.x);
        STAMP(5, 2);
    }
}

extern "C" void kernel_launch(void* const* d_in, const int* in_sizes, int n_in, void* d_out, int out_size, void* d_ws, size_t ws_size, hipStream_t stream) {
    const u16* x = (const u16*)d_in[0];
    const u16* wqkv = (const u16*)d_in[1];
    const u16* wo = (const u16*)d_in[2];
    const u16* w_in = (const u16*)d_in[4];
    const u16* w_sp = (const u16*)d_in[5];
    const u16* b_sp = (const u16*)d_in[6];
    const u16* w_out = (const u16*)d_in[7];
    const u16* gate_w = (const u16*)d_in[8];
    char* ws = (char*)d_ws;
    k1_proj<<<256, 512, 0, stream>>>(x, wqkv, w_in, gate_w, w_sp, wo, w_out, (const u16*)d_out, ws);
    k_main<<<512, 512, 0, stream>>>(x, w_in, w_sp, b_sp, wo, w_out, ws, (u16*)d_out);
}
```

```cpp
#include <hip/hip_runtime.h>
#include <hip/hip_bf16.h>
#include <stdint.h>

typedef __attribute__((ext_vector_type(8))) short bf16x8;
typedef __attribute__((ext_vector_type(4))) short s16x4;
typedef __attribute__((ext_vector_type(16))) float f32x16;
typedef __attribute__((ext_vector_type(4))) unsigned u32x4;
typedef __attribute__((ext_vector_type(2))) float f32x2;
typedef __attribute__((ext_vector_type(4))) float f32x4;
typedef __attribute__((ext_vector_type(2))) __bf16 bf16x2_t;
#define LDS_AS __attribute__((address_space(3)))
typedef LDS_AS const char* lds_cptr;
typedef LDS_AS char* lds_ptr;
typedef short v4i16_t __attribute__((ext_vector_type(4)));
typedef unsigned short u16;
#define DI __device__ __forceinline__

#define MFMA(a, b, c) __builtin_amdgcn_mfma_f32_32x32x16_bf16((a), (b), (c), 0, 0, 0)
#define SB() __builtin_amdgcn_sched_barrier(0)
template <int NM, int ND> DI void interleave() {
#pragma unroll
    for (int i = 0; i < NM; ++i) { __builtin_amdgcn_sched_group_barrier(0x008, 1, 0); __builtin_amdgcn_sched_group_barrier(0x100, ND, 0); }
}
#define TL(kid, who, idx) do {} while (0)
#define WAIT_BAR(N) asm volatile("s_waitcnt vmcnt(" #N ") lgkmcnt(0)\n\ts_barrier" ::: "memory")

#define K1_ROT(mt, p) 0
#define SP_ROT(tl, fl) ((tl) * 4 + (fl) * 2)
#define PACE 1
#define PACE_() do { if (PACE == 1) __builtin_amdgcn_s_sleep(1); else if (PACE == 2) asm volatile("s_nop 7"); else if (PACE == 3) asm volatile("s_nop 11"); else if (PACE == 4) asm volatile("s_nop 15"); SB(); } while (0)
#define LPRIO 0
#define K1_WT 1
#define K1_ST16(off, v) st16_wt(ws, (off), (v))
#define MAIN_BYTES 16256
#define CODE_PF 1
#define SELF_PF 0
#define GPRIO 2
#define FN_ROT(i, dt) ((((i) & 3) * 3 + (dt)) % 11)
constexpr int S_ = 2048, D_ = 512, FH = 896;
constexpr float QSCALE = 0.25f * 1.4426950408889634f;

constexpr size_t WS_QF    = 0;
constexpr size_t WS_KV    = WS_QF + 32u * 64 * 1024;
constexpr size_t WS_Z2    = WS_KV + 32u * 64 * 2048;
constexpr size_t WS_Z2N   = WS_Z2 + 64u * 56 * 1024;
constexpr size_t WS_HO    = WS_Z2N + 28u * 128 * 1024;
constexpr size_t WS_STATS = WS_HO + 64u * 88 * 1024;
constexpr size_t WS_KMAX  = WS_STATS + 2048u * 28 * 8;
constexpr size_t WS_GATE  = WS_KMAX + 64u * 32 * 4;
constexpr size_t WS_SYNC  = WS_GATE + 2048u * 4;
constexpr int SY_LN = 0, SY_H = 16, SY_O = 48, SY_TMO = 56, SY_N = 57;
constexpr size_t WS_END   = WS_SYNC + SY_N * 256;

#define STAMP(kid, slot) do {} while (0)
DI unsigned cvtpk(float a, float b) { f32x2 f = {a, b}; bf16x2_t r = __builtin_convertvector(f, bf16x2_t); return __builtin_bit_cast(unsigned, r); }
DI float bf_lo(unsigned u) { return __uint_as_float(u << 16); }
DI float bf_hi(unsigned u) { return __uint_as_float(u & 0xffff0000u); }
DI void glds16(const void* g, unsigned lds_base) {
    unsigned sv;
    asm volatile("s_mov_b32 %0, m0\n\ts_mov_b32 m0, %2\n\ts_nop 0\n\tglobal_load_lds_dwordx4 %1, off\n\ts_mov_b32 m0, %0"
                 : "=&s"(sv) : "v"(g), "s"(lds_base) : "memory");
}
DI void glds16s(const void* ubase, unsigned voff, unsigned lds_base) {
    asm volatile("s_mov_b32 m0, %2\n\ts_nop 0\n\tglobal_load_lds_dwordx4 %0, %1" :: "v"(voff), "s"(ubase), "s"(lds_base) : "memory");
}
#define GAS __attribute__((address_space(1)))
typedef GAS unsigned gu32;
#define RLX_AGENT __ATOMIC_RELAXED, __HIP_MEMORY_SCOPE_AGENT
DI gu32* sync_word(char* ws, int i) { return (gu32*)(ws + WS_SYNC + (size_t)i * 256); }
DI void st16_wt(char* ws, size_t off, u32x4 v) {
    __builtin_amdgcn_raw_buffer_store_b128(v, __builtin_amdgcn_make_buffer_rsrc(ws, 0, (int)WS_END, 0x00020000), (int)off, 0, 16);
}
DI bf16x8 ld16_dev(char* ws, size_t off) {
    return __builtin_bit_cast(bf16x8, __builtin_amdgcn_raw_buffer_load_b128(__builtin_amdgcn_make_buffer_rsrc(ws, 0, (int)WS_END, 0x00020000), (int)off, 0, 16));
}
DI void prefetch_line(const void* ubase, unsigned voff, unsigned lds_dump) {
    asm volatile("s_mov_b32 m0, %2\n\ts_nop 0\n\tglobal_load_lds_dword %0, %1" :: "v"(voff), "s"(ubase), "s"(lds_dump) : "memory");
}
DI void prefetch_ptr(const void* g, unsigned lds_dump) {
    asm volatile("s_mov_b32 m0, %1\n\ts_nop 0\n\tglobal_load_lds_dword %0, off" :: "v"(g), "s"(lds_dump) : "memory");
}
DI void drain_stores() { asm volatile("s_waitcnt vmcnt(0)" ::: "memory"); }
DI void wait_words(char* ws, int first, int n, unsigned want, int lane) {
    gu32* p = sync_word(ws, first + lane % n);
    for (unsigned spins = 0;; ++spins) {
        if (__all(__hip_atomic_load(p, RLX_AGENT) >= want)) break;
        if (spins > (1u << 22)) { if (lane == 0) __hip_atomic_store(sync_word(ws, SY_TMO), 1u, RLX_AGENT); break; }
        __builtin_amdgcn_s_sleep(2);
    }
    asm volatile("" ::: "memory");
}
DI void bar_only() { asm volatile("" ::: "memory"); __builtin_amdgcn_s_waitcnt(0xC07F); __builtin_amdgcn_s_barrier(); asm volatile("" ::: "memory"); }
template <int N> DI void wait_bar() { asm volatile("s_waitcnt vmcnt(%0)" :: "i"(N) : "memory"); bar_only(); }
DI s16x4 vtr(lds_cptr p) { return __builtin_bit_cast(s16x4, __builtin_amdgcn_ds_read_tr16_b64_v4i16((LDS_AS v4i16_t*)p)); }
DI bf16x8 trfrag(lds_cptr p0, lds_cptr p1) { s16x4 lo = vtr(p0), hi = vtr(p1); return __builtin_shufflevector(lo, hi, 0, 1, 2, 3, 4, 5, 6, 7); }
DI bf16x8 ldsfrag(lds_cptr p) { return *(const LDS_AS bf16x8*)p; }
DI u32x4 pack8(const f32x16& a, int o, float sc) {
    u32x4 r; r[0] = cvtpk(a[o] * sc, a[o + 1] * sc); r[1] = cvtpk(a[o + 2] * sc, a[o + 3] * sc);
    r[2] = cvtpk(a[o + 4] * sc, a[o + 5] * sc); r[3] = cvtpk(a[o + 6] * sc, a[o + 7] * sc); return r;
}
DI float swap32_sum(float v) { auto r = __builtin_amdgcn_permlane32_swap(__float_as_uint(v), __float_as_uint(v), false, false); return __uint_as_float(r[0]) + __uint_as_float(r[1]); }
DI float swap32_max(float v) { auto r = __builtin_amdgcn_permlane32_swap(__float_as_uint(v), __float_as_uint(v), false, false); return fmaxf(__uint_as_float(r[0]), __uint_as_float(r[1])); }
DI float dpp_max16(float v) {
    v = fmaxf(v, __uint_as_float(__builtin_amdgcn_update_dpp(0u, __float_as_uint(v), 0x128, 0xf, 0xf, false)));
    v = fmaxf(v, __uint_as_float(__builtin_amdgcn_update_dpp(0u, __float_as_uint(v), 0x124, 0xf, 0xf, false)));
    v = fmaxf(v, __uint_as_float(__builtin_amdgcn_update_dpp(0u, __float_as_uint(v), 0x122, 0xf, 0xf, false)));
    v = fmaxf(v, __uint_as_float(__builtin_amdgcn_update_dpp(0u, __float_as_uint(v), 0x121, 0xf, 0xf, false)));
    return v;
}
DI float wave_max_fast(float v) {
    v = dpp_max16(v);
    auto r = __builtin_amdgcn_permlane16_swap(__float_as_uint(v), __float_as_uint(v), false, false);
    v = fmaxf(__uint_as_float(r[0]), __uint_as_float(r[1]));
    return swap32_max(v);
}

DI float gelu_tanh(float x) {
    const float c1 = -2.0f * 0.7978845608028654f * 1.4426950408889634f, c2 = c1 * 0.044715f;
    const float a = x * __builtin_fmaf(x * x, c2, c1);
    return x * __builtin_amdgcn_rcpf(1.0f + __builtin_amdgcn_exp2f(a));
}

constexpr int K1_ST = 40960, K1_LDS = 3 * K1_ST + 1024 + 256;
extern "C" __global__ void k_main(const u16* __restrict__ x, const u16* __restrict__ w_in, const u16* __restrict__ w_sp, const u16* __restrict__ b_sp,
                                  const u16* __restrict__ wo, const u16* __restrict__ w_out, char* __restrict__ ws, u16* __restrict__ out);

template <int TYPE>
DI void k1_body(const u16* __restrict__ x, const u16* __restrict__ wqkv, const u16* __restrict__ w_in, const u16* __restrict__ gate_w,
                char* __restrict__ ws, char* lds, int mt, int p, int tid) {
    if (tid == 0) TL(0, 1, 99);
    constexpr int NG = TYPE == 0 ? 3 : (TYPE == 1 ? 2 : 1);
    constexpr int NA = TYPE == 2 ? 1 : (TYPE == 3 ? 2 : NG);
    constexpr int P = 4 + 2 * NG;
    const int l = tid & 63, w = __builtin_amdgcn_readfirstlane(tid >> 6);
    const int rot = K1_ROT(mt, p);
    if (w >= 4) {
        const int j = w - 4;
        const unsigned lds0 = (unsigned)(uintptr_t)lds;
        const int row = 8 * j + (l >> 3);
        const unsigned xv = (unsigned)(row * D_ + ((l & 7) ^ ((row >> 1) & 7)) * 8) * 2;
        const int wch = ((l & 7) ^ (((row >> 1) & 1) << 2)) * 8;
        const unsigned qv = (unsigned)(row * 1536 + wch) * 2, zv = (unsigned)(row * 1792 + wch) * 2;
        const char* xu = (const char*)(x + (size_t)mt * 128 * D_);
        const char* g0 = (const char*)(wqkv + p * 64 + (TYPE == 0 ? 0 : 1024));
        const char* g1 = TYPE == 0 ? (const char*)(wqkv + p * 64 + 512) : (const char*)(w_in + FH + (8 + p) * 64);
        const char* g2 = (const char*)(w_in + FH + p * 64);
        auto issue = [&](int s) {
            const unsigned dst = lds0 + (s % 3) * K1_ST + j * 1024;
            const int sr = (s + rot) & 7;
#pragma unroll
            for (int i = 0; i < 4; ++i) glds16s(xu + (size_t)i * 32 * D_ * 2 + sr * 128, xv, dst + i * 4096);
            glds16s(g0 + (size_t)sr * 64 * 3072, qv, dst + 16384); glds16s(g0 + (size_t)(sr * 64 + 32) * 3072, qv, dst + 16384 + 4096);
            if (NG >= 2) { constexpr int ld = TYPE == 0 ? 3072 : 3584; const unsigned gv = TYPE == 0 ? qv : zv;
                glds16s(g1 + (size_t)sr * 64 * ld, gv, dst + 16384 + 8192); glds16s(g1 + (size_t)(sr * 64 + 32) * ld, gv, dst + 16384 + 8192 + 4096); }
            if (NG >= 3) { glds16s(g2 + (size_t)sr * 64 * 3584, zv, dst + 32768); glds16s(g2 + (size_t)(sr * 64 + 32) * 3584, zv, dst + 32768 + 4096); }
        };
        if (TYPE == 3 && j == 0) glds16s(gate_w, l * 16, lds0 + 3 * K1_ST);
        issue(0); issue(1); issue(2);
        wait_bar<2 * P>();
#pragma unroll 1
        for (int s = 0; s < 7; ++s) {
            if (j == 0) TL(0, 0, 3 * s);
            if (s + 2 < 8) asm volatile("s_waitcnt vmcnt(%0)" :: "i"(P) : "memory"); else asm volatile("s_waitcnt vmcnt(0)" ::: "memory");
            if (j == 0) TL(0, 0, 3 * s + 1);
            bar_only();
            if (j == 0) TL(0, 0, 3 * s + 2);
            if (s + 3 < 8) issue(s + 3);
        }
        return;
    }
    if (w == 3) {
        const unsigned dump = (unsigned)(uintptr_t)lds + 3 * K1_ST + 1024;
        {
            unsigned clo, chi;
            asm volatile("s_getpc_b64 s[70:71]\n\ts_add_u32 s70, s70, k_main@rel32@lo+4\n\ts_addc_u32 s71, s71, k_main@rel32@hi+12\n\ts_mov_b32 %0, s70\n\ts_mov_b32 %1, s71"
                         : "=s"(clo), "=s"(chi) :: "s70", "s71", "scc");
            const char* cm = (const char*)(((unsigned long long)chi << 32) | clo);
#pragma unroll
            for (int i = 0; i < 2; ++i) prefetch_line(cm, min((unsigned)(i * 64 + l) * 128, (unsigned)MAIN_BYTES), dump);
        }
    }
    const int rh = w & 1, ch = w >> 1, h = l >> 5, r32 = l & 31;
    int xo[4];
    { const int row = rh * 64 + r32, f = (row >> 1) & 7;
#pragma unroll
      for (int c = 0; c < 4; ++c) xo[c] = row * 128 + (((2 * c + h) ^ f) << 4); }
    const int q4 = (l & 15) >> 2, p4 = l & 3, b16 = (l >> 4) & 1;
    const int wl = 16384 + (8 * h + q4) * 128 + (((ch * 4 + b16 * 2 + (p4 >> 1)) ^ ((q4 >> 1) << 2)) << 4) + (p4 & 1) * 8;
    f32x16 acc[NA][2];
#pragma unroll
    for (int g = 0; g < NA; ++g) { acc[g][0] = f32x16{}; acc[g][1] = f32x16{}; }
    bf16x8 xa[2][2], wa[NG][2], xb[2][2], wb[NG][2], ga[2], gb[2];
    auto rdh = [&](int s, int half, bf16x8 (&xf)[2][2], bf16x8 (&wf)[NG][2], bf16x8 (&gf)[2]) {
        lds_cptr base = (lds_cptr)lds + (s % 3) * K1_ST;
#pragma unroll
        for (int kk = 0; kk < 2; ++kk) {
            const int ks = 2 * half + kk;
            xf[0][kk] = ldsfrag(base + xo[ks]); xf[1][kk] = ldsfrag(base + xo[ks] + 4096);
#pragma unroll
            for (int g = 0; g < NG; ++g) wf[g][kk] = trfrag(base + wl + g * 8192 + ks * 2048, base + wl + g * 8192 + ks * 2048 + 512);
            if (TYPE == 3) gf[kk] = ldsfrag((lds_cptr)lds + 3 * K1_ST + (((s + rot) & 7) * 4 + ks) * 32 + h * 16);
        }
    };
    auto mm = [&](bf16x8 (&xf)[2][2], bf16x8 (&wf)[NG][2], bf16x8 (&gf)[2]) {
#pragma unroll
        for (int kk = 0; kk < 2; ++kk)
#pragma unroll
            for (int rb = 0; rb < 2; ++rb) {
                if (TYPE == 0) {
#pragma unroll
                    for (int g = 0; g < 3; ++g) acc[g % NA][rb] = MFMA(wf[g % NG][kk], xf[rb][kk], acc[g % NA][rb]);
                } else {
                    acc[0][rb] = MFMA(xf[rb][kk], wf[0][kk], acc[0][rb]);
                    if (TYPE == 1) acc[NA - 1][rb] = MFMA(wf[NG - 1][kk], xf[rb][kk], acc[NA - 1][rb]);
                    if (TYPE == 3) acc[NA - 1][rb] = MFMA(gf[kk], xf[rb][kk], acc[NA - 1][rb]);
                }
            }
    };
    if (w == 0) TL(0, 1, 100);
    bar_only();
    if (w == 0) TL(0, 1, 101);
    rdh(0, 0, xa, wa, ga);
#pragma unroll 1
    for (int s = 0; s < 7; ++s) {
        rdh(s, 1, xb, wb, gb); mm(xa, wa, ga); interleave<4 * NA, 2>(); SB();
        if (w == 0) TL(0, 1, 2 * s);
        bar_only();
        if (w == 0) TL(0, 1, 2 * s + 1);
        rdh(s + 1, 0, xa, wa, ga); mm(xb, wb, gb); interleave<4 * NA, 2>(); SB();
    }
    rdh(7, 1, xb, wb, gb); mm(xa, wa, ga); interleave<4 * NA, 2>(); SB();
    mm(xb, wb, gb);
    STAMP(0, 1);
    if (w == 0) TL(0, 1, 102);
#pragma unroll
    for (int rb = 0; rb < 2; ++rb) {
        const int ttile = mt * 4 + rh * 2 + rb;
        if (TYPE == 0) {
            const int hd0 = 4 * p + 2 * ch;
            const f32x16 &q = acc[0][rb], &k = acc[1 % NA][rb];
            K1_ST16(WS_QF + ((size_t)(hd0 * 64 + ttile) * 1024) + l * 16, pack8(q, 0, QSCALE));
            K1_ST16(WS_QF + ((size_t)((hd0 + 1) * 64 + ttile) * 1024) + l * 16, pack8(q, 8, QSCALE));
            K1_ST16(WS_KV + ((size_t)(hd0 * 64 + ttile) * 2048) + l * 16, pack8(k, 0, 1.0f));
            K1_ST16(WS_KV + ((size_t)((hd0 + 1) * 64 + ttile) * 2048) + l * 16, pack8(k, 8, 1.0f));
            float n0 = 0.f, n1 = 0.f;
#pragma unroll
            for (int r = 0; r < 8; ++r) { n0 = __builtin_fmaf(k[r], k[r], n0); n1 = __builtin_fmaf(k[8 + r], k[8 + r], n1); }
            n0 = wave_max_fast(swap32_sum(n0)); n1 = wave_max_fast(swap32_sum(n1));
            if (l == 0) { float* km = (float*)(ws + WS_KMAX) + ttile * 32 + hd0; km[0] = n0 * 1.01f; km[1] = n1 * 1.01f; }
        } else {
            const int hd = 4 * p + 2 * ch + (r32 >> 4), d = r32 & 15;
            const size_t vp = WS_KV + ((size_t)(hd * 64 + ttile) * 2048) + 1024 + h * 256 + d * 16;
            K1_ST16(vp, pack8(acc[0][rb], 0, 1.0f)); K1_ST16(vp + 512, pack8(acc[0][rb], 8, 1.0f));
            if (TYPE == 3 && ch == 0 && h == 0)
                ((float*)(ws + WS_GATE))[ttile * 32 + r32] = __builtin_amdgcn_rcpf(1.0f + __builtin_amdgcn_exp2f(-1.4426950408889634f * acc[NA - 1][rb][0]));
        }
        if (TYPE <= 1) {
            f32x16& z = acc[NA - 1][rb];
            const int zg = TYPE == 0 ? p : 8 + p;
            float s1 = 0.f, s2 = 0.f;
#pragma unroll
            for (int r = 0; r < 16; ++r) { const float v = gelu_tanh(z[r]); z[r] = v; s1 += v; s2 = __builtin_fmaf(v, v, s2); }
            const int fb = 2 * zg + ch;
            const size_t zp = WS_Z2 + ((size_t)(ttile * 56 + 2 * fb) * 1024) + l * 16;
            K1_ST16(zp, pack8(z, 0, 1.0f)); K1_ST16(zp + 1024, pack8(z, 8, 1.0f));
            s1 = swap32_sum(s1); s2 = swap32_sum(s2);
            if (h == 0) ((f32x2*)(ws + WS_STATS))[(size_t)(ttile * 32 + r32) * 28 + fb] = f32x2{s1, s2};
        }
    }
    if (w == 3) drain_stores();
    if (w == 0) TL(0, 1, 103);
}

extern "C" __global__ __launch_bounds__(512) void k1_proj(const u16* __restrict__ x, const u16* __restrict__ wqkv, const u16* __restrict__ w_in, const u16* __restrict__ gate_w,
                                               char* __restrict__ ws) {
    __shared__ __attribute__((aligned(1024))) char lds[K1_LDS];
    const int b = blockIdx.x, xcd = b & 7, idx = b >> 3;
    const int mt = (xcd & 1) * 8 + (idx & 7), nt = (xcd >> 1) * 4 + (idx >> 3), p = nt >> 1;
    STAMP(0, 0);
    if (b == 255 && threadIdx.x < SY_N) *(unsigned*)(ws + WS_SYNC + threadIdx.x * 256) = 0u;
    if (!(nt & 1)) k1_body<0>(x, wqkv, w_in, gate_w, ws, lds, mt, p, threadIdx.x);
    else if (p < 6) k1_body<1>(x, wqkv, w_in, gate_w, ws, lds, mt, p, threadIdx.x);
    else if (p == 6) k1_body<2>(x, wqkv, w_in, gate_w, ws, lds, mt, p, threadIdx.x);
    else k1_body<3>(x, wqkv, w_in, gate_w, ws, lds, mt, p, threadIdx.x);
    STAMP(0, 2);
}

struct LnStat { float rstd, nm; };
DI LnStat ln_stats(const f32x4 (&pv)[7]) {
    float s1 = 0.f, s2 = 0.f;
#pragma unroll
    for (int i = 0; i < 7; ++i) { const f32x4 v = pv[i]; s1 += v[0] + v[2]; s2 += v[1] + v[3]; }
    s1 = swap32_sum(s1); s2 = swap32_sum(s2);
    const float mean = s1 * (1.0f / FH);
    const float var = fmaxf(s2 * (1.0f / FH) - mean * mean, 0.f);
    LnStat r; r.rstd = __builtin_amdgcn_rsqf(var + 1e-5f); r.nm = -mean * r.rstd; return r;
}
DI void ln_block(char* __restrict__ ws, char* scratch, int ttile, int fb, const u32x4& v0, const u32x4& v1, LnStat st, int l) {
    const int h = l >> 5, r32 = l & 31, q4 = (l & 15) >> 2, p4 = l & 3, b16 = (l >> 4) & 1;
    u32x4 ov[2];
#pragma unroll
    for (int g = 0; g < 2; ++g) {
        const u32x4 v = g ? v1 : v0;
        unsigned o[4];
#pragma unroll
        for (int e = 0; e < 4; ++e) o[e] = cvtpk(__builtin_fmaf(bf_lo(v[e]), st.rstd, st.nm), __builtin_fmaf(bf_hi(v[e]), st.rstd, st.nm));
        auto r0 = __builtin_amdgcn_permlane32_swap(o[0], o[2], false, false);
        auto r1 = __builtin_amdgcn_permlane32_swap(o[1], o[3], false, false);
        ov[g] = u32x4{r0[0], r1[0], r0[1], r1[1]};
    }
    lds_ptr tile = (lds_ptr)scratch;
#pragma unroll
    for (int kk = 0; kk < 2; ++kk) {
        if ((r32 >> 4) == kk) {
            *(LDS_AS u32x4*)(tile + (r32 & 15) * 64 + h * 16) = ov[0]; *(LDS_AS u32x4*)(tile + (r32 & 15) * 64 + 32 + h * 16) = ov[1]; }
        asm volatile("" ::: "memory");
        lds_cptr rp = (lds_cptr)tile + (8 * h + q4) * 64 + b16 * 32 + p4 * 8;
        const bf16x8 fr = trfrag(rp, rp + 256);
        asm volatile("" ::: "memory");
        st16_wt(ws, WS_Z2N + ((size_t)(fb * 128 + ttile * 2 + kk) * 1024) + l * 16, __builtin_bit_cast(u32x4, fr));
    }
}

constexpr int AT_TS = 2304, AT_SLOT = 8 * AT_TS, AT_LDS = 4 * AT_SLOT + 64;
#define ATTN_ASM \
    "s_mov_b32 %[sv], m0\n\t" \
    "v_mov_b32 v88, 0\n\t" \
    "v_mov_b32 v89, 0\n\t" \
    "v_mov_b32 v90, 0\n\t" \
    "v_mov_b32 v91, 0\n\t" \
    "v_mov_b32 v96, 0\n\t" \
    "v_mov_b32 v97, 0\n\t" \
    "v_mov_b32 v98, 0\n\t" \
    "v_mov_b32 v99, 0\n\t" \
    "v_mov_b32 v0, 0\n\t" \
    "v_mov_b32 v1, 0\n\t" \
    "v_mov_b32 v2, 0\n\t" \
    "v_mov_b32 v3, 0\n\t" \
    "v_mov_b32 v4, 0\n\t" \
    "v_mov_b32 v5, 0\n\t" \
    "v_mov_b32 v6, 0\n\t" \
    "v_mov_b32 v7, 0\n\t" \
    "v_mov_b32 v8, 0\n\t" \
    "v_mov_b32 v9, 0\n\t" \
    "v_mov_b32 v10, 0\n\t" \
    "v_mov_b32 v11, 0\n\t" \
    "v_mov_b32 v12, 0\n\t" \
    "v_mov_b32 v13, 0\n\t" \
    "v_mov_b32 v14, 0\n\t" \
    "v_mov_b32 v15, 0\n\t" \
    "s_mov_b32 %[cnt], 0\n\t" \
    "s_waitcnt vmcnt(4) lgkmcnt(0)\n\t" \
    "s_barrier\n\t" \
    "ds_read_b128 v[68:71], %[kl] offset:0\n\t" \
    "ds_read_b128 v[72:75], %[kl] offset:2304\n\t" \
    "ds_read_b128 v[76:79], %[v0l] offset:0\n\t" \
    "ds_read_b128 v[80:83], %[v1l] offset:0\n\t" \
    "s_waitcnt lgkmcnt(0)\n\t" \
    "s_setprio 3\n\t" \
    "v_mfma_f32_32x32x16_bf16 v[32:47], v[68:71], v[64:67], v[16:31]\n\t" \
    "s_setprio 0\n\t" \
    "s_nop 7\n\t" \
    "s_nop 3\n\t" \
    "Lloop%=:\n\t" \
    "s_setprio 3\n\t" \
    "v_mfma_f32_32x32x16_bf16 v[48:63], v[72:75], v[64:67], v[16:31]\n\t" \
    "s_setprio 2\n\t" \
    "v_exp_f32 v32, v32\n\t" \
    "ds_read_b128 v[68:71], %[kl] offset:4608\n\t" \
    "v_exp_f32 v33, v33\n\t" \
    "v_exp_f32 v34, v34\n\t" \
    "v_exp_f32 v35, v35\n\t" \
    "v_exp_f32 v36, v36\n\t" \
    "v_exp_f32 v37, v37\n\t" \
    "v_exp_f32 v38, v38\n\t" \
    "s_setprio 3\n\t" \
    "v_mfma_f32_32x32x16_bf16 v[0:15], v[88:91], v[96:99], v[0:15]\n\t" \
    "s_setprio 2\n\t" \
    "v_exp_f32 v39, v39\n\t" \
    "ds_read_b128 v[84:87], %[v0l] offset:2304\n\t" \
    "v_cvt_pk_bf16_f32 v92, v32, v33\n\t" \
    "v_cvt_pk_bf16_f32 v93, v34, v35\n\t" \
    "v_cvt_pk_bf16_f32 v94, v36, v37\n\t" \
    "v_cvt_pk_bf16_f32 v95, v38, v39\n\t" \
    "v_exp_f32 v40, v40\n\t" \
    "ds_read_b128 v[88:91], %[v1l] offset:2304\n\t" \
    "v_exp_f32 v41, v41\n\t" \
    "v_exp_f32 v42, v42\n\t" \
    "v_exp_f32 v43, v43\n\t" \
    "s_waitcnt lgkmcnt(4)\n\t" \
    "s_setprio 3\n\t" \
    "v_mfma_f32_32x32x16_bf16 v[0:15], v[76:79], v[92:95], v[0:15]\n\t" \
    "s_setprio 2\n\t" \
    "v_exp_f32 v44, v44\n\t" \
    "v_exp_f32 v45, v45\n\t" \
    "v_exp_f32 v46, v46\n\t" \
    "v_exp_f32 v47, v47\n\t" \
    "s_waitcnt lgkmcnt(2)\n\t" \
    "v_cvt_pk_bf16_f32 v96, v40, v41\n\t" \
    "v_cvt_pk_bf16_f32 v97, v42, v43\n\t" \
    "v_cvt_pk_bf16_f32 v98, v44, v45\n\t" \
    "v_cvt_pk_bf16_f32 v99, v46, v47\n\t" \
    "s_setprio 3\n\t" \
    "v_mfma_f32_32x32x16_bf16 v[32:47], v[68:71], v[64:67], v[16:31]\n\t" \
    "s_setprio 1\n\t" \
    "v_exp_f32 v48, v48\n\t" \
    "ds_read_b128 v[72:75], %[kl] offset:6912\n\t" \
    "v_exp_f32 v49, v49\n\t" \
    "v_exp_f32 v50, v50\n\t" \
    "v_exp_f32 v51, v51\n\t" \
    "v_exp_f32 v52, v52\n\t" \
    "v_exp_f32 v53, v53\n\t" \
    "v_exp_f32 v54, v54\n\t" \
    "s_setprio 3\n\t" \
    "v_mfma_f32_32x32x16_bf16 v[0:15], v[80:83], v[96:99], v[0:15]\n\t" \
    "s_setprio 1\n\t" \
    "v_exp_f32 v55, v55\n\t" \
    "ds_read_b128 v[76:79], %[v0l] offset:4608\n\t" \
    "v_cvt_pk_bf16_f32 v92, v48, v49\n\t" \
    "v_cvt_pk_bf16_f32 v93, v50, v51\n\t" \
    "v_cvt_pk_bf16_f32 v94, v52, v53\n\t" \
    "v_cvt_pk_bf16_f32 v95, v54, v55\n\t" \
    "v_exp_f32 v56, v56\n\t" \
    "ds_read_b128 v[80:83], %[v1l] offset:4608\n\t" \
    "v_exp_f32 v57, v57\n\t" \
    "v_exp_f32 v58, v58\n\t" \
    "v_exp_f32 v59, v59\n\t" \
    "s_waitcnt lgkmcnt(4)\n\t" \
    "s_setprio 3\n\t" \
    "v_mfma_f32_32x32x16_bf16 v[0:15], v[84:87], v[92:95], v[0:15]\n\t" \
    "s_setprio 1\n\t" \
    "v_exp_f32 v60, v60\n\t" \
    "v_exp_f32 v61, v61\n\t" \
    "v_exp_f32 v62, v62\n\t" \
    "v_exp_f32 v63, v63\n\t" \
    "s_waitcnt lgkmcnt(2)\n\t" \
    "v_cvt_pk_bf16_f32 v96, v56, v57\n\t" \
    "v_cvt_pk_bf16_f32 v97, v58, v59\n\t" \
    "v_cvt_pk_bf16_f32 v98, v60, v61\n\t" \
    "v_cvt_pk_bf16_f32 v99, v62, v63\n\t" \
    "s_setprio 3\n\t" \
    "v_mfma_f32_32x32x16_bf16 v[48:63], v[72:75], v[64:67], v[16:31]\n\t" \
    "s_setprio 0\n\t" \
    "v_exp_f32 v32, v32\n\t" \
    "ds_read_b128 v[68:71], %[kl] offset:9216\n\t" \
    "v_exp_f32 v33, v33\n\t" \
    "v_exp_f32 v34, v34\n\t" \
    "v_exp_f32 v35, v35\n\t" \
    "v_exp_f32 v36, v36\n\t" \
    "v_exp_f32 v37, v37\n\t" \
    "v_exp_f32 v38, v38\n\t" \
    "s_setprio 3\n\t" \
    "v_mfma_f32_32x32x16_bf16 v[0:15], v[88:91], v[96:99], v[0:15]\n\t" \
    "s_setprio 0\n\t" \
    "v_exp_f32 v39, v39\n\t" \
    "ds_read_b128 v[84:87], %[v0l] offset:6912\n\t" \
    "v_cvt_pk_bf16_f32 v92, v32, v33\n\t" \
    "v_cvt_pk_bf16_f32 v93, v34, v35\n\t" \
    "v_cvt_pk_bf16_f32 v94, v36, v37\n\t" \
    "v_cvt_pk_bf16_f32 v95, v38, v39\n\t" \
    "v_exp_f32 v40, v40\n\t" \
    "ds_read_b128 v[88:91], %[v1l] offset:6912\n\t" \
    "v_exp_f32 v41, v41\n\t" \
    "v_exp_f32 v42, v42\n\t" \
    "v_exp_f32 v43, v43\n\t" \
    "s_waitcnt lgkmcnt(4)\n\t" \
    "s_setprio 3\n\t" \
    "v_mfma_f32_32x32x16_bf16 v[0:15], v[76:79], v[92:95], v[0:15]\n\t" \
    "s_setprio 0\n\t" \
    "v_exp_f32 v44, v44\n\t" \
    "v_exp_f32 v45, v45\n\t" \
    "v_exp_f32 v46, v46\n\t" \
    "v_exp_f32 v47, v47\n\t" \
    "s_waitcnt lgkmcnt(2)\n\t" \
    "v_cvt_pk_bf16_f32 v96, v40, v41\n\t" \
    "v_cvt_pk_bf16_f32 v97, v42, v43\n\t" \
    "v_cvt_pk_bf16_f32 v98, v44, v45\n\t" \
    "v_cvt_pk_bf16_f32 v99, v46, v47\n\t" \
    "s_setprio 3\n\t" \
    "v_mfma_f32_32x32x16_bf16 v[32:47], v[68:71], v[64:67], v[16:31]\n\t" \
    "s_setprio 0\n\t" \
    "v_exp_f32 v48, v48\n\t" \
    "ds_read_b128 v[72:75], %[kl] offset:11520\n\t" \
    "v_exp_f32 v49, v49\n\t" \
    "v_exp_f32 v50, v50\n\t" \
    "v_exp_f32 v51, v51\n\t" \
    "v_exp_f32 v52, v52\n\t" \
    "v_exp_f32 v53, v53\n\t" \
    "v_exp_f32 v54, v54\n\t" \
    "s_setprio 3\n\t" \
    "v_mfma_f32_32x32x16_bf16 v[0:15], v[80:83], v[96:99], v[0:15]\n\t" \
    "s_setprio 0\n\t" \
    "v_exp_f32 v55, v55\n\t" \
    "ds_read_b128 v[76:79], %[v0l] offset:9216\n\t" \
    "v_cvt_pk_bf16_f32 v92, v48, v49\n\t" \
    "v_cvt_pk_bf16_f32 v93, v50, v51\n\t" \
    "v_cvt_pk_bf16_f32 v94, v52, v53\n\t" \
    "v_cvt_pk_bf16_f32 v95, v54, v55\n\t" \
    "v_exp_f32 v56, v56\n\t" \
    "ds_read_b128 v[80:83], %[v1l] offset:9216\n\t" \
    "v_exp_f32 v57, v57\n\t" \
    "v_exp_f32 v58, v58\n\t" \
    "v_exp_f32 v59, v59\n\t" \
    "s_waitcnt lgkmcnt(4)\n\t" \
    "s_setprio 3\n\t" \
    "v_mfma_f32_32x32x16_bf16 v[0:15], v[84:87], v[92:95], v[0:15]\n\t" \
    "s_setprio 0\n\t" \
    "v_exp_f32 v60, v60\n\t" \
    "v_exp_f32 v61, v61\n\t" \
    "v_exp_f32 v62, v62\n\t" \
    "v_exp_f32 v63, v63\n\t" \
    "s_waitcnt lgkmcnt(2)\n\t" \
    "v_cvt_pk_bf16_f32 v96, v56, v57\n\t" \
    "v_cvt_pk_bf16_f32 v97, v58, v59\n\t" \
    "v_cvt_pk_bf16_f32 v98, v60, v61\n\t" \
    "v_cvt_pk_bf16_f32 v99, v62, v63\n\t" \
    "s_setprio 3\n\t" \
    "v_mfma_f32_32x32x16_bf16 v[48:63], v[72:75], v[64:67], v[16:31]\n\t" \
    "s_setprio 2\n\t" \
    "v_exp_f32 v32, v32\n\t" \
    "ds_read_b128 v[68:71], %[kl] offset:13824\n\t" \
    "v_exp_f32 v33, v33\n\t" \
    "v_exp_f32 v34, v34\n\t" \
    "v_exp_f32 v35, v35\n\t" \
    "v_exp_f32 v36, v36\n\t" \
    "v_exp_f32 v37, v37\n\t" \
    "v_exp_f32 v38, v38\n\t" \
    "s_setprio 3\n\t" \
    "v_mfma_f32_32x32x16_bf16 v[0:15], v[88:91], v[96:99], v[0:15]\n\t" \
    "s_setprio 2\n\t" \
    "v_exp_f32 v39, v39\n\t" \
    "ds_read_b128 v[84:87], %[v0l] offset:11520\n\t" \
    "v_cvt_pk_bf16_f32 v92, v32, v33\n\t" \
    "v_cvt_pk_bf16_f32 v93, v34, v35\n\t" \
    "v_cvt_pk_bf16_f32 v94, v36, v37\n\t" \
    "v_cvt_pk_bf16_f32 v95, v38, v39\n\t" \
    "v_exp_f32 v40, v40\n\t" \
    "ds_read_b128 v[88:91], %[v1l] offset:11520\n\t" \
    "v_exp_f32 v41, v41\n\t" \
    "v_exp_f32 v42, v42\n\t" \
    "v_exp_f32 v43, v43\n\t" \
    "s_waitcnt lgkmcnt(4)\n\t" \
    "s_setprio 3\n\t" \
    "v_mfma_f32_32x32x16_bf16 v[0:15], v[76:79], v[92:95], v[0:15]\n\t" \
    "s_setprio 2\n\t" \
    "v_exp_f32 v44, v44\n\t" \
    "v_exp_f32 v45, v45\n\t" \
    "v_exp_f32 v46, v46\n\t" \
    "v_exp_f32 v47, v47\n\t" \
    "s_waitcnt lgkmcnt(2)\n\t" \
    "v_cvt_pk_bf16_f32 v96, v40, v41\n\t" \
    "v_cvt_pk_bf16_f32 v97, v42, v43\n\t" \
    "v_cvt_pk_bf16_f32 v98, v44, v45\n\t" \
    "v_cvt_pk_bf16_f32 v99, v46, v47\n\t" \
    "s_setprio 3\n\t" \
    "v_mfma_f32_32x32x16_bf16 v[32:47], v[68:71], v[64:67], v[16:31]\n\t" \
    "s_setprio 1\n\t" \
    "v_exp_f32 v48, v48\n\t" \
    "ds_read_b128 v[72:75], %[kl] offset:16128\n\t" \
    "s_and_b32 %[t0], %[cnt], 3\n\t" \
    "s_cmp_eq_u32 %[t0], 3\n\t" \
    "s_mov_b32 %[dl], 18432\n\t" \
    "s_cselect_b32 %[dl], -55296, %[dl]\n\t" \
    "v_add_u32 %[kl], %[dl], %[kl]\n\t" \
    "v_exp_f32 v49, v49\n\t" \
    "v_exp_f32 v50, v50\n\t" \
    "v_exp_f32 v51, v51\n\t" \
    "v_exp_f32 v52, v52\n\t" \
    "v_exp_f32 v53, v53\n\t" \
    "v_exp_f32 v54, v54\n\t" \
    "s_setprio 3\n\t" \
    "v_mfma_f32_32x32x16_bf16 v[0:15], v[80:83], v[96:99], v[0:15]\n\t" \
    "s_setprio 1\n\t" \
    "v_exp_f32 v55, v55\n\t" \
    "ds_read_b128 v[76:79], %[v0l] offset:13824\n\t" \
    "v_cvt_pk_bf16_f32 v92, v48, v49\n\t" \
    "v_cvt_pk_bf16_f32 v93, v50, v51\n\t" \
    "v_cvt_pk_bf16_f32 v94, v52, v53\n\t" \
    "v_cvt_pk_bf16_f32 v95, v54, v55\n\t" \
    "v_exp_f32 v56, v56\n\t" \
    "ds_read_b128 v[80:83], %[v1l] offset:13824\n\t" \
    "v_exp_f32 v57, v57\n\t" \
    "v_exp_f32 v58, v58\n\t" \
    "v_exp_f32 v59, v59\n\t" \
    "s_waitcnt lgkmcnt(4)\n\t" \
    "s_setprio 3\n\t" \
    "v_mfma_f32_32x32x16_bf16 v[0:15], v[84:87], v[92:95], v[0:15]\n\t" \
    "s_setprio 1\n\t" \
    "v_exp_f32 v60, v60\n\t" \
    "v_exp_f32 v61, v61\n\t" \
    "v_exp_f32 v62, v62\n\t" \
    "v_exp_f32 v63, v63\n\t" \
    "s_waitcnt lgkmcnt(2)\n\t" \
    "v_cvt_pk_bf16_f32 v96, v56, v57\n\t" \
    "v_cvt_pk_bf16_f32 v97, v58, v59\n\t" \
    "v_cvt_pk_bf16_f32 v98, v60, v61\n\t" \
    "v_cvt_pk_bf16_f32 v99, v62, v63\n\t" \
    "s_cmp_eq_u32 %[cnt], 7\n\t" \
    "s_cbranch_scc1 Lskip%=\n\t" \
    "s_cmp_eq_u32 %[cnt], 6\n\t" \
    "s_cbranch_scc1 Lw0%=\n\t" \
    "s_waitcnt vmcnt(2)\n\t" \
    "s_barrier\n\t" \
    "s_cmp_gt_u32 %[cnt], 4\n\t" \
    "s_cbranch_scc1 Lskip%=\n\t" \
    "s_add_u32 %[t0], %[cnt], 3\n\t" \
    "s_and_b32 %[t0], %[t0], 3\n\t" \
    "s_mul_i32 %[t0], %[t0], 18432\n\t" \
    "s_add_u32 m0, %[t0], %[dst]\n\t" \
    "s_setprio 3\n\t" \
    "global_load_lds_dwordx4 %[goff], %[base]\n\t" \
    "global_load_lds_dwordx4 %[goff], %[base] offset:1024\n\t" \
    "s_setprio 1\n\t" \
    "v_add_u32 %[goff], 0x4000, %[goff]\n\t" \
    "s_branch Lskip%=\n\t" \
    "Lw0%=:\n\t" \
    "s_waitcnt vmcnt(0)\n\t" \
    "s_barrier\n\t" \
    "Lskip%=:\n\t" \
    "s_setprio 3\n\t" \
    "v_mfma_f32_32x32x16_bf16 v[48:63], v[72:75], v[64:67], v[16:31]\n\t" \
    "s_setprio 0\n\t" \
    "v_exp_f32 v32, v32\n\t" \
    "ds_read_b128 v[68:71], %[kl] offset:0\n\t" \
    "v_exp_f32 v33, v33\n\t" \
    "v_exp_f32 v34, v34\n\t" \
    "v_exp_f32 v35, v35\n\t" \
    "v_exp_f32 v36, v36\n\t" \
    "v_exp_f32 v37, v37\n\t" \
    "v_exp_f32 v38, v38\n\t" \
    "s_setprio 3\n\t" \
    "v_mfma_f32_32x32x16_bf16 v[0:15], v[88:91], v[96:99], v[0:15]\n\t" \
    "s_setprio 0\n\t" \
    "v_exp_f32 v39, v39\n\t" \
    "ds_read_b128 v[84:87], %[v0l] offset:16128\n\t" \
    "v_cvt_pk_bf16_f32 v92, v32, v33\n\t" \
    "v_cvt_pk_bf16_f32 v93, v34, v35\n\t" \
    "v_cvt_pk_bf16_f32 v94, v36, v37\n\t" \
    "v_cvt_pk_bf16_f32 v95, v38, v39\n\t" \
    "v_exp_f32 v40, v40\n\t" \
    "ds_read_b128 v[88:91], %[v1l] offset:16128\n\t" \
    "v_add_u32 %[v0l], %[dl], %[v0l]\n\t" \
    "v_add_u32 %[v1l], %[dl], %[v1l]\n\t" \
    "v_exp_f32 v41, v41\n\t" \
    "v_exp_f32 v42, v42\n\t" \
    "v_exp_f32 v43, v43\n\t" \
    "s_waitcnt lgkmcnt(4)\n\t" \
    "s_setprio 3\n\t" \
    "v_mfma_f32_32x32x16_bf16 v[0:15], v[76:79], v[92:95], v[0:15]\n\t" \
    "s_setprio 0\n\t" \
    "v_exp_f32 v44, v44\n\t" \
    "v_exp_f32 v45, v45\n\t" \
    "v_exp_f32 v46, v46\n\t" \
    "v_exp_f32 v47, v47\n\t" \
    "s_waitcnt lgkmcnt(2)\n\t" \
    "v_cvt_pk_bf16_f32 v96, v40, v41\n\t" \
    "v_cvt_pk_bf16_f32 v97, v42, v43\n\t" \
    "v_cvt_pk_bf16_f32 v98, v44, v45\n\t" \
    "v_cvt_pk_bf16_f32 v99, v46, v47\n\t" \
    "s_setprio 3\n\t" \
    "v_mfma_f32_32x32x16_bf16 v[32:47], v[68:71], v[64:67], v[16:31]\n\t" \
    "s_setprio 0\n\t" \
    "v_exp_f32 v48, v48\n\t" \
    "ds_read_b128 v[72:75], %[kl] offset:2304\n\t" \
    "v_exp_f32 v49, v49\n\t" \
    "v_exp_f32 v50, v50\n\t" \
    "v_exp_f32 v51, v51\n\t" \
    "v_exp_f32 v52, v52\n\t" \
    "v_exp_f32 v53, v53\n\t" \
    "v_exp_f32 v54, v54\n\t" \
    "s_setprio 3\n\t" \
    "v_mfma_f32_32x32x16_bf16 v[0:15], v[80:83], v[96:99], v[0:15]\n\t" \
    "s_setprio 0\n\t" \
    "v_exp_f32 v55, v55\n\t" \
    "ds_read_b128 v[76:79], %[v0l] offset:0\n\t" \
    "v_cvt_pk_bf16_f32 v92, v48, v49\n\t" \
    "v_cvt_pk_bf16_f32 v93, v50, v51\n\t" \
    "v_cvt_pk_bf16_f32 v94, v52, v53\n\t" \
    "v_cvt_pk_bf16_f32 v95, v54, v55\n\t" \
    "v_exp_f32 v56, v56\n\t" \
    "ds_read_b128 v[80:83], %[v1l] offset:0\n\t" \
    "v_exp_f32 v57, v57\n\t" \
    "v_exp_f32 v58, v58\n\t" \
    "v_exp_f32 v59, v59\n\t" \
    "s_waitcnt lgkmcnt(4)\n\t" \
    "s_setprio 3\n\t" \
    "v_mfma_f32_32x32x16_bf16 v[0:15], v[84:87], v[92:95], v[0:15]\n\t" \
    "s_setprio 0\n\t" \
    "v_exp_f32 v60, v60\n\t" \
    "v_exp_f32 v61, v61\n\t" \
    "v_exp_f32 v62, v62\n\t" \
    "v_exp_f32 v63, v63\n\t" \
    "s_waitcnt lgkmcnt(2)\n\t" \
    "v_cvt_pk_bf16_f32 v96, v56, v57\n\t" \
    "v_cvt_pk_bf16_f32 v97, v58, v59\n\t" \
    "v_cvt_pk_bf16_f32 v98, v60, v61\n\t" \
    "v_cvt_pk_bf16_f32 v99, v62, v63\n\t" \
    "s_add_u32 %[cnt], %[cnt], 1\n\t" \
    "s_cmp_lt_u32 %[cnt], 8\n\t" \
    "s_cbranch_scc1 Lloop%=\n\t" \
    "s_waitcnt lgkmcnt(0)\n\t" \
    "s_setprio 3\n\t" \
    "v_mfma_f32_32x32x16_bf16 v[0:15], v[88:91], v[96:99], v[0:15]\n\t" \
    "s_setprio 0\n\t" \
    "s_mov_b32 m0, %[sv]\n\t" \
    "s_setprio 0\n\t" \
    "s_nop 7\n\t" \
    "s_nop 7\n\t"
#define ATTN_CLOBBERS "v32", "v33", "v34", "v35", "v36", "v37", "v38", "v39", "v40", "v41", "v42", "v43", "v44", "v45", "v46", "v47", "v48", "v49", "v50", "v51", "v52", "v53", "v54", "v55", "v56", "v57", "v58", "v59", "v60", "v61", "v62", "v63", "v68", "v69", "v70", "v71", "v72", "v73", "v74", "v75", "v76", "v77", "v78", "v79", "v80", "v81", "v82", "v83", "v84", "v85", "v86", "v87", "v88", "v89", "v90", "v91", "v92", "v93", "v94", "v95", "v96", "v97", "v98", "v99"
#define ATTN_INSTR 0

DI void attn_block(char* __restrict__ ws, char* lds, int head, int chunk, int tid) {
    const int l = tid & 63, w = __builtin_amdgcn_readfirstlane(tid >> 6), h = l >> 5;
    const int qtile = chunk * 8 + w;
    const unsigned lds0 = (unsigned)(uintptr_t)lds;
    const char* kvbase = ws + WS_KV + (size_t)head * 64 * 2048;
    const unsigned dst = lds0 + w * AT_TS;
    __builtin_amdgcn_s_setprio(3);
    bf16x8 qf; float km, gate;
    asm volatile("global_load_dwordx4 %0, %3, off\n\tglobal_load_dword %1, %4, off\n\tglobal_load_dword %2, %5, off"
                 : "=&v"(qf), "=&v"(km), "=&v"(gate)
                 : "v"(ws + WS_QF + ((size_t)(head * 64 + qtile) * 1024) + l * 16), "v"((const float*)(ws + WS_KMAX) + l * 32 + head),
                   "v"((const float*)(ws + WS_GATE) + qtile * 32 + (l & 31)) : "memory");
    auto issue3 = [&]() {
#pragma unroll
        for (int c = 0; c < 3; ++c) { glds16(kvbase + c * 16384 + w * 2048 + l * 16, dst + c * AT_SLOT); glds16(kvbase + c * 16384 + w * 2048 + 1024 + l * 16, dst + c * AT_SLOT + 1024); }
    };
    issue3();
    {
        const u32x4 ones = {0x3f803f80u, 0x3f803f80u, 0x3f803f80u, 0x3f803f80u};
        *(LDS_AS u32x4*)((lds_ptr)lds + (tid >> 4) * AT_TS + 2048 + (tid & 15) * 16) = ones;
        if (tid == 0) *(LDS_AS unsigned*)((lds_ptr)lds + 4 * AT_SLOT) = 0u;
    }
    asm volatile("s_waitcnt vmcnt(6)" : "+v"(qf), "+v"(km), "+v"(gate));
    float qn = 0.f;
    { const u32x4 qu = __builtin_bit_cast(u32x4, qf);
#pragma unroll
      for (int e = 0; e < 4; ++e) { qn = __builtin_fmaf(bf_lo(qu[e]), bf_lo(qu[e]), qn); qn = __builtin_fmaf(bf_hi(qu[e]), bf_hi(qu[e]), qn); } }
    qn = swap32_sum(qn);
    float ref = __builtin_amdgcn_sqrtf(qn * wave_max_fast(km));
    unsigned kl = lds0 + l * 16;
    const bool onesl = (l & 16) != 0;
    const unsigned ol = lds0 + 2048 + ((((l & 15) >= 4) && ((l & 15) < 12)) ? 64 : 0);
    unsigned v0l = onesl ? ol : lds0 + 1024 + h * 256 + (l & 15) * 16, v1l = onesl ? ol : v0l + 512;
    LDS_AS volatile unsigned* flag = (LDS_AS volatile unsigned*)((lds_ptr)lds + 4 * AT_SLOT);
    const size_t ooff = WS_HO + ((size_t)(qtile * 88 + head) * 1024) + l * 16;
#pragma unroll 1
    for (int pass = 0;; ++pass) {
        f32x16 negm, o;
#pragma unroll
        for (int r = 0; r < 16; ++r) negm[r] = -ref;
        unsigned sv, cnt, t0, dl, goff = 3 * 16384 + w * 2048 + l * 16;
        STAMP(2, 1);
        asm volatile(ATTN_ASM
                     : "=&{v[0:15]}"(o), [sv] "=&s"(sv), [cnt] "=&s"(cnt), [t0] "=&s"(t0), [dl] "=&s"(dl), [goff] "+v"(goff), [kl] "+v"(kl), [v0l] "+v"(v0l), [v1l] "+v"(v1l)
                     : "{v[16:31]}"(negm), "{v[64:67]}"(qf), [dst] "s"(dst), [base] "s"(kvbase)
                     : ATTN_CLOBBERS, "scc", "memory");
        STAMP(2, 3);
        st16_wt(ws, ooff, pack8(o, 0, gate * __builtin_amdgcn_rcpf(o[8])));
        drain_stores();
        if (pass == 1) { __syncthreads(); break; }
        const bool bad = !(o[8] >= 9.094947e-13f);
        if (__any(bad) && l == 0) *flag = 1u;
        __syncthreads();
        if (*flag == 0u) break;
        float m = -3.0e38f;
        for (int t = 0; t < 64; ++t) {
            const bf16x8 kf = *(const bf16x8*)(kvbase + (size_t)t * 2048 + l * 16);
            const f32x16 s = MFMA(kf, qf, f32x16{});
#pragma unroll
            for (int r = 0; r < 16; ++r) m = fmaxf(m, s[r]);
        }
        ref = swap32_max(m);
        asm volatile("s_waitcnt vmcnt(0)" ::: "memory");
        issue3();
    }
    if (tid == 0) __hip_atomic_fetch_add(sync_word(ws, SY_O + chunk), 1u, RLX_AGENT);
}
constexpr int SP_ST = 24576, SP_LDS = 3 * SP_ST + 256, FN_ST = 16384;

template <int N> DI void wait_vm() { asm volatile("s_waitcnt vmcnt(%0)" :: "i"(N) : "memory"); }

DI void gate_block(const u16* __restrict__ x, const u16* __restrict__ w_in, const u16* __restrict__ w_sp, const u16* __restrict__ b_sp,
                   const u16* __restrict__ wo, const u16* __restrict__ w_out, char* __restrict__ ws, u16* __restrict__ out, char* lds, int g, int tid) {
    const int l = tid & 63, w = __builtin_amdgcn_readfirstlane(tid >> 6), h = l >> 5, r32 = l & 31;
    const int tl = (g >> 3) & 7, tt = (g & 3) * 8 + tl, ft = ((g >> 2) & 1) * 4 + (g >> 6);
    const int rot = SP_ROT(tl, g >> 6);
    const unsigned lds0 = (unsigned)(uintptr_t)lds;
    const int q4 = (l & 15) >> 2, p4 = l & 3, b16 = (l >> 4) & 1;
    const unsigned lo = l * 16;
    if (tid == 0) TL(5, 0, 99);
    if (GPRIO) __builtin_amdgcn_s_setprio(GPRIO);
    const int j = w - 4, lrow = 8 * j + (l >> 3);
    const unsigned xv = (unsigned)(lrow * D_ + ((l & 7) ^ ((lrow >> 1) & 7)) * 8) * 2;
    const int wr = 4 * j + (l >> 4), wc = ((((l & 15) >> 2) ^ (wr & 3)) << 2) | (l & 3);
    const unsigned wv = (unsigned)(wr * 1792 + wc * 8) * 2;
    auto issue_z = [&](int s) {
        const char* xu = (const char*)(x + (size_t)tt * 64 * D_); const char* wu = (const char*)(w_in + ft * 128);
        const unsigned dst = lds0 + (s % 3) * SP_ST + j * 1024; const int sr = (s + rot) & 7;
        glds16s(xu + sr * 128, xv, dst); glds16s(xu + 32 * D_ * 2 + sr * 128, xv, dst + 4096);
#pragma unroll
        for (int i = 0; i < 4; ++i) glds16s(wu + (size_t)(sr * 64 + 16 * i) * 3584, wv, dst + 8192 + i * 4096);
    };
    STAMP(5, 1);
    if (w >= 4 && ft < 7) { issue_z(0); issue_z(1); }
    if (w < 7) {
        const int ttile = g >> 2, fb = (g & 3) * 7 + w;
        const char* z2 = ws + WS_Z2 + (size_t)(ttile * 56 + 2 * fb) * 1024 + lo;
        const u32x4 z0 = *(const u32x4*)z2, z1 = *(const u32x4*)(z2 + 1024);
        f32x4 pv[7];
        const f32x4* sp = (const f32x4*)(ws + WS_STATS) + (size_t)(ttile * 32 + r32) * 14 + h * 7;
#pragma unroll
        for (int i = 0; i < 7; ++i) pv[i] = sp[i];
        ln_block(ws, lds + 2 * SP_ST + w * 1024, ttile, fb, z0, z1, ln_stats(pv), l);
    }
    if (ft >= 7) { drain_stores(); bar_only(); if (tid == 0) __hip_atomic_fetch_add(sync_word(ws, SY_LN + (g & 15)), 1u, RLX_AGENT); }
    if (ft < 7) {
        if (w >= 4) {
            if (LPRIO != GPRIO) __builtin_amdgcn_s_setprio(LPRIO);
            if (w < 7) wait_bar<2>(); else wait_bar<0>();
            issue_z(2);
#pragma unroll 1
            for (int s = 0; s < 7; ++s) {
                if (s == 0) bar_only(); else if (s + 2 < 8) wait_bar<6>(); else wait_bar<0>();
                if (s + 3 < 8) issue_z(s + 3);
            }
            bar_only();
            if (j == 3) {
                wait_words(ws, SY_LN, 16, 16u, l);
                const char* zu = ws + WS_Z2N + (size_t)(ft * 4) * 128 * 1024;
                const unsigned pv = (unsigned)((tl >> 1) * 128 * 1024 + ((tl & 1) * 16 + (l & 15)) * 128);
                auto pf = [&](int s4) { const int sr = (4 * s4 + (l >> 4) + rot) & 31; prefetch_line(zu, pv + sr * 4096, lds0 + 3 * SP_ST); };
                pf(0); pf(1); pf(2);
                bar_only();
                bar_only();
#pragma unroll 1
                for (int s = 0; s < 31; ++s) { bar_only(); if ((s & 3) == 0 && s + 12 < 32) pf((s >> 2) + 3); }
                drain_stores();
            } else {
                const unsigned a0 = (unsigned)((l >> 3) * S_ * 2 + (((l & 7) ^ ((l >> 4) & 7)) << 4)), a1 = a0 ^ 64u;
                const unsigned va = (j & 1) ? a1 : a0, vb = (j & 1) ? a0 : a1;
                const char* au = (const char*)(w_sp + (size_t)(tt * 64 + 8 * j) * S_);
                auto issue = [&](int s) {
                    const unsigned dst = lds0 + (s % 6) * 8192 + j * 1024; const int sr = (s + rot) & 31;
                    glds16s(au + sr * 128, va, dst); glds16s(au + 24 * S_ * 2 + sr * 128, vb, dst + 3072);
                    if (j < 2) glds16s(au + 48 * S_ * 2 + sr * 128, va, dst + 6144);
                };
                auto waitst = [&](int younger) {
                    if (j < 2) { if (younger >= 4) wait_vm<12>(); else if (younger == 3) wait_vm<9>(); else if (younger == 2) wait_vm<6>(); else if (younger == 1) wait_vm<3>(); else wait_vm<0>(); }
                    else { if (younger >= 4) wait_vm<8>(); else if (younger == 3) wait_vm<6>(); else if (younger == 2) wait_vm<4>(); else if (younger == 1) wait_vm<2>(); else wait_vm<0>(); }
                };
#pragma unroll 1
                for (int s = 0; s < 5; ++s) issue(s);
                bar_only();
                waitst(4); bar_only();
                issue(5);
#pragma unroll 1
                for (int s = 0; s < 31; ++s) {
                    waitst(min(s + 5, 31) - (s + 1)); bar_only();
                    if (s + 6 < 32) issue(s + 6);
                }
            }
            bar_only();
        } else {
            const int fb = w;
            int boff[4];
#pragma unroll
            for (int ks = 0; ks < 4; ++ks) { const int f = (r32 >> 1) & 7; boff[ks] = r32 * 128 + (((ks * 2 + h) ^ f) << 4); }
            const int fbg = ft * 4 + fb, fg = 2 * fbg;
            const float gt0 = ((const float*)(ws + WS_GATE))[(tt * 2) * 32 + r32], gt1 = ((const float*)(ws + WS_GATE))[(tt * 2 + 1) * 32 + r32];
            const float bs0 = __uint_as_float((unsigned)b_sp[(tt * 2) * 32 + r32] << 16), bs1 = __uint_as_float((unsigned)b_sp[(tt * 2 + 1) * 32 + r32] << 16);
            LDS_AS u32x4* z1p = (LDS_AS u32x4*)((lds_ptr)lds + 49152 + w * 4096) + l;
            {
                f32x16 acc[2]; acc[0] = f32x16{}; acc[1] = f32x16{};
                const int zoff = 8192 + (8 * h + q4) * 256 + ((fb ^ q4) << 6) + b16 * 32 + p4 * 8;
                bf16x8 aa[2], ba[2][2], ab[2], bb[2][2];
                auto rdh = [&](int s, int half, bf16x8 (&af)[2], bf16x8 (&bf)[2][2]) {
                    lds_cptr base = (lds_cptr)lds + (s % 3) * SP_ST;
#pragma unroll
                    for (int kk = 0; kk < 2; ++kk) {
                        const int ks = 2 * half + kk;
                        af[kk] = trfrag(base + zoff + ks * 4096, base + zoff + ks * 4096 + 1024);
                        bf[0][kk] = ldsfrag(base + boff[ks]); bf[1][kk] = ldsfrag(base + boff[ks] + 4096);
                    }
                };
                auto mm = [&](bf16x8 (&af)[2], bf16x8 (&bf)[2][2]) {
#pragma unroll
                    for (int kk = 0; kk < 2; ++kk) { acc[0] = MFMA(af[kk], bf[0][kk], acc[0]); acc[1] = MFMA(af[kk], bf[1][kk], acc[1]); }
                };
                bar_only();
                rdh(0, 0, aa, ba);
#pragma unroll 1
                for (int s = 0; s < 7; ++s) {
                    rdh(s, 1, ab, bb); mm(aa, ba); interleave<4, 2>(); SB();
                    if (s == 1) drain_stores();
                    bar_only();
                    if (s == 1 && tid == 0) __hip_atomic_fetch_add(sync_word(ws, SY_LN + (g & 15)), 1u, RLX_AGENT);
                    rdh(s + 1, 0, aa, ba); mm(ab, bb); interleave<4, 2>(); SB();
                }
                rdh(7, 1, ab, bb); mm(aa, ba); interleave<4, 2>(); SB();
                mm(ab, bb);
                bar_only();
                if (tid == 0) TL(5, 0, 101);
#pragma unroll
                for (int tb = 0; tb < 2; ++tb) {
                    const float og = 1.0f - (tb ? gt1 : gt0);
#pragma unroll
                    for (int gq = 0; gq < 2; ++gq) {
                        u32x4 zv;
#pragma unroll
                        for (int e = 0; e < 4; ++e) zv[e] = cvtpk(gelu_tanh(acc[tb][8 * gq + 2 * e]) * og, gelu_tanh(acc[tb][8 * gq + 2 * e + 1]) * og);
                        z1p[(tb * 2 + gq) * 64] = zv;
                    }
                }
            }
            f32x16 acc[2]; acc[0] = f32x16{}; acc[1] = f32x16{};
            {
                if (tid == 0) TL(5, 0, 102);
                bar_only();
                if (tid == 0) TL(5, 0, 103);
                bf16x8 za[4], zb[4];
                const size_t zoff = WS_Z2N + (size_t)fbg * 128 * 1024 + lo;
                auto ldz = [&](int s, int ks) { const int sr = (min(s, 31) + rot) & 31; return ld16_dev(ws, zoff + (size_t)((sr * 4 + ks) * 1024)); };
#pragma unroll
                for (int ks = 0; ks < 4; ++ks) za[ks] = ldz(0, ks);
#pragma unroll
                for (int ks = 0; ks < 4; ++ks) zb[ks] = ldz(1, ks);
                bf16x8 bx[2][2], by[2][2];
                auto rdh = [&](int s, int half, bf16x8 (&bf)[2][2]) {
                    lds_cptr base = (lds_cptr)lds + (s % 6) * 8192;
#pragma unroll
                    for (int kk = 0; kk < 2; ++kk) { bf[0][kk] = ldsfrag(base + boff[2 * half + kk]); bf[1][kk] = ldsfrag(base + boff[2 * half + kk] + 4096); }
                };
                auto stage = [&](int s, bf16x8 (&z)[4], bool last) {
                    rdh(s, 1, by);
                    acc[0] = MFMA(z[0], bx[0][0], acc[0]); acc[1] = MFMA(z[0], bx[1][0], acc[1]);
                    acc[0] = MFMA(z[1], bx[0][1], acc[0]); acc[1] = MFMA(z[1], bx[1][1], acc[1]); interleave<4, 1>(); SB();
                    z[0] = ldz(s + 2, 0); z[1] = ldz(s + 2, 1); SB();
                    if (!last) { bar_only(); rdh(s + 1, 0, bx); }
                    acc[0] = MFMA(z[2], by[0][0], acc[0]); acc[1] = MFMA(z[2], by[1][0], acc[1]);
                    acc[0] = MFMA(z[3], by[0][1], acc[0]); acc[1] = MFMA(z[3], by[1][1], acc[1]); interleave<4, 1>(); SB();
                    z[2] = ldz(s + 2, 2); z[3] = ldz(s + 2, 3); SB();
                };
                bar_only();
                rdh(0, 0, bx);
#pragma unroll 1
                for (int s = 0; s < 30; s += 2) { stage(s, za, false); stage(s + 1, zb, false); }
                stage(30, za, false); stage(31, zb, true);
                if (tid == 0) TL(5, 0, 104);
            }
#pragma unroll
            for (int tb = 0; tb < 2; ++tb) {
                const float bs = tb ? bs1 : bs0;
#pragma unroll
                for (int gq = 0; gq < 2; ++gq) {
                    u32x4 ov; const u32x4 zv = z1p[(tb * 2 + gq) * 64];
#pragma unroll
                    for (int e = 0; e < 4; ++e) ov[e] = cvtpk((acc[tb][8 * gq + 2 * e] + bs) * bf_lo(zv[e]), (acc[tb][8 * gq + 2 * e + 1] + bs) * bf_hi(zv[e]));
                    st16_wt(ws, WS_HO + ((size_t)((tt * 2 + tb) * 88 + 32 + fg + gq) * 1024) + lo, ov);
                }
            }
            drain_stores();
            bar_only();
            if (tid == 0) __hip_atomic_fetch_add(sync_word(ws, SY_H + tt), 1u, RLX_AGENT);
        }
    }
    STAMP(5, 3);
    if (tid == 0) TL(5, 0, 105);
    {
        if (GPRIO) __builtin_amdgcn_s_setprio(GPRIO);
        const int tb = w & 1, kq = w >> 1;
        const int prow = 8 * w + (l >> 3);
        const unsigned pv = (unsigned)(prow * D_ + ((l & 7) ^ (((prow >> 1) & 1) << 2)) * 8) * 2;
        const char* w0 = (const char*)(w_out + ft * 64);
        const char* w1 = (const char*)(wo + ft * 64);
        auto issue = [&](int s) {
            const unsigned dst = lds0 + (s & 3) * FN_ST + w * 1024;
            const char* wp = s < 7 ? w0 + (size_t)s * 128 * D_ * 2 : w1 + (size_t)(s - 7) * 128 * D_ * 2;
            glds16s(wp, pv, dst); glds16s(wp + (size_t)64 * D_ * 2, pv, dst + 8192);
        };
        issue(0); issue(1); issue(2); issue(3);
        const int woff0 = (kq * 32 + 4 * h + q4) * 128 + (((b16 * 2 + (p4 >> 1)) ^ ((q4 >> 1) << 2)) << 4) + (p4 & 1) * 8, woff1 = woff0 ^ 64;
        const size_t hoff = WS_HO + ((size_t)((tt * 2 + tb) * 88 + 2 * kq) * 1024) + lo;
        f32x16 acc[2]; acc[0] = f32x16{}; acc[1] = f32x16{};
        bf16x8 hf[14];
        auto comp = [&](int s, const bf16x8& h0, const bf16x8& h1) {
            lds_cptr base = (lds_cptr)lds + (s & 3) * FN_ST;
            const bf16x8 w00 = trfrag(base + woff0, base + woff0 + 1024), w01 = trfrag(base + woff1, base + woff1 + 1024);
            const bf16x8 w10 = trfrag(base + woff0 + 2048, base + woff0 + 3072), w11 = trfrag(base + woff1 + 2048, base + woff1 + 3072);
            acc[0] = MFMA(w00, h0, acc[0]); acc[1] = MFMA(w01, h0, acc[1]);
            acc[0] = MFMA(w10, h1, acc[0]); acc[1] = MFMA(w11, h1, acc[1]);
            SB();
        };
        if (w == 7) wait_words(ws, SY_H + tt, 1, 7u, l);
        if (w == 0) TL(5, 1, 99);
        bar_only();
        if (w == 0) TL(5, 1, 100);
#pragma unroll
        for (int q = 0; q < 14; ++q) hf[q] = ld16_dev(ws, hoff + (size_t)((32 + 8 * (q >> 1) + (q & 1)) * 1024));
        wait_vm<20>(); bar_only();            comp(0, hf[0], hf[1]);
        wait_vm<18>(); bar_only(); issue(4);  comp(1, hf[2], hf[3]);
        wait_vm<18>(); bar_only(); issue(5);  comp(2, hf[4], hf[5]);
        wait_vm<18>(); bar_only(); issue(6);  comp(3, hf[6], hf[7]);
        wait_vm<4>();  bar_only(); issue(7);  comp(4, hf[8], hf[9]);
        wait_vm<4>();  bar_only(); issue(8);  comp(5, hf[10], hf[11]);
        wait_vm<4>();  bar_only(); issue(9);  comp(6, hf[12], hf[13]);
        if (w == 0) TL(5, 1, 101);
        if (w == 7) wait_words(ws, SY_O + (tt >> 2), 1, 32u, l);
        bar_only();
        if (w == 0) TL(5, 1, 102);
#pragma unroll
        for (int q = 0; q < 8; ++q) hf[q] = ld16_dev(ws, hoff + (size_t)((8 * (q >> 1) + (q & 1)) * 1024));
        wait_vm<12>(); bar_only(); issue(10); comp(7, hf[0], hf[1]);
        wait_vm<12>(); bar_only();            comp(8, hf[2], hf[3]);
        wait_vm<10>(); bar_only();            comp(9, hf[4], hf[5]);
        wait_vm<0>();  bar_only();            comp(10, hf[6], hf[7]);
        if (w == 0) TL(5, 1, 103);
        bar_only();
        LDS_AS f32x4* red = (LDS_AS f32x4*)lds;
#pragma unroll
        for (int db = 0; db < 2; ++db)
#pragma unroll
            for (int q = 0; q < 4; ++q) red[((w * 2 + db) * 4 + q) * 64 + l] = f32x4{acc[db][4 * q], acc[db][4 * q + 1], acc[db][4 * q + 2], acc[db][4 * q + 3]};
        bar_only();
        const int db = kq & 1, gq = kq >> 1;
        f32x4 v0 = {0.f, 0.f, 0.f, 0.f}, v1 = v0;
#pragma unroll
        for (int q = 0; q < 4; ++q) { const int ww = q * 2 + tb; v0 += red[((ww * 2 + db) * 4 + 2 * gq) * 64 + l]; v1 += red[((ww * 2 + db) * 4 + 2 * gq + 1) * 64 + l]; }
        const unsigned a0 = cvtpk(v0[0], v0[1]), a1 = cvtpk(v0[2], v0[3]), b0 = cvtpk(v1[0], v1[1]), b1 = cvtpk(v1[2], v1[3]);
        auto r0 = __builtin_amdgcn_permlane32_swap(a0, b0, false, false);
        auto r1 = __builtin_amdgcn_permlane32_swap(a1, b1, false, false);
        const u32x4 ov = {r0[0], r1[0], r0[1], r1[1]};
        *(u32x4*)(out + (size_t)(tt * 64 + tb * 32 + r32) * D_ + ft * 64 + db * 32 + gq * 16 + h * 8) = ov;
        if (w == 0) TL(5, 1, 104);
    }
}

extern "C" __global__ __launch_bounds__(512, 4) void k_main(const u16* __restrict__ x, const u16* __restrict__ w_in, const u16* __restrict__ w_sp, const u16* __restrict__ b_sp,
                                                 const u16* __restrict__ wo, const u16* __restrict__ w_out, char* __restrict__ ws, u16* __restrict__ out) {
    __shared__ __attribute__((aligned(1024))) char lds[AT_LDS > SP_LDS ? AT_LDS : SP_LDS];
    const int b = blockIdx.x;
    if (b < 256) {
        STAMP(2, 0);
        attn_block(ws, lds, (b & 7) * 4 + ((b >> 3) & 3), b >> 5, threadIdx.x);
        STAMP(2, 2);
    } else {
        STAMP(5, 0);
        gate_block(x, w_in, w_sp, b_sp, wo, w_out, ws, out, lds, b - 256, threadIdx.x);
        STAMP(5, 2);
    }
}

extern "C" void kernel_launch(void* const* d_in, const int* in_sizes, int n_in, void* d_out, int out_size, void* d_ws, size_t ws_size, hipStream_t stream) {
    const u16* x = (const u16*)d_in[0];
    const u16* wqkv = (const u16*)d_in[1];
    const u16* wo = (const u16*)d_in[2];
    const u16* w_in = (const u16*)d_in[4];
    const u16* w_sp = (const u16*)d_in[5];
    const u16* b_sp = (const u16*)d_in[6];
    const u16* w_out = (const u16*)d_in[7];
    const u16* gate_w = (const u16*)d_in[8];
    char* ws = (char*)d_ws;
    k1_proj<<<256, 512, 0, stream>>>(x, wqkv, w_in, gate_w, ws);
    k_main<<<512, 512, 0, stream>>>(x, w_in, w_sp, b_sp, wo, w_out, ws, (u16*)d_out);
}
```

```cpp
#include <hip/hip_runtime.h>
#include <hip/hip_bf16.h>
#include <stdint.h>

typedef __attribute__((ext_vector_type(8))) short bf16x8;
typedef __attribute__((ext_vector_type(4))) short s16x4;
typedef __attribute__((ext_vector_type(16))) float f32x16;
typedef __attribute__((ext_vector_type(4))) unsigned u32x4;
typedef __attribute__((ext_vector_type(2))) float f32x2;
typedef __attribute__((ext_vector_type(4))) float f32x4;
typedef __attribute__((ext_vector_type(2))) __bf16 bf16x2_t;
#define LDS_AS __attribute__((address_space(3)))
typedef LDS_AS const char* lds_cptr;
typedef LDS_AS char* lds_ptr;
typedef short v4i16_t __attribute__((ext_vector_type(4)));
typedef unsigned short u16;
#define DI __device__ __forceinline__

#define MFMA(a, b, c) __builtin_amdgcn_mfma_f32_32x32x16_bf16((a), (b), (c), 0, 0, 0)
#define SB() __builtin_amdgcn_sched_barrier(0)
template <int NM, int ND> DI void interleave() {
#pragma unroll
    for (int i = 0; i < NM; ++i) { __builtin_amdgcn_sched_group_barrier(0x008, 1, 0); __builtin_amdgcn_sched_group_barrier(0x100, ND, 0); }
}
#define TL(kid, who, idx) do {} while (0)
#define WAIT_BAR(N) asm volatile("s_waitcnt vmcnt(" #N ") lgkmcnt(0)\n\ts_barrier" ::: "memory")

#define K1_ROT(mt, nl) ((mt) & 7)
#define SP_ROT(tl, fl) ((tl) * 4 + (fl) * 2)
#define PACE 1
#define PACE_() do { if (PACE == 1) __builtin_amdgcn_s_sleep(1); else if (PACE == 2) asm volatile("s_nop 7"); else if (PACE == 3) asm volatile("s_nop 11"); else if (PACE == 4) asm volatile("s_nop 15"); SB(); } while (0)
#define LPRIO 0
#define K1_WT 1
#define K1_ST16(off, v) st16_wt(ws, (off), (v))
#define GPF 3
#define K1_PF 1
#define K1_PFS 4
#define GPRIO 2
#define FN_ROT(i, dt) ((((i) & 3) * 3 + (dt)) % 11)
constexpr int S_ = 2048, D_ = 512, FH = 896;
constexpr float QSCALE = 0.25f * 1.4426950408889634f;

constexpr size_t WS_QF    = 0;
constexpr size_t WS_KV    = WS_QF + 32u * 64 * 1024;
constexpr size_t WS_Z2    = WS_KV + 32u * 64 * 2048;
constexpr size_t WS_Z2N   = WS_Z2 + 64u * 56 * 1024;
constexpr size_t WS_HO    = WS_Z2N + 28u * 128 * 1024;
constexpr size_t WS_STATS = WS_HO + 64u * 88 * 1024;
constexpr size_t WS_KMAX  = WS_STATS + 2048u * 28 * 8;
constexpr size_t WS_GATE  = WS_KMAX + 64u * 32 * 4;
constexpr size_t WS_SYNC  = WS_GATE + 2048u * 4;
constexpr int SY_LN = 0, SY_H = 16, SY_O = 48, SY_TMO = 56, SY_N = 57;
constexpr size_t WS_END   = WS_SYNC + SY_N * 256;

#define STAMP(kid, slot) do {} while (0)
DI unsigned cvtpk(float a, float b) { f32x2 f = {a, b}; bf16x2_t r = __builtin_convertvector(f, bf16x2_t); return __builtin_bit_cast(unsigned, r); }
DI float bf_lo(unsigned u) { return __uint_as_float(u << 16); }
DI float bf_hi(unsigned u) { return __uint_as_float(u & 0xffff0000u); }
DI void glds16(const void* g, unsigned lds_base) {
    unsigned sv;
    asm volatile("s_mov_b32 %0, m0\n\ts_mov_b32 m0, %2\n\ts_nop 0\n\tglobal_load_lds_dwordx4 %1, off\n\ts_mov_b32 m0, %0"
                 : "=&s"(sv) : "v"(g), "s"(lds_base) : "memory");
}
DI void glds16s(const void* ubase, unsigned voff, unsigned lds_base) {
    asm volatile("s_mov_b32 m0, %2\n\ts_nop 0\n\tglobal_load_lds_dwordx4 %0, %1" :: "v"(voff), "s"(ubase), "s"(lds_base) : "memory");
}
#define GAS __attribute__((address_space(1)))
typedef GAS unsigned gu32;
#define RLX_AGENT __ATOMIC_RELAXED, __HIP_MEMORY_SCOPE_AGENT
DI gu32* sync_word(char* ws, int i) { return (gu32*)(ws + WS_SYNC + (size_t)i * 256); }
DI void st16_wt(char* ws, size_t off, u32x4 v) {
    __builtin_amdgcn_raw_buffer_store_b128(v, __builtin_amdgcn_make_buffer_rsrc(ws, 0, (int)WS_END, 0x00020000), (int)off, 0, 16);
}
DI bf16x8 ld16_dev(char* ws, size_t off) {
    return __builtin_bit_cast(bf16x8, __builtin_amdgcn_raw_buffer_load_b128(__builtin_amdgcn_make_buffer_rsrc(ws, 0, (int)WS_END, 0x00020000), (int)off, 0, 16));
}
DI void prefetch_line(const void* ubase, unsigned voff, unsigned lds_dump) {
    asm volatile("s_mov_b32 m0, %2\n\ts_nop 0\n\tglobal_load_lds_dword %0, %1" :: "v"(voff), "s"(ubase), "s"(lds_dump) : "memory");
}
DI void prefetch_ptr(const void* g, unsigned lds_dump) {
    asm volatile("s_mov_b32 m0, %1\n\ts_nop 0\n\tglobal_load_lds_dword %0, off" :: "v"(g), "s"(lds_dump) : "memory");
}
DI void drain_stores() { asm volatile("s_waitcnt vmcnt(0)" ::: "memory"); }
DI void wait_words(char* ws, int first, int n, unsigned want, int lane) {
    gu32* p = sync_word(ws, first + lane % n);
    for (unsigned spins = 0;; ++spins) {
        if (__all(__hip_atomic_load(p, RLX_AGENT) >= want)) break;
        if (spins > (1u << 22)) { if (lane == 0) __hip_atomic_store(sync_word(ws, SY_TMO), 1u, RLX_AGENT); break; }
        __builtin_amdgcn_s_sleep(2);
    }
    asm volatile("" ::: "memory");
}
DI void bar_only() { asm volatile("" ::: "memory"); __builtin_amdgcn_s_waitcnt(0xC07F); __builtin_amdgcn_s_barrier(); asm volatile("" ::: "memory"); }
template <int N> DI void wait_bar() { asm volatile("s_waitcnt vmcnt(%0)" :: "i"(N) : "memory"); bar_only(); }
DI s16x4 vtr(lds_cptr p) { return __builtin_bit_cast(s16x4, __builtin_amdgcn_ds_read_tr16_b64_v4i16((LDS_AS v4i16_t*)p)); }
DI bf16x8 trfrag(lds_cptr p0, lds_cptr p1) { s16x4 lo = vtr(p0), hi = vtr(p1); return __builtin_shufflevector(lo, hi, 0, 1, 2, 3, 4, 5, 6, 7); }
DI bf16x8 ldsfrag(lds_cptr p) { return *(const LDS_AS bf16x8*)p; }
DI u32x4 pack8(const f32x16& a, int o, float sc) {
    u32x4 r; r[0] = cvtpk(a[o] * sc, a[o + 1] * sc); r[1] = cvtpk(a[o + 2] * sc, a[o + 3] * sc);
    r[2] = cvtpk(a[o + 4] * sc, a[o + 5] * sc); r[3] = cvtpk(a[o + 6] * sc, a[o + 7] * sc); return r;
}
DI float swap32_sum(float v) { auto r = __builtin_amdgcn_permlane32_swap(__float_as_uint(v), __float_as_uint(v), false, false); return __uint_as_float(r[0]) + __uint_as_float(r[1]); }
DI float swap32_max(float v) { auto r = __builtin_amdgcn_permlane32_swap(__float_as_uint(v), __float_as_uint(v), false, false); return fmaxf(__uint_as_float(r[0]), __uint_as_float(r[1])); }
DI float dpp_max16(float v) {
    v = fmaxf(v, __uint_as_float(__builtin_amdgcn_update_dpp(0u, __float_as_uint(v), 0x128, 0xf, 0xf, false)));
    v = fmaxf(v, __uint_as_float(__builtin_amdgcn_update_dpp(0u, __float_as_uint(v), 0x124, 0xf, 0xf, false)));
    v = fmaxf(v, __uint_as_float(__builtin_amdgcn_update_dpp(0u, __float_as_uint(v), 0x122, 0xf, 0xf, false)));
    v = fmaxf(v, __uint_as_float(__builtin_amdgcn_update_dpp(0u, __float_as_uint(v), 0x121, 0xf, 0xf, false)));
    return v;
}
DI float wave_max_fast(float v) {
    v = dpp_max16(v);
    auto r = __builtin_amdgcn_permlane16_swap(__float_as_uint(v), __float_as_uint(v), false, false);
    v = fmaxf(__uint_as_float(r[0]), __uint_as_float(r[1]));
    return swap32_max(v);
}

DI float gelu_tanh(float x) {
    const float c1 = -2.0f * 0.7978845608028654f * 1.4426950408889634f, c2 = c1 * 0.044715f;
    const float a = x * __builtin_fmaf(x * x, c2, c1);
    return x * __builtin_amdgcn_rcpf(1.0f + __builtin_amdgcn_exp2f(a));
}

constexpr int K1_ST = 40960, K1_LDS = 3 * K1_ST + 1024 + 256;

template <int TYPE>
DI void k1_body(const u16* __restrict__ x, const u16* __restrict__ wqkv, const u16* __restrict__ w_in, const u16* __restrict__ gate_w,
                const u16* __restrict__ w_sp, const u16* __restrict__ wo, const u16* __restrict__ w_out,
                char* __restrict__ ws, char* lds, int mt, int p, int nl, int tid) {
    if (tid == 0) TL(0, 1, 99);
    constexpr int NG = TYPE == 0 ? 3 : (TYPE == 1 ? 2 : 1);
    constexpr int NA = TYPE == 2 ? 1 : (TYPE == 3 ? 2 : NG);
    constexpr int P = 4 + 2 * NG;
    const int l = tid & 63, w = __builtin_amdgcn_readfirstlane(tid >> 6);
    const int rot = K1_ROT(mt, nl);
    if (w >= 4) {
        const int j = w - 4;
        const unsigned lds0 = (unsigned)(uintptr_t)lds;
        const int row = 8 * j + (l >> 3);
        const unsigned xv = (unsigned)(row * D_ + ((l & 7) ^ ((row >> 1) & 7)) * 8) * 2;
        const int wch = ((l & 7) ^ (((row >> 1) & 1) << 2)) * 8;
        const unsigned qv = (unsigned)(row * 1536 + wch) * 2, zv = (unsigned)(row * 1792 + wch) * 2;
        const char* xu = (const char*)(x + (size_t)mt * 128 * D_);
        const char* g0 = (const char*)(wqkv + p * 64 + (TYPE == 0 ? 0 : 1024));
        const char* g1 = TYPE == 0 ? (const char*)(wqkv + p * 64 + 512) : (const char*)(w_in + FH + (8 + p) * 64);
        const char* g2 = (const char*)(w_in + FH + p * 64);
        auto issue = [&](int s) {
            const unsigned dst = lds0 + (s % 3) * K1_ST + j * 1024;
            const int sr = (s + rot) & 7;
#pragma unroll
            for (int i = 0; i < 4; ++i) glds16s(xu + (size_t)i * 32 * D_ * 2 + sr * 128, xv, dst + i * 4096);
            glds16s(g0 + (size_t)sr * 64 * 3072, qv, dst + 16384); glds16s(g0 + (size_t)(sr * 64 + 32) * 3072, qv, dst + 16384 + 4096);
            if (NG >= 2) { constexpr int ld = TYPE == 0 ? 3072 : 3584; const unsigned gv = TYPE == 0 ? qv : zv;
                glds16s(g1 + (size_t)sr * 64 * ld, gv, dst + 16384 + 8192); glds16s(g1 + (size_t)(sr * 64 + 32) * ld, gv, dst + 16384 + 8192 + 4096); }
            if (NG >= 3) { glds16s(g2 + (size_t)sr * 64 * 3584, zv, dst + 32768); glds16s(g2 + (size_t)(sr * 64 + 32) * 3584, zv, dst + 32768 + 4096); }
        };
        if (TYPE == 3 && j == 0) glds16s(gate_w, l * 16, lds0 + 3 * K1_ST);
        issue(0); issue(1); issue(2);
        wait_bar<2 * P>();
#pragma unroll 1
        for (int s = 0; s < 7; ++s) {
            if (j == 0) TL(0, 0, 3 * s);
            if (s + 2 < 8) asm volatile("s_waitcnt vmcnt(%0)" :: "i"(P) : "memory"); else asm volatile("s_waitcnt vmcnt(0)" ::: "memory");
            if (j == 0) TL(0, 0, 3 * s + 1);
            bar_only();
            if (j == 0) TL(0, 0, 3 * s + 2);
            if (s + 3 < 8) issue(s + 3);
        }
        return;
    }
    {
        const int xcd = blockIdx.x & 7, idx = blockIdx.x >> 3;
        const unsigned dump = (unsigned)(uintptr_t)lds + 3 * K1_ST + 1024;
        if ((K1_PF & 1) && w < 2) { const int L = idx * 128 + w * 64 + l; prefetch_line(w_in, (unsigned)((L >> 3) * 3584 + min((xcd >> 2) * 1024 + (L & 7) * 128, 1664)), dump); }
        if ((K1_PF & 2) && w >= 1) { const int L = min(idx * 176 + (w - 1) * 64 + l, idx * 176 + 175), row = L >> 2; const unsigned co = (unsigned)((xcd >> 2) * 512 + (L & 3) * 128);
            prefetch_ptr(row < 896 ? (const char*)w_out + (size_t)row * 1024 + co : (const char*)wo + (size_t)(row - 896) * 1024 + co, dump); }
    }
    const int rh = w & 1, ch = w >> 1, h = l >> 5, r32 = l & 31;
    int xo[4];
    { const int row = rh * 64 + r32, f = (row >> 1) & 7;
#pragma unroll
      for (int c = 0; c < 4; ++c) xo[c] = row * 128 + (((2 * c + h) ^ f) << 4); }
    const int q4 = (l & 15) >> 2, p4 = l & 3, b16 = (l >> 4) & 1;
    const int wl = 16384 + (8 * h + q4) * 128 + (((ch * 4 + b16 * 2 + (p4 >> 1)) ^ ((q4 >> 1) << 2)) << 4) + (p4 & 1) * 8;
    f32x16 acc[NA][2];
#pragma unroll
    for (int g = 0; g < NA; ++g) { acc[g][0] = f32x16{}; acc[g][1] = f32x16{}; }
    bf16x8 xa[2][2], wa[NG][2], xb[2][2], wb[NG][2], ga[2], gb[2];
    auto rdh = [&](int s, int half, bf16x8 (&xf)[2][2], bf16x8 (&wf)[NG][2], bf16x8 (&gf)[2]) {
        lds_cptr base = (lds_cptr)lds + (s % 3) * K1_ST;
#pragma unroll
        for (int kk = 0; kk < 2; ++kk) {
            const int ks = 2 * half + kk;
            xf[0][kk] = ldsfrag(base + xo[ks]); xf[1][kk] = ldsfrag(base + xo[ks] + 4096);
#pragma unroll
            for (int g = 0; g < NG; ++g) wf[g][kk] = trfrag(base + wl + g * 8192 + ks * 2048, base + wl + g * 8192 + ks * 2048 + 512);
            if (TYPE == 3) gf[kk] = ldsfrag((lds_cptr)lds + 3 * K1_ST + (((s + rot) & 7) * 4 + ks) * 32 + h * 16);
        }
    };
    auto mm = [&](bf16x8 (&xf)[2][2], bf16x8 (&wf)[NG][2], bf16x8 (&gf)[2]) {
#pragma unroll
        for (int kk = 0; kk < 2; ++kk)
#pragma unroll
            for (int rb = 0; rb < 2; ++rb) {
                if (TYPE == 0) {
#pragma unroll
                    for (int g = 0; g < 3; ++g) acc[g % NA][rb] = MFMA(wf[g % NG][kk], xf[rb][kk], acc[g % NA][rb]);
                } else {
                    acc[0][rb] = MFMA(xf[rb][kk], wf[0][kk], acc[0][rb]);
                    if (TYPE == 1) acc[NA - 1][rb] = MFMA(wf[NG - 1][kk], xf[rb][kk], acc[NA - 1][rb]);
                    if (TYPE == 3) acc[NA - 1][rb] = MFMA(gf[kk], xf[rb][kk], acc[NA - 1][rb]);
                }
            }
    };
    if (w == 0) TL(0, 1, 100);
    bar_only();
    if (w == 0) TL(0, 1, 101);
    rdh(0, 0, xa, wa, ga);
#pragma unroll 1
    for (int s = 0; s < 7; ++s) {
        rdh(s, 1, xb, wb, gb); mm(xa, wa, ga); interleave<4 * NA, 2>(); SB();
        if (w == 0) TL(0, 1, 2 * s);
        bar_only();
        if (w == 0) TL(0, 1, 2 * s + 1);
        if ((K1_PF & 4) && s == K1_PFS) {
            const int xcd = blockIdx.x & 7, idx = blockIdx.x >> 3;
#pragma unroll
            for (int i = 0; i < 2; ++i) { const int L = (idx * 4 + w) * 128 + i * 64 + l; prefetch_line(w_sp + (size_t)(xcd & 3) * 512 * S_, (unsigned)((L >> 5) * S_ * 2 + (L & 31) * 128), (unsigned)(uintptr_t)lds + 3 * K1_ST + 1024); }
        }
        rdh(s + 1, 0, xa, wa, ga); mm(xb, wb, gb); interleave<4 * NA, 2>(); SB();
    }
    rdh(7, 1, xb, wb, gb); mm(xa, wa, ga); interleave<4 * NA, 2>(); SB();
    mm(xb, wb, gb);
    STAMP(0, 1);
    if (w == 0) TL(0, 1, 102);
#pragma unroll
    for (int rb = 0; rb < 2; ++rb) {
        const int ttile = mt * 4 + rh * 2 + rb;
        if (TYPE == 0) {
            const int hd0 = 4 * p + 2 * ch;
            const f32x16 &q = acc[0][rb], &k = acc[1 % NA][rb];
            K1_ST16(WS_QF + ((size_t)(hd0 * 64 + ttile) * 1024) + l * 16, pack8(q, 0, QSCALE));
            K1_ST16(WS_QF + ((size_t)((hd0 + 1) * 64 + ttile) * 1024) + l * 16, pack8(q, 8, QSCALE));
            K1_ST16(WS_KV + ((size_t)(hd0 * 64 + ttile) * 2048) + l * 16, pack8(k, 0, 1.0f));
            K1_ST16(WS_KV + ((size_t)((hd0 + 1) * 64 + ttile) * 2048) + l * 16, pack8(k, 8, 1.0f));
            float n0 = 0.f, n1 = 0.f;
#pragma unroll
            for (int r = 0; r < 8; ++r) { n0 = __builtin_fmaf(k[r], k[r], n0); n1 = __builtin_fmaf(k[8 + r], k[8 + r], n1); }
            n0 = wave_max_fast(swap32_sum(n0)); n1 = wave_max_fast(swap32_sum(n1));
            if (l == 0) { float* km = (float*)(ws + WS_KMAX) + ttile * 32 + hd0; km[0] = n0 * 1.01f; km[1] = n1 * 1.01f; }
        } else {
            const int hd = 4 * p + 2 * ch + (r32 >> 4), d = r32 & 15;
            const size_t vp = WS_KV + ((size_t)(hd * 64 + ttile) * 2048) + 1024 + h * 256 + d * 16;
            K1_ST16(vp, pack8(acc[0][rb], 0, 1.0f)); K1_ST16(vp + 512, pack8(acc[0][rb], 8, 1.0f));
            if (TYPE == 3 && ch == 0 && h == 0)
                ((float*)(ws + WS_GATE))[ttile * 32 + r32] = __builtin_amdgcn_rcpf(1.0f + __builtin_amdgcn_exp2f(-1.4426950408889634f * acc[NA - 1][rb][0]));
        }
        if (TYPE <= 1) {
            f32x16& z = acc[NA - 1][rb];
            const int zg = TYPE == 0 ? p : 8 + p;
            float s1 = 0.f, s2 = 0.f;
#pragma unroll
            for (int r = 0; r < 16; ++r) { const float v = gelu_tanh(z[r]); z[r] = v; s1 += v; s2 = __builtin_fmaf(v, v, s2); }
            const int fb = 2 * zg + ch;
            const size_t zp = WS_Z2 + ((size_t)(ttile * 56 + 2 * fb) * 1024) + l * 16;
            K1_ST16(zp, pack8(z, 0, 1.0f)); K1_ST16(zp + 1024, pack8(z, 8, 1.0f));
            s1 = swap32_sum(s1); s2 = swap32_sum(s2);
            if (h == 0) ((f32x2*)(ws + WS_STATS))[(size_t)(ttile * 32 + r32) * 28 + fb] = f32x2{s1, s2};
        }
    }
    if (K1_PF) drain_stores();
    if (w == 0) TL(0, 1, 103);
}

extern "C" __global__ __launch_bounds__(512) void k1_proj(const u16* __restrict__ x, const u16* __restrict__ wqkv, const u16* __restrict__ w_in, const u16* __restrict__ gate_w,
                                               const u16* __restrict__ w_sp, const u16* __restrict__ wo, const u16* __restrict__ w_out, char* __restrict__ ws) {
    __shared__ __attribute__((aligned(1024))) char lds[K1_LDS];
    const int b = blockIdx.x, xcd = b & 7, idx = b >> 3;
    const int mt = (xcd & 1) * 8 + (idx & 7), nt = (xcd >> 1) * 4 + (idx >> 3), p = nt >> 1;
    STAMP(0, 0);
    if (b == 255 && threadIdx.x < SY_N) *(unsigned*)(ws + WS_SYNC + threadIdx.x * 256) = 0u;
    if (!(nt & 1)) k1_body<0>(x, wqkv, w_in, gate_w, w_sp, wo, w_out, ws, lds, mt, p, idx >> 3, threadIdx.x);
    else if (p < 6) k1_body<1>(x, wqkv, w_in, gate_w, w_sp, wo, w_out, ws, lds, mt, p, idx >> 3, threadIdx.x);
    else if (p == 6) k1_body<2>(x, wqkv, w_in, gate_w, w_sp, wo, w_out, ws, lds, mt, p, idx >> 3, threadIdx.x);
    else k1_body<3>(x, wqkv, w_in, gate_w, w_sp, wo, w_out, ws, lds, mt, p, idx >> 3, threadIdx.x);
    STAMP(0, 2);
}

struct LnStat { float rstd, nm; };
DI LnStat ln_stats(const f32x4 (&pv)[7]) {
    float s1 = 0.f, s2 = 0.f;
#pragma unroll
    for (int i = 0; i < 7; ++i) { const f32x4 v = pv[i]; s1 += v[0] + v[2]; s2 += v[1] + v[3]; }
    s1 = swap32_sum(s1); s2 = swap32_sum(s2);
    const float mean = s1 * (1.0f / FH);
    const float var = fmaxf(s2 * (1.0f / FH) - mean * mean, 0.f);
    LnStat r; r.rstd = __builtin_amdgcn_rsqf(var + 1e-5f); r.nm = -mean * r.rstd; return r;
}
DI void ln_block(char* __restrict__ ws, char* scratch, int ttile, int fb, const u32x4& v0, const u32x4& v1, LnStat st, int l) {
    const int h = l >> 5, r32 = l & 31, q4 = (l & 15) >> 2, p4 = l & 3, b16 = (l >> 4) & 1;
    u32x4 ov[2];
#pragma unroll
    for (int g = 0; g < 2; ++g) {
        const u32x4 v = g ? v1 : v0;
        unsigned o[4];
#pragma unroll
        for (int e = 0; e < 4; ++e) o[e] = cvtpk(__builtin_fmaf(bf_lo(v[e]), st.rstd, st.nm), __builtin_fmaf(bf_hi(v[e]), st.rstd, st.nm));
        auto r0 = __builtin_amdgcn_permlane32_swap(o[0], o[2], false, false);
        auto r1 = __builtin_amdgcn_permlane32_swap(o[1], o[3], false, false);
        ov[g] = u32x4{r0[0], r1[0], r0[1], r1[1]};
    }
    lds_ptr tile = (lds_ptr)scratch;
#pragma unroll
    for (int kk = 0; kk < 2; ++kk) {
        if ((r32 >> 4) == kk) {
            *(LDS_AS u32x4*)(tile + (r32 & 15) * 64 + h * 16) = ov[0]; *(LDS_AS u32x4*)(tile + (r32 & 15) * 64 + 32 + h * 16) = ov[1]; }
        asm volatile("" ::: "memory");
        lds_cptr rp = (lds_cptr)tile + (8 * h + q4) * 64 + b16 * 32 + p4 * 8;
        const bf16x8 fr = trfrag(rp, rp + 256);
        asm volatile("" ::: "memory");
        st16_wt(ws, WS_Z2N + ((size_t)(fb * 128 + ttile * 2 + kk) * 1024) + l * 16, __builtin_bit_cast(u32x4, fr));
    }
}

constexpr int AT_TS = 2304, AT_SLOT = 8 * AT_TS, AT_LDS = 4 * AT_SLOT + 64;
#define ATTN_ASM \
    "s_mov_b32 %[sv], m0\n\t" \
    "v_mov_b32 v88, 0\n\t" \
    "v_mov_b32 v89, 0\n\t" \
    "v_mov_b32 v90, 0\n\t" \
    "v_mov_b32 v91, 0\n\t" \
    "v_mov_b32 v96, 0\n\t" \
    "v_mov_b32 v97, 0\n\t" \
    "v_mov_b32 v98, 0\n\t" \
    "v_mov_b32 v99, 0\n\t" \
    "v_mov_b32 v0, 0\n\t" \
    "v_mov_b32 v1, 0\n\t" \
    "v_mov_b32 v2, 0\n\t" \
    "v_mov_b32 v3, 0\n\t" \
    "v_mov_b32 v4, 0\n\t" \
    "v_mov_b32 v5, 0\n\t" \
    "v_mov_b32 v6, 0\n\t" \
    "v_mov_b32 v7, 0\n\t" \
    "v_mov_b32 v8, 0\n\t" \
    "v_mov_b32 v9, 0\n\t" \
    "v_mov_b32 v10, 0\n\t" \
    "v_mov_b32 v11, 0\n\t" \
    "v_mov_b32 v12, 0\n\t" \
    "v_mov_b32 v13, 0\n\t" \
    "v_mov_b32 v14, 0\n\t" \
    "v_mov_b32 v15, 0\n\t" \
    "s_mov_b32 %[cnt], 0\n\t" \
    "s_waitcnt vmcnt(4) lgkmcnt(0)\n\t" \
    "s_barrier\n\t" \
    "ds_read_b128 v[68:71], %[kl] offset:0\n\t" \
    "ds_read_b128 v[72:75], %[kl] offset:2304\n\t" \
    "ds_read_b128 v[76:79], %[v0l] offset:0\n\t" \
    "ds_read_b128 v[80:83], %[v1l] offset:0\n\t" \
    "s_waitcnt lgkmcnt(0)\n\t" \
    "s_setprio 3\n\t" \
    "v_mfma_f32_32x32x16_bf16 v[32:47], v[68:71], v[64:67], v[16:31]\n\t" \
    "s_setprio 0\n\t" \
    "s_nop 7\n\t" \
    "s_nop 3\n\t" \
    "Lloop%=:\n\t" \
    "s_setprio 3\n\t" \
    "v_mfma_f32_32x32x16_bf16 v[48:63], v[72:75], v[64:67], v[16:31]\n\t" \
    "s_setprio 2\n\t" \
    "v_exp_f32 v32, v32\n\t" \
    "ds_read_b128 v[68:71], %[kl] offset:4608\n\t" \
    "v_exp_f32 v33, v33\n\t" \
    "v_exp_f32 v34, v34\n\t" \
    "v_exp_f32 v35, v35\n\t" \
    "v_exp_f32 v36, v36\n\t" \
    "v_exp_f32 v37, v37\n\t" \
    "v_exp_f32 v38, v38\n\t" \
    "s_setprio 3\n\t" \
    "v_mfma_f32_32x32x16_bf16 v[0:15], v[88:91], v[96:99], v[0:15]\n\t" \
    "s_setprio 2\n\t" \
    "v_exp_f32 v39, v39\n\t" \
    "ds_read_b128 v[84:87], %[v0l] offset:2304\n\t" \
    "v_cvt_pk_bf16_f32 v92, v32, v33\n\t" \
    "v_cvt_pk_bf16_f32 v93, v34, v35\n\t" \
    "v_cvt_pk_bf16_f32 v94, v36, v37\n\t" \
    "v_cvt_pk_bf16_f32 v95, v38, v39\n\t" \
    "v_exp_f32 v40, v40\n\t" \
    "ds_read_b128 v[88:91], %[v1l] offset:2304\n\t" \
    "v_exp_f32 v41, v41\n\t" \
    "v_exp_f32 v42, v42\n\t" \
    "v_exp_f32 v43, v43\n\t" \
    "s_waitcnt lgkmcnt(4)\n\t" \
    "s_setprio 3\n\t" \
    "v_mfma_f32_32x32x16_bf16 v[0:15], v[76:79], v[92:95], v[0:15]\n\t" \
    "s_setprio 2\n\t" \
    "v_exp_f32 v44, v44\n\t" \
    "v_exp_f32 v45, v45\n\t" \
    "v_exp_f32 v46, v46\n\t" \
    "v_exp_f32 v47, v47\n\t" \
    "s_waitcnt lgkmcnt(2)\n\t" \
    "v_cvt_pk_bf16_f32 v96, v40, v41\n\t" \
    "v_cvt_pk_bf16_f32 v97, v42, v43\n\t" \
    "v_cvt_pk_bf16_f32 v98, v44, v45\n\t" \
    "v_cvt_pk_bf16_f32 v99, v46, v47\n\t" \
    "s_setprio 3\n\t" \
    "v_mfma_f32_32x32x16_bf16 v[32:47], v[68:71], v[64:67], v[16:31]\n\t" \
    "s_setprio 1\n\t" \
    "v_exp_f32 v48, v48\n\t" \
    "ds_read_b128 v[72:75], %[kl] offset:6912\n\t" \
    "v_exp_f32 v49, v49\n\t" \
    "v_exp_f32 v50, v50\n\t" \
    "v_exp_f32 v51, v51\n\t" \
    "v_exp_f32 v52, v52\n\t" \
    "v_exp_f32 v53, v53\n\t" \
    "v_exp_f32 v54, v54\n\t" \
    "s_setprio 3\n\t" \
    "v_mfma_f32_32x32x16_bf16 v[0:15], v[80:83], v[96:99], v[0:15]\n\t" \
    "s_setprio 1\n\t" \
    "v_exp_f32 v55, v55\n\t" \
    "ds_read_b128 v[76:79], %[v0l] offset:4608\n\t" \
    "v_cvt_pk_bf16_f32 v92, v48, v49\n\t" \
    "v_cvt_pk_bf16_f32 v93, v50, v51\n\t" \
    "v_cvt_pk_bf16_f32 v94, v52, v53\n\t" \
    "v_cvt_pk_bf16_f32 v95, v54, v55\n\t" \
    "v_exp_f32 v56, v56\n\t" \
    "ds_read_b128 v[80:83], %[v1l] offset:4608\n\t" \
    "v_exp_f32 v57, v57\n\t" \
    "v_exp_f32 v58, v58\n\t" \
    "v_exp_f32 v59, v59\n\t" \
    "s_waitcnt lgkmcnt(4)\n\t" \
    "s_setprio 3\n\t" \
    "v_mfma_f32_32x32x16_bf16 v[0:15], v[84:87], v[92:95], v[0:15]\n\t" \
    "s_setprio 1\n\t" \
    "v_exp_f32 v60, v60\n\t" \
    "v_exp_f32 v61, v61\n\t" \
    "v_exp_f32 v62, v62\n\t" \
    "v_exp_f32 v63, v63\n\t" \
    "s_waitcnt lgkmcnt(2)\n\t" \
    "v_cvt_pk_bf16_f32 v96, v56, v57\n\t" \
    "v_cvt_pk_bf16_f32 v97, v58, v59\n\t" \
    "v_cvt_pk_bf16_f32 v98, v60, v61\n\t" \
    "v_cvt_pk_bf16_f32 v99, v62, v63\n\t" \
    "s_setprio 3\n\t" \
    "v_mfma_f32_32x32x16_bf16 v[48:63], v[72:75], v[64:67], v[16:31]\n\t" \
    "s_setprio 0\n\t" \
    "v_exp_f32 v32, v32\n\t" \
    "ds_read_b128 v[68:71], %[kl] offset:9216\n\t" \
    "v_exp_f32 v33, v33\n\t" \
    "v_exp_f32 v34, v34\n\t" \
    "v_exp_f32 v35, v35\n\t" \
    "v_exp_f32 v36, v36\n\t" \
    "v_exp_f32 v37, v37\n\t" \
    "v_exp_f32 v38, v38\n\t" \
    "s_setprio 3\n\t" \
    "v_mfma_f32_32x32x16_bf16 v[0:15], v[88:91], v[96:99], v[0:15]\n\t" \
    "s_setprio 0\n\t" \
    "v_exp_f32 v39, v39\n\t" \
    "ds_read_b128 v[84:87], %[v0l] offset:6912\n\t" \
    "v_cvt_pk_bf16_f32 v92, v32, v33\n\t" \
    "v_cvt_pk_bf16_f32 v93, v34, v35\n\t" \
    "v_cvt_pk_bf16_f32 v94, v36, v37\n\t" \
    "v_cvt_pk_bf16_f32 v95, v38, v39\n\t" \
    "v_exp_f32 v40, v40\n\t" \
    "ds_read_b128 v[88:91], %[v1l] offset:6912\n\t" \
    "v_exp_f32 v41, v41\n\t" \
    "v_exp_f32 v42, v42\n\t" \
    "v_exp_f32 v43, v43\n\t" \
    "s_waitcnt lgkmcnt(4)\n\t" \
    "s_setprio 3\n\t" \
    "v_mfma_f32_32x32x16_bf16 v[0:15], v[76:79], v[92:95], v[0:15]\n\t" \
    "s_setprio 0\n\t" \
    "v_exp_f32 v44, v44\n\t" \
    "v_exp_f32 v45, v45\n\t" \
    "v_exp_f32 v46, v46\n\t" \
    "v_exp_f32 v47, v47\n\t" \
    "s_waitcnt lgkmcnt(2)\n\t" \
    "v_cvt_pk_bf16_f32 v96, v40, v41\n\t" \
    "v_cvt_pk_bf16_f32 v97, v42, v43\n\t" \
    "v_cvt_pk_bf16_f32 v98, v44, v45\n\t" \
    "v_cvt_pk_bf16_f32 v99, v46, v47\n\t" \
    "s_setprio 3\n\t" \
    "v_mfma_f32_32x32x16_bf16 v[32:47], v[68:71], v[64:67], v[16:31]\n\t" \
    "s_setprio 0\n\t" \
    "v_exp_f32 v48, v48\n\t" \
    "ds_read_b128 v[72:75], %[kl] offset:11520\n\t" \
    "v_exp_f32 v49, v49\n\t" \
    "v_exp_f32 v50, v50\n\t" \
    "v_exp_f32 v51, v51\n\t" \
    "v_exp_f32 v52, v52\n\t" \
    "v_exp_f32 v53, v53\n\t" \
    "v_exp_f32 v54, v54\n\t" \
    "s_setprio 3\n\t" \
    "v_mfma_f32_32x32x16_bf16 v[0:15], v[80:83], v[96:99], v[0:15]\n\t" \
    "s_setprio 0\n\t" \
    "v_exp_f32 v55, v55\n\t" \
    "ds_read_b128 v[76:79], %[v0l] offset:9216\n\t" \
    "v_cvt_pk_bf16_f32 v92, v48, v49\n\t" \
    "v_cvt_pk_bf16_f32 v93, v50, v51\n\t" \
    "v_cvt_pk_bf16_f32 v94, v52, v53\n\t" \
    "v_cvt_pk_bf16_f32 v95, v54, v55\n\t" \
    "v_exp_f32 v56, v56\n\t" \
    "ds_read_b128 v[80:83], %[v1l] offset:9216\n\t" \
    "v_exp_f32 v57, v57\n\t" \
    "v_exp_f32 v58, v58\n\t" \
    "v_exp_f32 v59, v59\n\t" \
    "s_waitcnt lgkmcnt(4)\n\t" \
    "s_setprio 3\n\t" \
    "v_mfma_f32_32x32x16_bf16 v[0:15], v[84:87], v[92:95], v[0:15]\n\t" \
    "s_setprio 0\n\t" \
    "v_exp_f32 v60, v60\n\t" \
    "v_exp_f32 v61, v61\n\t" \
    "v_exp_f32 v62, v62\n\t" \
    "v_exp_f32 v63, v63\n\t" \
    "s_waitcnt lgkmcnt(2)\n\t" \
    "v_cvt_pk_bf16_f32 v96, v56, v57\n\t" \
    "v_cvt_pk_bf16_f32 v97, v58, v59\n\t" \
    "v_cvt_pk_bf16_f32 v98, v60, v61\n\t" \
    "v_cvt_pk_bf16_f32 v99, v62, v63\n\t" \
    "s_setprio 3\n\t" \
    "v_mfma_f32_32x32x16_bf16 v[48:63], v[72:75], v[64:67], v[16:31]\n\t" \
    "s_setprio 2\n\t" \
    "v_exp_f32 v32, v32\n\t" \
    "ds_read_b128 v[68:71], %[kl] offset:13824\n\t" \
    "v_exp_f32 v33, v33\n\t" \
    "v_exp_f32 v34, v34\n\t" \
    "v_exp_f32 v35, v35\n\t" \
    "v_exp_f32 v36, v36\n\t" \
    "v_exp_f32 v37, v37\n\t" \
    "v_exp_f32 v38, v38\n\t" \
    "s_setprio 3\n\t" \
    "v_mfma_f32_32x32x16_bf16 v[0:15], v[88:91], v[96:99], v[0:15]\n\t" \
    "s_setprio 2\n\t" \
    "v_exp_f32 v39, v39\n\t" \
    "ds_read_b128 v[84:87], %[v0l] offset:11520\n\t" \
    "v_cvt_pk_bf16_f32 v92, v32, v33\n\t" \
    "v_cvt_pk_bf16_f32 v93, v34, v35\n\t" \
    "v_cvt_pk_bf16_f32 v94, v36, v37\n\t" \
    "v_cvt_pk_bf16_f32 v95, v38, v39\n\t" \
    "v_exp_f32 v40, v40\n\t" \
    "ds_read_b128 v[88:91], %[v1l] offset:11520\n\t" \
    "v_exp_f32 v41, v41\n\t" \
    "v_exp_f32 v42, v42\n\t" \
    "v_exp_f32 v43, v43\n\t" \
    "s_waitcnt lgkmcnt(4)\n\t" \
    "s_setprio 3\n\t" \
    "v_mfma_f32_32x32x16_bf16 v[0:15], v[76:79], v[92:95], v[0:15]\n\t" \
    "s_setprio 2\n\t" \
    "v_exp_f32 v44, v44\n\t" \
    "v_exp_f32 v45, v45\n\t" \
    "v_exp_f32 v46, v46\n\t" \
    "v_exp_f32 v47, v47\n\t" \
    "s_waitcnt lgkmcnt(2)\n\t" \
    "v_cvt_pk_bf16_f32 v96, v40, v41\n\t" \
    "v_cvt_pk_bf16_f32 v97, v42, v43\n\t" \
    "v_cvt_pk_bf16_f32 v98, v44, v45\n\t" \
    "v_cvt_pk_bf16_f32 v99, v46, v47\n\t" \
    "s_setprio 3\n\t" \
    "v_mfma_f32_32x32x16_bf16 v[32:47], v[68:71], v[64:67], v[16:31]\n\t" \
    "s_setprio 1\n\t" \
    "v_exp_f32 v48, v48\n\t" \
    "ds_read_b128 v[72:75], %[kl] offset:16128\n\t" \
    "s_and_b32 %[t0], %[cnt], 3\n\t" \
    "s_cmp_eq_u32 %[t0], 3\n\t" \
    "s_mov_b32 %[dl], 18432\n\t" \
    "s_cselect_b32 %[dl], -55296, %[dl]\n\t" \
    "v_add_u32 %[kl], %[dl], %[kl]\n\t" \
    "v_exp_f32 v49, v49\n\t" \
    "v_exp_f32 v50, v50\n\t" \
    "v_exp_f32 v51, v51\n\t" \
    "v_exp_f32 v52, v52\n\t" \
    "v_exp_f32 v53, v53\n\t" \
    "v_exp_f32 v54, v54\n\t" \
    "s_setprio 3\n\t" \
    "v_mfma_f32_32x32x16_bf16 v[0:15], v[80:83], v[96:99], v[0:15]\n\t" \
    "s_setprio 1\n\t" \
    "v_exp_f32 v55, v55\n\t" \
    "ds_read_b128 v[76:79], %[v0l] offset:13824\n\t" \
    "v_cvt_pk_bf16_f32 v92, v48, v49\n\t" \
    "v_cvt_pk_bf16_f32 v93, v50, v51\n\t" \
    "v_cvt_pk_bf16_f32 v94, v52, v53\n\t" \
    "v_cvt_pk_bf16_f32 v95, v54, v55\n\t" \
    "v_exp_f32 v56, v56\n\t" \
    "ds_read_b128 v[80:83], %[v1l] offset:13824\n\t" \
    "v_exp_f32 v57, v57\n\t" \
    "v_exp_f32 v58, v58\n\t" \
    "v_exp_f32 v59, v59\n\t" \
    "s_waitcnt lgkmcnt(4)\n\t" \
    "s_setprio 3\n\t" \
    "v_mfma_f32_32x32x16_bf16 v[0:15], v[84:87], v[92:95], v[0:15]\n\t" \
    "s_setprio 1\n\t" \
    "v_exp_f32 v60, v60\n\t" \
    "v_exp_f32 v61, v61\n\t" \
    "v_exp_f32 v62, v62\n\t" \
    "v_exp_f32 v63, v63\n\t" \
    "s_waitcnt lgkmcnt(2)\n\t" \
    "v_cvt_pk_bf16_f32 v96, v56, v57\n\t" \
    "v_cvt_pk_bf16_f32 v97, v58, v59\n\t" \
    "v_cvt_pk_bf16_f32 v98, v60, v61\n\t" \
    "v_cvt_pk_bf16_f32 v99, v62, v63\n\t" \
    "s_cmp_eq_u32 %[cnt], 7\n\t" \
    "s_cbranch_scc1 Lskip%=\n\t" \
    "s_cmp_eq_u32 %[cnt], 6\n\t" \
    "s_cbranch_scc1 Lw0%=\n\t" \
    "s_waitcnt vmcnt(2)\n\t" \
    "s_barrier\n\t" \
    "s_cmp_gt_u32 %[cnt], 4\n\t" \
    "s_cbranch_scc1 Lskip%=\n\t" \
    "s_add_u32 %[t0], %[cnt], 3\n\t" \
    "s_and_b32 %[t0], %[t0], 3\n\t" \
    "s_mul_i32 %[t0], %[t0], 18432\n\t" \
    "s_add_u32 m0, %[t0], %[dst]\n\t" \
    "s_setprio 3\n\t" \
    "global_load_lds_dwordx4 %[goff], %[base]\n\t" \
    "global_load_lds_dwordx4 %[goff], %[base] offset:1024\n\t" \
    "s_setprio 1\n\t" \
    "v_add_u32 %[goff], 0x4000, %[goff]\n\t" \
    "s_branch Lskip%=\n\t" \
    "Lw0%=:\n\t" \
    "s_waitcnt vmcnt(0)\n\t" \
    "s_barrier\n\t" \
    "Lskip%=:\n\t" \
    "s_setprio 3\n\t" \
    "v_mfma_f32_32x32x16_bf16 v[48:63], v[72:75], v[64:67], v[16:31]\n\t" \
    "s_setprio 0\n\t" \
    "v_exp_f32 v32, v32\n\t" \
    "ds_read_b128 v[68:71], %[kl] offset:0\n\t" \
    "v_exp_f32 v33, v33\n\t" \
    "v_exp_f32 v34, v34\n\t" \
    "v_exp_f32 v35, v35\n\t" \
    "v_exp_f32 v36, v36\n\t" \
    "v_exp_f32 v37, v37\n\t" \
    "v_exp_f32 v38, v38\n\t" \
    "s_setprio 3\n\t" \
    "v_mfma_f32_32x32x16_bf16 v[0:15], v[88:91], v[96:99], v[0:15]\n\t" \
    "s_setprio 0\n\t" \
    "v_exp_f32 v39, v39\n\t" \
    "ds_read_b128 v[84:87], %[v0l] offset:16128\n\t" \
    "v_cvt_pk_bf16_f32 v92, v32, v33\n\t" \
    "v_cvt_pk_bf16_f32 v93, v34, v35\n\t" \
    "v_cvt_pk_bf16_f32 v94, v36, v37\n\t" \
    "v_cvt_pk_bf16_f32 v95, v38, v39\n\t" \
    "v_exp_f32 v40, v40\n\t" \
    "ds_read_b128 v[88:91], %[v1l] offset:16128\n\t" \
    "v_add_u32 %[v0l], %[dl], %[v0l]\n\t" \
    "v_add_u32 %[v1l], %[dl], %[v1l]\n\t" \
    "v_exp_f32 v41, v41\n\t" \
    "v_exp_f32 v42, v42\n\t" \
    "v_exp_f32 v43, v43\n\t" \
    "s_waitcnt lgkmcnt(4)\n\t" \
    "s_setprio 3\n\t" \
    "v_mfma_f32_32x32x16_bf16 v[0:15], v[76:79], v[92:95], v[0:15]\n\t" \
    "s_setprio 0\n\t" \
    "v_exp_f32 v44, v44\n\t" \
    "v_exp_f32 v45, v45\n\t" \
    "v_exp_f32 v46, v46\n\t" \
    "v_exp_f32 v47, v47\n\t" \
    "s_waitcnt lgkmcnt(2)\n\t" \
    "v_cvt_pk_bf16_f32 v96, v40, v41\n\t" \
    "v_cvt_pk_bf16_f32 v97, v42, v43\n\t" \
    "v_cvt_pk_bf16_f32 v98, v44, v45\n\t" \
    "v_cvt_pk_bf16_f32 v99, v46, v47\n\t" \
    "s_setprio 3\n\t" \
    "v_mfma_f32_32x32x16_bf16 v[32:47], v[68:71], v[64:67], v[16:31]\n\t" \
    "s_setprio 0\n\t" \
    "v_exp_f32 v48, v48\n\t" \
    "ds_read_b128 v[72:75], %[kl] offset:2304\n\t" \
    "v_exp_f32 v49, v49\n\t" \
    "v_exp_f32 v50, v50\n\t" \
    "v_exp_f32 v51, v51\n\t" \
    "v_exp_f32 v52, v52\n\t" \
    "v_exp_f32 v53, v53\n\t" \
    "v_exp_f32 v54, v54\n\t" \
    "s_setprio 3\n\t" \
    "v_mfma_f32_32x32x16_bf16 v[0:15], v[80:83], v[96:99], v[0:15]\n\t" \
    "s_setprio 0\n\t" \
    "v_exp_f32 v55, v55\n\t" \
    "ds_read_b128 v[76:79], %[v0l] offset:0\n\t" \
    "v_cvt_pk_bf16_f32 v92, v48, v49\n\t" \
    "v_cvt_pk_bf16_f32 v93, v50, v51\n\t" \
    "v_cvt_pk_bf16_f32 v94, v52, v53\n\t" \
    "v_cvt_pk_bf16_f32 v95, v54, v55\n\t" \
    "v_exp_f32 v56, v56\n\t" \
    "ds_read_b128 v[80:83], %[v1l] offset:0\n\t" \
    "v_exp_f32 v57, v57\n\t" \
    "v_exp_f32 v58, v58\n\t" \
    "v_exp_f32 v59, v59\n\t" \
    "s_waitcnt lgkmcnt(4)\n\t" \
    "s_setprio 3\n\t" \
    "v_mfma_f32_32x32x16_bf16 v[0:15], v[84:87], v[92:95], v[0:15]\n\t" \
    "s_setprio 0\n\t" \
    "v_exp_f32 v60, v60\n\t" \
    "v_exp_f32 v61, v61\n\t" \
    "v_exp_f32 v62, v62\n\t" \
    "v_exp_f32 v63, v63\n\t" \
    "s_waitcnt lgkmcnt(2)\n\t" \
    "v_cvt_pk_bf16_f32 v96, v56, v57\n\t" \
    "v_cvt_pk_bf16_f32 v97, v58, v59\n\t" \
    "v_cvt_pk_bf16_f32 v98, v60, v61\n\t" \
    "v_cvt_pk_bf16_f32 v99, v62, v63\n\t" \
    "s_add_u32 %[cnt], %[cnt], 1\n\t" \
    "s_cmp_lt_u32 %[cnt], 8\n\t" \
    "s_cbranch_scc1 Lloop%=\n\t" \
    "s_waitcnt lgkmcnt(0)\n\t" \
    "s_setprio 3\n\t" \
    "v_mfma_f32_32x32x16_bf16 v[0:15], v[88:91], v[96:99], v[0:15]\n\t" \
    "s_setprio 0\n\t" \
    "s_mov_b32 m0, %[sv]\n\t" \
    "s_setprio 0\n\t" \
    "s_nop 7\n\t" \
    "s_nop 7\n\t"
#define ATTN_CLOBBERS "v32", "v33", "v34", "v35", "v36", "v37", "v38", "v39", "v40", "v41", "v42", "v43", "v44", "v45", "v46", "v47", "v48", "v49", "v50", "v51", "v52", "v53", "v54", "v55", "v56", "v57", "v58", "v59", "v60", "v61", "v62", "v63", "v68", "v69", "v70", "v71", "v72", "v73", "v74", "v75", "v76", "v77", "v78", "v79", "v80", "v81", "v82", "v83", "v84", "v85", "v86", "v87", "v88", "v89", "v90", "v91", "v92", "v93", "v94", "v95", "v96", "v97", "v98", "v99"
#define ATTN_INSTR 0

DI void attn_block(char* __restrict__ ws, char* lds, int head, int chunk, int tid) {
    const int l = tid & 63, w = __builtin_amdgcn_readfirstlane(tid >> 6), h = l >> 5;
    const int qtile = chunk * 8 + w;
    const unsigned lds0 = (unsigned)(uintptr_t)lds;
    const char* kvbase = ws + WS_KV + (size_t)head * 64 * 2048;
    const unsigned dst = lds0 + w * AT_TS;
    __builtin_amdgcn_s_setprio(3);
    bf16x8 qf; float km, gate;
    asm volatile("global_load_dwordx4 %0, %3, off\n\tglobal_load_dword %1, %4, off\n\tglobal_load_dword %2, %5, off"
                 : "=&v"(qf), "=&v"(km), "=&v"(gate)
                 : "v"(ws + WS_QF + ((size_t)(head * 64 + qtile) * 1024) + l * 16), "v"((const float*)(ws + WS_KMAX) + l * 32 + head),
                   "v"((const float*)(ws + WS_GATE) + qtile * 32 + (l & 31)) : "memory");
    auto issue3 = [&]() {
#pragma unroll
        for (int c = 0; c < 3; ++c) { glds16(kvbase + c * 16384 + w * 2048 + l * 16, dst + c * AT_SLOT); glds16(kvbase + c * 16384 + w * 2048 + 1024 + l * 16, dst + c * AT_SLOT + 1024); }
    };
    issue3();
    {
        const u32x4 ones = {0x3f803f80u, 0x3f803f80u, 0x3f803f80u, 0x3f803f80u};
        *(LDS_AS u32x4*)((lds_ptr)lds + (tid >> 4) * AT_TS + 2048 + (tid & 15) * 16) = ones;
        if (tid == 0) *(LDS_AS unsigned*)((lds_ptr)lds + 4 * AT_SLOT) = 0u;
    }
    asm volatile("s_waitcnt vmcnt(6)" : "+v"(qf), "+v"(km), "+v"(gate));
    float qn = 0.f;
    { const u32x4 qu = __builtin_bit_cast(u32x4, qf);
#pragma unroll
      for (int e = 0; e < 4; ++e) { qn = __builtin_fmaf(bf_lo(qu[e]), bf_lo(qu[e]), qn); qn = __builtin_fmaf(bf_hi(qu[e]), bf_hi(qu[e]), qn); } }
    qn = swap32_sum(qn);
    float ref = __builtin_amdgcn_sqrtf(qn * wave_max_fast(km));
    unsigned kl = lds0 + l * 16;
    const bool onesl = (l & 16) != 0;
    const unsigned ol = lds0 + 2048 + ((((l & 15) >= 4) && ((l & 15) < 12)) ? 64 : 0);
    unsigned v0l = onesl ? ol : lds0 + 1024 + h * 256 + (l & 15) * 16, v1l = onesl ? ol : v0l + 512;
    LDS_AS volatile unsigned* flag = (LDS_AS volatile unsigned*)((lds_ptr)lds + 4 * AT_SLOT);
    const size_t ooff = WS_HO + ((size_t)(qtile * 88 + head) * 1024) + l * 16;
#pragma unroll 1
    for (int pass = 0;; ++pass) {
        f32x16 negm, o;
#pragma unroll
        for (int r = 0; r < 16; ++r) negm[r] = -ref;
        unsigned sv, cnt, t0, dl, goff = 3 * 16384 + w * 2048 + l * 16;
        STAMP(2, 1);
        asm volatile(ATTN_ASM
                     : "=&{v[0:15]}"(o), [sv] "=&s"(sv), [cnt] "=&s"(cnt), [t0] "=&s"(t0), [dl] "=&s"(dl), [goff] "+v"(goff), [kl] "+v"(kl), [v0l] "+v"(v0l), [v1l] "+v"(v1l)
                     : "{v[16:31]}"(negm), "{v[64:67]}"(qf), [dst] "s"(dst), [base] "s"(kvbase)
                     : ATTN_CLOBBERS, "scc", "memory");
        STAMP(2, 3);
        st16_wt(ws, ooff, pack8(o, 0, gate * __builtin_amdgcn_rcpf(o[8])));
        drain_stores();
        if (pass == 1) { __syncthreads(); break; }
        const bool bad = !(o[8] >= 9.094947e-13f);
        if (__any(bad) && l == 0) *flag = 1u;
        __syncthreads();
        if (*flag == 0u) break;
        float m = -3.0e38f;
        for (int t = 0; t < 64; ++t) {
            const bf16x8 kf = *(const bf16x8*)(kvbase + (size_t)t * 2048 + l * 16);
            const f32x16 s = MFMA(kf, qf, f32x16{});
#pragma unroll
            for (int r = 0; r < 16; ++r) m = fmaxf(m, s[r]);
        }
        ref = swap32_max(m);
        asm volatile("s_waitcnt vmcnt(0)" ::: "memory");
        issue3();
    }
    if (tid == 0) __hip_atomic_fetch_add(sync_word(ws, SY_O + chunk), 1u, RLX_AGENT);
}
constexpr int SP_ST = 24576, SP_LDS = 3 * SP_ST + 256, FN_ST = 16384;

template <int N> DI void wait_vm() { asm volatile("s_waitcnt vmcnt(%0)" :: "i"(N) : "memory"); }

DI void gate_block(const u16* __restrict__ x, const u16* __restrict__ w_in, const u16* __restrict__ w_sp, const u16* __restrict__ b_sp,
                   const u16* __restrict__ wo, const u16* __restrict__ w_out, char* __restrict__ ws, u16* __restrict__ out, char* lds, int g, int tid) {
    const int l = tid & 63, w = __builtin_amdgcn_readfirstlane(tid >> 6), h = l >> 5, r32 = l & 31;
    const int tl = (g >> 3) & 7, tt = (g & 3) * 8 + tl, ft = ((g >> 2) & 1) * 4 + (g >> 6);
    const int rot = SP_ROT(tl, g >> 6);
    const unsigned lds0 = (unsigned)(uintptr_t)lds;
    const int q4 = (l & 15) >> 2, p4 = l & 3, b16 = (l >> 4) & 1;
    const unsigned lo = l * 16;
    if (tid == 0) TL(5, 0, 99);
    if (GPRIO) __builtin_amdgcn_s_setprio(GPRIO);
    const int j = w - 4, lrow = 8 * j + (l >> 3);
    const unsigned xv = (unsigned)(lrow * D_ + ((l & 7) ^ ((lrow >> 1) & 7)) * 8) * 2;
    const int wr = 4 * j + (l >> 4), wc = ((((l & 15) >> 2) ^ (wr & 3)) << 2) | (l & 3);
    const unsigned wv = (unsigned)(wr * 1792 + wc * 8) * 2;
    auto issue_z = [&](int s) {
        const char* xu = (const char*)(x + (size_t)tt * 64 * D_); const char* wu = (const char*)(w_in + ft * 128);
        const unsigned dst = lds0 + (s % 3) * SP_ST + j * 1024; const int sr = (s + rot) & 7;
        glds16s(xu + sr * 128, xv, dst); glds16s(xu + 32 * D_ * 2 + sr * 128, xv, dst + 4096);
#pragma unroll
        for (int i = 0; i < 4; ++i) glds16s(wu + (size_t)(sr * 64 + 16 * i) * 3584, wv, dst + 8192 + i * 4096);
    };
    STAMP(5, 1);
    if (w >= 4 && ft < 7) { issue_z(0); issue_z(1); }
    if (w < 7) {
        const int ttile = g >> 2, fb = (g & 3) * 7 + w;
        const char* z2 = ws + WS_Z2 + (size_t)(ttile * 56 + 2 * fb) * 1024 + lo;
        const u32x4 z0 = *(const u32x4*)z2, z1 = *(const u32x4*)(z2 + 1024);
        f32x4 pv[7];
        const f32x4* sp = (const f32x4*)(ws + WS_STATS) + (size_t)(ttile * 32 + r32) * 14 + h * 7;
#pragma unroll
        for (int i = 0; i < 7; ++i) pv[i] = sp[i];
        ln_block(ws, lds + 2 * SP_ST + w * 1024, ttile, fb, z0, z1, ln_stats(pv), l);
    }
    if (ft >= 7) { drain_stores(); bar_only(); if (tid == 0) __hip_atomic_fetch_add(sync_word(ws, SY_LN + (g & 15)), 1u, RLX_AGENT); }
    if (ft < 7) {
        if (w >= 4) {
            if (LPRIO != GPRIO) __builtin_amdgcn_s_setprio(LPRIO);
            if (w < 7) wait_bar<2>(); else wait_bar<0>();
            issue_z(2);
#pragma unroll 1
            for (int s = 0; s < 7; ++s) {
                if (s == 0) bar_only(); else if (s + 2 < 8) wait_bar<6>(); else wait_bar<0>();
                if (s + 3 < 8) issue_z(s + 3);
            }
            bar_only();
            if (j == 3) {
                wait_words(ws, SY_LN, 16, 16u, l);
                const char* zu = ws + WS_Z2N + (size_t)(ft * 4) * 128 * 1024;
                const unsigned pv = (unsigned)((tl >> 1) * 128 * 1024 + ((tl & 1) * 16 + (l & 15)) * 128);
                auto pf = [&](int s4) { const int sr = (4 * s4 + (l >> 4) + rot) & 31; prefetch_line(zu, pv + sr * 4096, lds0 + 3 * SP_ST); };
                pf(0); pf(1); pf(2);
                bar_only();
                bar_only();
#pragma unroll 1
                for (int s = 0; s < 31; ++s) { bar_only(); if ((s & 3) == 0 && s + 12 < 32) pf((s >> 2) + 3); }
                drain_stores();
            } else {
                const unsigned a0 = (unsigned)((l >> 3) * S_ * 2 + (((l & 7) ^ ((l >> 4) & 7)) << 4)), a1 = a0 ^ 64u;
                const unsigned va = (j & 1) ? a1 : a0, vb = (j & 1) ? a0 : a1;
                const char* au = (const char*)(w_sp + (size_t)(tt * 64 + 8 * j) * S_);
                auto issue = [&](int s) {
                    const unsigned dst = lds0 + (s % 6) * 8192 + j * 1024; const int sr = (s + rot) & 31;
                    glds16s(au + sr * 128, va, dst); glds16s(au + 24 * S_ * 2 + sr * 128, vb, dst + 3072);
                    if (j < 2) glds16s(au + 48 * S_ * 2 + sr * 128, va, dst + 6144);
                };
                auto waitst = [&](int younger) {
                    if (j < 2) { if (younger >= 4) wait_vm<12>(); else if (younger == 3) wait_vm<9>(); else if (younger == 2) wait_vm<6>(); else if (younger == 1) wait_vm<3>(); else wait_vm<0>(); }
                    else { if (younger >= 4) wait_vm<8>(); else if (younger == 3) wait_vm<6>(); else if (younger == 2) wait_vm<4>(); else if (younger == 1) wait_vm<2>(); else wait_vm<0>(); }
                };
#pragma unroll 1
                for (int s = 0; s < 5; ++s) issue(s);
                bar_only();
                waitst(4); bar_only();
                issue(5);
#pragma unroll 1
                for (int s = 0; s < 31; ++s) {
                    waitst(min(s + 5, 31) - (s + 1)); bar_only();
                    if (s + 6 < 32) issue(s + 6);
                }
            }
            bar_only();
        } else {
            {
                const int idx = (w & 1) * 64 + l, fl = g >> 6;
                if (w < 2) prefetch_line(x + (size_t)tt * 64 * D_, (unsigned)((16 * fl + (idx >> 3)) * D_ * 2 + (idx & 7) * 128), lds0 + 3 * SP_ST);
                else prefetch_line(w_in + ft * 128, (unsigned)((64 * tl + (idx >> 1)) * 3584 + (idx & 1) * 128), lds0 + 3 * SP_ST);
            }
            const int fb = w;
            int boff[4];
#pragma unroll
            for (int ks = 0; ks < 4; ++ks) { const int f = (r32 >> 1) & 7; boff[ks] = r32 * 128 + (((ks * 2 + h) ^ f) << 4); }
            const int fbg = ft * 4 + fb, fg = 2 * fbg;
            const float gt0 = ((const float*)(ws + WS_GATE))[(tt * 2) * 32 + r32], gt1 = ((const float*)(ws + WS_GATE))[(tt * 2 + 1) * 32 + r32];
            const float bs0 = __uint_as_float((unsigned)b_sp[(tt * 2) * 32 + r32] << 16), bs1 = __uint_as_float((unsigned)b_sp[(tt * 2 + 1) * 32 + r32] << 16);
            LDS_AS u32x4* z1p = (LDS_AS u32x4*)((lds_ptr)lds + 49152 + w * 4096) + l;
            {
                f32x16 acc[2]; acc[0] = f32x16{}; acc[1] = f32x16{};
                const int zoff = 8192 + (8 * h + q4) * 256 + ((fb ^ q4) << 6) + b16 * 32 + p4 * 8;
                bf16x8 aa[2], ba[2][2], ab[2], bb[2][2];
                auto rdh = [&](int s, int half, bf16x8 (&af)[2], bf16x8 (&bf)[2][2]) {
                    lds_cptr base = (lds_cptr)lds + (s % 3) * SP_ST;
#pragma unroll
                    for (int kk = 0; kk < 2; ++kk) {
                        const int ks = 2 * half + kk;
                        af[kk] = trfrag(base + zoff + ks * 4096, base + zoff + ks * 4096 + 1024);
                        bf[0][kk] = ldsfrag(base + boff[ks]); bf[1][kk] = ldsfrag(base + boff[ks] + 4096);
                    }
                };
                auto mm = [&](bf16x8 (&af)[2], bf16x8 (&bf)[2][2]) {
#pragma unroll
                    for (int kk = 0; kk < 2; ++kk) { acc[0] = MFMA(af[kk], bf[0][kk], acc[0]); acc[1] = MFMA(af[kk], bf[1][kk], acc[1]); }
                };
                bar_only();
                rdh(0, 0, aa, ba);
#pragma unroll 1
                for (int s = 0; s < 7; ++s) {
                    rdh(s, 1, ab, bb); mm(aa, ba); interleave<4, 2>(); SB();
                    if (s == 1) drain_stores();
                    bar_only();
                    if (s == 1 && tid == 0) __hip_atomic_fetch_add(sync_word(ws, SY_LN + (g & 15)), 1u, RLX_AGENT);
                    rdh(s + 1, 0, aa, ba); mm(ab, bb); interleave<4, 2>(); SB();
                }
                rdh(7, 1, ab, bb); mm(aa, ba); interleave<4, 2>(); SB();
                mm(ab, bb);
                bar_only();
                if (tid == 0) TL(5, 0, 101);
#pragma unroll
                for (int tb = 0; tb < 2; ++tb) {
                    const float og = 1.0f - (tb ? gt1 : gt0);
#pragma unroll
                    for (int gq = 0; gq < 2; ++gq) {
                        u32x4 zv;
#pragma unroll
                        for (int e = 0; e < 4; ++e) zv[e] = cvtpk(gelu_tanh(acc[tb][8 * gq + 2 * e]) * og, gelu_tanh(acc[tb][8 * gq + 2 * e + 1]) * og);
                        z1p[(tb * 2 + gq) * 64] = zv;
                    }
                }
            }
            if (w < 3) {
                const int row = min(176 * tl + w * 64 + l, 176 * tl + 175);
                prefetch_ptr(row < 896 ? (const char*)(w_out + (size_t)row * D_ + ft * 64) : (const char*)(wo + (size_t)(row - 896) * D_ + ft * 64), lds0 + 3 * SP_ST);
            }
            f32x16 acc[2]; acc[0] = f32x16{}; acc[1] = f32x16{};
            {
                if (tid == 0) TL(5, 0, 102);
                bar_only();
                if (tid == 0) TL(5, 0, 103);
                bf16x8 za[4], zb[4];
                const size_t zoff = WS_Z2N + (size_t)fbg * 128 * 1024 + lo;
                auto ldz = [&](int s, int ks) { const int sr = (min(s, 31) + rot) & 31; return ld16_dev(ws, zoff + (size_t)((sr * 4 + ks) * 1024)); };
#pragma unroll
                for (int ks = 0; ks < 4; ++ks) za[ks] = ldz(0, ks);
#pragma unroll
                for (int ks = 0; ks < 4; ++ks) zb[ks] = ldz(1, ks);
                bf16x8 bx[2][2], by[2][2];
                auto rdh = [&](int s, int half, bf16x8 (&bf)[2][2]) {
                    lds_cptr base = (lds_cptr)lds + (s % 6) * 8192;
#pragma unroll
                    for (int kk = 0; kk < 2; ++kk) { bf[0][kk] = ldsfrag(base + boff[2 * half + kk]); bf[1][kk] = ldsfrag(base + boff[2 * half + kk] + 4096); }
                };
                auto stage = [&](int s, bf16x8 (&z)[4], bool last) {
                    rdh(s, 1, by);
                    acc[0] = MFMA(z[0], bx[0][0], acc[0]); acc[1] = MFMA(z[0], bx[1][0], acc[1]);
                    acc[0] = MFMA(z[1], bx[0][1], acc[0]); acc[1] = MFMA(z[1], bx[1][1], acc[1]); interleave<4, 1>(); SB();
                    z[0] = ldz(s + 2, 0); z[1] = ldz(s + 2, 1); SB();
                    if (!last) { bar_only(); rdh(s + 1, 0, bx); }
                    acc[0] = MFMA(z[2], by[0][0], acc[0]); acc[1] = MFMA(z[2], by[1][0], acc[1]);
                    acc[0] = MFMA(z[3], by[0][1], acc[0]); acc[1] = MFMA(z[3], by[1][1], acc[1]); interleave<4, 1>(); SB();
                    z[2] = ldz(s + 2, 2); z[3] = ldz(s + 2, 3); SB();
                };
                bar_only();
                rdh(0, 0, bx);
#pragma unroll 1
                for (int s = 0; s < 30; s += 2) { stage(s, za, false); stage(s + 1, zb, false); }
                stage(30, za, false); stage(31, zb, true);
                if (tid == 0) TL(5, 0, 104);
            }
#pragma unroll
            for (int tb = 0; tb < 2; ++tb) {
                const float bs = tb ? bs1 : bs0;
#pragma unroll
                for (int gq = 0; gq < 2; ++gq) {
                    u32x4 ov; const u32x4 zv = z1p[(tb * 2 + gq) * 64];
#pragma unroll
                    for (int e = 0; e < 4; ++e) ov[e] = cvtpk((acc[tb][8 * gq + 2 * e] + bs) * bf_lo(zv[e]), (acc[tb][8 * gq + 2 * e + 1] + bs) * bf_hi(zv[e]));
                    st16_wt(ws, WS_HO + ((size_t)((tt * 2 + tb) * 88 + 32 + fg + gq) * 1024) + lo, ov);
                }
            }
            drain_stores();
            bar_only();
            if (tid == 0) __hip_atomic_fetch_add(sync_word(ws, SY_H + tt), 1u, RLX_AGENT);
        }
    }
    STAMP(5, 3);
    if (tid == 0) TL(5, 0, 105);
    {
        if (GPRIO) __builtin_amdgcn_s_setprio(GPRIO);
        const int tb = w & 1, kq = w >> 1;
        const int prow = 8 * w + (l >> 3);
        const unsigned pv = (unsigned)(prow * D_ + ((l & 7) ^ (((prow >> 1) & 1) << 2)) * 8) * 2;
        const char* w0 = (const char*)(w_out + ft * 64);
        const char* w1 = (const char*)(wo + ft * 64);
        auto issue = [&](int s) {
            const unsigned dst = lds0 + (s & 3) * FN_ST + w * 1024;
            const char* wp = s < 7 ? w0 + (size_t)s * 128 * D_ * 2 : w1 + (size_t)(s - 7) * 128 * D_ * 2;
            glds16s(wp, pv, dst); glds16s(wp + (size_t)64 * D_ * 2, pv, dst + 8192);
        };
        issue(0); issue(1); issue(2); issue(3);
        const int woff0 = (kq * 32 + 4 * h + q4) * 128 + (((b16 * 2 + (p4 >> 1)) ^ ((q4 >> 1) << 2)) << 4) + (p4 & 1) * 8, woff1 = woff0 ^ 64;
        const size_t hoff = WS_HO + ((size_t)((tt * 2 + tb) * 88 + 2 * kq) * 1024) + lo;
        f32x16 acc[2]; acc[0] = f32x16{}; acc[1] = f32x16{};
        bf16x8 hf[14];
        auto comp = [&](int s, const bf16x8& h0, const bf16x8& h1) {
            lds_cptr base = (lds_cptr)lds + (s & 3) * FN_ST;
            const bf16x8 w00 = trfrag(base + woff0, base + woff0 + 1024), w01 = trfrag(base + woff1, base + woff1 + 1024);
            const bf16x8 w10 = trfrag(base + woff0 + 2048, base + woff0 + 3072), w11 = trfrag(base + woff1 + 2048, base + woff1 + 3072);
            acc[0] = MFMA(w00, h0, acc[0]); acc[1] = MFMA(w01, h0, acc[1]);
            acc[0] = MFMA(w10, h1, acc[0]); acc[1] = MFMA(w11, h1, acc[1]);
            SB();
        };
        if (w == 7) wait_words(ws, SY_H + tt, 1, 7u, l);
        if (w == 0) TL(5, 1, 99);
        bar_only();
        if (w == 0) TL(5, 1, 100);
#pragma unroll
        for (int q = 0; q < 14; ++q) hf[q] = ld16_dev(ws, hoff + (size_t)((32 + 8 * (q >> 1) + (q & 1)) * 1024));
        wait_vm<20>(); bar_only();            comp(0, hf[0], hf[1]);
        wait_vm<18>(); bar_only(); issue(4);  comp(1, hf[2], hf[3]);
        wait_vm<18>(); bar_only(); issue(5);  comp(2, hf[4], hf[5]);
        wait_vm<18>(); bar_only(); issue(6);  comp(3, hf[6], hf[7]);
        wait_vm<4>();  bar_only(); issue(7);  comp(4, hf[8], hf[9]);
        wait_vm<4>();  bar_only(); issue(8);  comp(5, hf[10], hf[11]);
        wait_vm<4>();  bar_only(); issue(9);  comp(6, hf[12], hf[13]);
        if (w == 0) TL(5, 1, 101);
        if (w == 7) wait_words(ws, SY_O + (tt >> 2), 1, 32u, l);
        bar_only();
        if (w == 0) TL(5, 1, 102);
#pragma unroll
        for (int q = 0; q < 8; ++q) hf[q] = ld16_dev(ws, hoff + (size_t)((8 * (q >> 1) + (q & 1)) * 1024));
        wait_vm<12>(); bar_only(); issue(10); comp(7, hf[0], hf[1]);
        wait_vm<12>(); bar_only();            comp(8, hf[2], hf[3]);
        wait_vm<10>(); bar_only();            comp(9, hf[4], hf[5]);
        wait_vm<0>();  bar_only();            comp(10, hf[6], hf[7]);
        if (w == 0) TL(5, 1, 103);
        bar_only();
        LDS_AS f32x4* red = (LDS_AS f32x4*)lds;
#pragma unroll
        for (int db = 0; db < 2; ++db)
#pragma unroll
            for (int q = 0; q < 4; ++q) red[((w * 2 + db) * 4 + q) * 64 + l] = f32x4{acc[db][4 * q], acc[db][4 * q + 1], acc[db][4 * q + 2], acc[db][4 * q + 3]};
        bar_only();
        const int db = kq & 1, gq = kq >> 1;
        f32x4 v0 = {0.f, 0.f, 0.f, 0.f}, v1 = v0;
#pragma unroll
        for (int q = 0; q < 4; ++q) { const int ww = q * 2 + tb; v0 += red[((ww * 2 + db) * 4 + 2 * gq) * 64 + l]; v1 += red[((ww * 2 + db) * 4 + 2 * gq + 1) * 64 + l]; }
        const unsigned a0 = cvtpk(v0[0], v0[1]), a1 = cvtpk(v0[2], v0[3]), b0 = cvtpk(v1[0], v1[1]), b1 = cvtpk(v1[2], v1[3]);
        auto r0 = __builtin_amdgcn_permlane32_swap(a0, b0, false, false);
        auto r1 = __builtin_amdgcn_permlane32_swap(a1, b1, false, false);
        const u32x4 ov = {r0[0], r1[0], r0[1], r1[1]};
        *(u32x4*)(out + (size_t)(tt * 64 + tb * 32 + r32) * D_ + ft * 64 + db * 32 + gq * 16 + h * 8) = ov;
        if (w == 0) TL(5, 1, 104);
    }
}

extern "C" __global__ __launch_bounds__(512, 4) void k_main(const u16* __restrict__ x, const u16* __restrict__ w_in, const u16* __restrict__ w_sp, const u16* __restrict__ b_sp,
                                                 const u16* __restrict__ wo, const u16* __restrict__ w_out, char* __restrict__ ws, u16* __restrict__ out) {
    __shared__ __attribute__((aligned(1024))) char lds[AT_LDS > SP_LDS ? AT_LDS : SP_LDS];
    const int b = blockIdx.x;
    if (b < 256) {
        STAMP(2, 0);
        attn_block(ws, lds, (b & 7) * 4 + ((b >> 3) & 3), b >> 5, threadIdx.x);
        STAMP(2, 2);
    } else {
        STAMP(5, 0);
        gate_block(x, w_in, w_sp, b_sp, wo, w_out, ws, out, lds, b - 256, threadIdx.x);
        STAMP(5, 2);
    }
}

extern "C" void kernel_launch(void* const* d_in, const int* in_sizes, int n_in, void* d_out, int out_size, void* d_ws, size_t ws_size, hipStream_t stream) {
    const u16* x = (const u16*)d_in[0];
    const u16* wqkv = (const u16*)d_in[1];
    const u16* wo = (const u16*)d_in[2];
    const u16* w_in = (const u16*)d_in[4];
    const u16* w_sp = (const u16*)d_in[5];
    const u16* b_sp = (const u16*)d_in[6];
    const u16* w_out = (const u16*)d_in[7];
    const u16* gate_w = (const u16*)d_in[8];
    char* ws = (char*)d_ws;
    k1_proj<<<256, 512, 0, stream>>>(x, wqkv, w_in, gate_w, w_sp, wo, w_out, ws);
    k_main<<<512, 512, 0, stream>>>(x, w_in, w_sp, b_sp, wo, w_out, ws, (u16*)d_out);
}
```

```cpp
#include <hip/hip_runtime.h>
#include <hip/hip_bf16.h>
#include <stdint.h>

typedef __attribute__((ext_vector_type(8))) short bf16x8;
typedef __attribute__((ext_vector_type(4))) short s16x4;
typedef __attribute__((ext_vector_type(16))) float f32x16;
typedef __attribute__((ext_vector_type(4))) unsigned u32x4;
typedef __attribute__((ext_vector_type(2))) float f32x2;
typedef __attribute__((ext_vector_type(4))) float f32x4;
typedef __attribute__((ext_vector_type(2))) __bf16 bf16x2_t;
#define LDS_AS __attribute__((address_space(3)))
typedef LDS_AS const char* lds_cptr;
typedef LDS_AS char* lds_ptr;
typedef short v4i16_t __attribute__((ext_vector_type(4)));
typedef unsigned short u16;
#define DI __device__ __forceinline__

#define MFMA(a, b, c) __builtin_amdgcn_mfma_f32_32x32x16_bf16((a), (b), (c), 0, 0, 0)
#define SB() __builtin_amdgcn_sched_barrier(0)
template <int NM, int ND> DI void interleave() {
#pragma unroll
    for (int i = 0; i < NM; ++i) { __builtin_amdgcn_sched_group_barrier(0x008, 1, 0); __builtin_amdgcn_sched_group_barrier(0x100, ND, 0); }
}

constexpr int S_ = 2048, D_ = 512, FH = 896;
constexpr float QSCALE = 0.25f * 1.4426950408889634f;

constexpr size_t WS_QF    = 0;
constexpr size_t WS_KV    = WS_QF + 32u * 64 * 1024;
constexpr size_t WS_Z2    = WS_KV + 32u * 64 * 2048;
constexpr size_t WS_Z2N   = WS_Z2 + 64u * 56 * 1024;
constexpr size_t WS_HO    = WS_Z2N + 28u * 128 * 1024;
constexpr size_t WS_STATS = WS_HO + 64u * 88 * 1024;
constexpr size_t WS_KMAX  = WS_STATS + 2048u * 28 * 8;
constexpr size_t WS_GATE  = WS_KMAX + 64u * 32 * 4;
constexpr size_t WS_SYNC  = WS_GATE + 2048u * 4;
constexpr int SY_LN = 0, SY_H = 16, SY_O = 48, SY_TMO = 56, SY_N = 57;
constexpr size_t WS_END   = WS_SYNC + SY_N * 256;

DI unsigned cvtpk(float a, float b) { f32x2 f = {a, b}; bf16x2_t r = __builtin_convertvector(f, bf16x2_t); return __builtin_bit_cast(unsigned, r); }
DI float bf_lo(unsigned u) { return __uint_as_float(u << 16); }
DI float bf_hi(unsigned u) { return __uint_as_float(u & 0xffff0000u); }
DI void glds16(const void* g, unsigned lds_base) {
    unsigned sv;
    asm volatile("s_mov_b32 %0, m0\n\ts_mov_b32 m0, %2\n\ts_nop 0\n\tglobal_load_lds_dwordx4 %1, off\n\ts_mov_b32 m0, %0"
                 : "=&s"(sv) : "v"(g), "s"(lds_base) : "memory");
}
DI void glds16s(const void* ubase, unsigned voff, unsigned lds_base) {
    asm volatile("s_mov_b32 m0, %2\n\ts_nop 0\n\tglobal_load_lds_dwordx4 %0, %1" :: "v"(voff), "s"(ubase), "s"(lds_base) : "memory");
}
#define GAS __attribute__((address_space(1)))
typedef GAS unsigned gu32;
#define RLX_AGENT __ATOMIC_RELAXED, __HIP_MEMORY_SCOPE_AGENT
DI gu32* sync_word(char* ws, int i) { return (gu32*)(ws + WS_SYNC + (size_t)i * 256); }
DI void st16_wt(char* ws, size_t off, u32x4 v) {
    __builtin_amdgcn_raw_buffer_store_b128(v, __builtin_amdgcn_make_buffer_rsrc(ws, 0, (int)WS_END, 0x00020000), (int)off, 0, 16);
}
DI bf16x8 ld16_dev(char* ws, size_t off) {
    return __builtin_bit_cast(bf16x8, __builtin_amdgcn_raw_buffer_load_b128(__builtin_amdgcn_make_buffer_rsrc(ws, 0, (int)WS_END, 0x00020000), (int)off, 0, 16));
}
DI void prefetch_line(const void* ubase, unsigned voff, unsigned lds_dump) {
    asm volatile("s_mov_b32 m0, %2\n\ts_nop 0\n\tglobal_load_lds_dword %0, %1" :: "v"(voff), "s"(ubase), "s"(lds_dump) : "memory");
}
DI void prefetch_ptr(const void* g, unsigned lds_dump) {
    asm volatile("s_mov_b32 m0, %1\n\ts_nop 0\n\tglobal_load_lds_dword %0, off" :: "v"(g), "s"(lds_dump) : "memory");
}
DI void drain_stores() { asm volatile("s_waitcnt vmcnt(0)" ::: "memory"); }
DI void wait_words(char* ws, int first, int n, unsigned want, int lane) {
    gu32* p = sync_word(ws, first + lane % n);
    for (unsigned spins = 0;; ++spins) {
        if (__all(__hip_atomic_load(p, RLX_AGENT) >= want)) break;
        if (spins > (1u << 18)) { if (lane == 0) __hip_atomic_store(sync_word(ws, SY_TMO), 1u, RLX_AGENT); break; }
        __builtin_amdgcn_s_sleep(2);
    }
    asm volatile("" ::: "memory");
}
DI void bar_only() { asm volatile("" ::: "memory"); __builtin_amdgcn_s_waitcnt(0xC07F); __builtin_amdgcn_s_barrier(); asm volatile("" ::: "memory"); }
template <int N> DI void wait_bar() { asm volatile("s_waitcnt vmcnt(%0)" :: "i"(N) : "memory"); bar_only(); }
DI s16x4 vtr(lds_cptr p) { return __builtin_bit_cast(s16x4, __builtin_amdgcn_ds_read_tr16_b64_v4i16((LDS_AS v4i16_t*)p)); }
DI bf16x8 trfrag(lds_cptr p0, lds_cptr p1) { s16x4 lo = vtr(p0), hi = vtr(p1); return __builtin_shufflevector(lo, hi, 0, 1, 2, 3, 4, 5, 6, 7); }
DI bf16x8 ldsfrag(lds_cptr p) { return *(const LDS_AS bf16x8*)p; }
DI u32x4 pack8(const f32x16& a, int o, float sc) {
    u32x4 r; r[0] = cvtpk(a[o] * sc, a[o + 1] * sc); r[1] = cvtpk(a[o + 2] * sc, a[o + 3] * sc);
    r[2] = cvtpk(a[o + 4] * sc, a[o + 5] * sc); r[3] = cvtpk(a[o + 6] * sc, a[o + 7] * sc); return r;
}
DI float swap32_sum(float v) { auto r = __builtin_amdgcn_permlane32_swap(__float_as_uint(v), __float_as_uint(v), false, false); return __uint_as_float(r[0]) + __uint_as_float(r[1]); }
DI float swap32_max(float v) { auto r = __builtin_amdgcn_permlane32_swap(__float_as_uint(v), __float_as_uint(v), false, false); return fmaxf(__uint_as_float(r[0]), __uint_as_float(r[1])); }
DI float dpp_max16(float v) {
    v = fmaxf(v, __uint_as_float(__builtin_amdgcn_update_dpp(0u, __float_as_uint(v), 0x128, 0xf, 0xf, false)));
    v = fmaxf(v, __uint_as_float(__builtin_amdgcn_update_dpp(0u, __float_as_uint(v), 0x124, 0xf, 0xf, false)));
    v = fmaxf(v, __uint_as_float(__builtin_amdgcn_update_dpp(0u, __float_as_uint(v), 0x122, 0xf, 0xf, false)));
    v = fmaxf(v, __uint_as_float(__builtin_amdgcn_update_dpp(0u, __float_as_uint(v), 0x121, 0xf, 0xf, false)));
    return v;
}
DI float wave_max_fast(float v) {
    v = dpp_max16(v);
    auto r = __builtin_amdgcn_permlane16_swap(__float_as_uint(v), __float_as_uint(v), false, false);
    v = fmaxf(__uint_as_float(r[0]), __uint_as_float(r[1]));
    return swap32_max(v);
}

DI float gelu_tanh(float x) {
    const float c1 = -2.0f * 0.7978845608028654f * 1.4426950408889634f, c2 = c1 * 0.044715f;
    const float a = x * __builtin_fmaf(x * x, c2, c1);
    return x * __builtin_amdgcn_rcpf(1.0f + __builtin_amdgcn_exp2f(a));
}

constexpr int K1_ST = 40960, K1_LDS = 3 * K1_ST + 1024 + 256;

template <int TYPE>
DI void k1_body(const u16* __restrict__ x, const u16* __restrict__ wqkv, const u16* __restrict__ w_in, const u16* __restrict__ gate_w,
                const u16* __restrict__ w_sp, const u16* __restrict__ wo, const u16* __restrict__ w_out,
                char* __restrict__ ws, char* lds, int mt, int p, int nl, int tid) {
    constexpr int NG = TYPE == 0 ? 3 : (TYPE == 1 ? 2 : 1);
    constexpr int NA = TYPE == 2 ? 1 : (TYPE == 3 ? 2 : NG);
    constexpr int P = 4 + 2 * NG;
    const int l = tid & 63, w = __builtin_amdgcn_readfirstlane(tid >> 6);
    const int rot = mt & 7;
    if (w >= 4) {
        const int j = w - 4;
        const unsigned lds0 = (unsigned)(uintptr_t)lds;
        const int row = 8 * j + (l >> 3);
        const unsigned xv = (unsigned)(row * D_ + ((l & 7) ^ ((row >> 1) & 7)) * 8) * 2;
        const int wch = ((l & 7) ^ (((row >> 1) & 1) << 2)) * 8;
        const unsigned qv = (unsigned)(row * 1536 + wch) * 2, zv = (unsigned)(row * 1792 + wch) * 2;
        const char* xu = (const char*)(x + (size_t)mt * 128 * D_);
        const char* g0 = (const char*)(wqkv + p * 64 + (TYPE == 0 ? 0 : 1024));
        const char* g1 = TYPE == 0 ? (const char*)(wqkv + p * 64 + 512) : (const char*)(w_in + FH + (8 + p) * 64);
        const char* g2 = (const char*)(w_in + FH + p * 64);
        auto issue = [&](int s) {
            const unsigned dst = lds0 + (s % 3) * K1_ST + j * 1024;
            const int sr = (s + rot) & 7;
#pragma unroll
            for (int i = 0; i < 4; ++i) glds16s(xu + (size_t)i * 32 * D_ * 2 + sr * 128, xv, dst + i * 4096);
            glds16s(g0 + (size_t)sr * 64 * 3072, qv, dst + 16384); glds16s(g0 + (size_t)(sr * 64 + 32) * 3072, qv, dst + 16384 + 4096);
            if (NG >= 2) { constexpr int ld = TYPE == 0 ? 3072 : 3584; const unsigned gv = TYPE == 0 ? qv : zv;
                glds16s(g1 + (size_t)sr * 64 * ld, gv, dst + 16384 + 8192); glds16s(g1 + (size_t)(sr * 64 + 32) * ld, gv, dst + 16384 + 8192 + 4096); }
            if (NG >= 3) { glds16s(g2 + (size_t)sr * 64 * 3584, zv, dst + 32768); glds16s(g2 + (size_t)(sr * 64 + 32) * 3584, zv, dst + 32768 + 4096); }
        };
        if (TYPE == 3 && j == 0) glds16s(gate_w, l * 16, lds0 + 3 * K1_ST);
        issue(0); issue(1); issue(2);
        wait_bar<2 * P>();
#pragma unroll 1
        for (int s = 0; s < 7; ++s) {
            if (s + 2 < 8) asm volatile("s_waitcnt vmcnt(%0)" :: "i"(P) : "memory"); else asm volatile("s_waitcnt vmcnt(0)" ::: "memory");
            bar_only();
            if (s + 3 < 8) issue(s + 3);
        }
        return;
    }
    const int rh = w & 1, ch = w >> 1, h = l >> 5, r32 = l & 31;
    int xo[4];
    { const int row = rh * 64 + r32, f = (row >> 1) & 7;
#pragma unroll
      for (int c = 0; c < 4; ++c) xo[c] = row * 128 + (((2 * c + h) ^ f) << 4); }
    const int q4 = (l & 15) >> 2, p4 = l & 3, b16 = (l >> 4) & 1;
    const int wl = 16384 + (8 * h + q4) * 128 + (((ch * 4 + b16 * 2 + (p4 >> 1)) ^ ((q4 >> 1) << 2)) << 4) + (p4 & 1) * 8;
    f32x16 acc[NA][2];
#pragma unroll
    for (int g = 0; g < NA; ++g) { acc[g][0] = f32x16{}; acc[g][1] = f32x16{}; }
    bf16x8 xa[2][2], wa[NG][2], xb[2][2], wb[NG][2], ga[2], gb[2];
    auto rdh = [&](int s, int half, bf16x8 (&xf)[2][2], bf16x8 (&wf)[NG][2], bf16x8 (&gf)[2]) {
        lds_cptr base = (lds_cptr)lds + (s % 3) * K1_ST;
#pragma unroll
        for (int kk = 0; kk < 2; ++kk) {
            const int ks = 2 * half + kk;
            xf[0][kk] = ldsfrag(base + xo[ks]); xf[1][kk] = ldsfrag(base + xo[ks] + 4096);
#pragma unroll
            for (int g = 0; g < NG; ++g) wf[g][kk] = trfrag(base + wl + g * 8192 + ks * 2048, base + wl + g * 8192 + ks * 2048 + 512);
            if (TYPE == 3) gf[kk] = ldsfrag((lds_cptr)lds + 3 * K1_ST + (((s + rot) & 7) * 4 + ks) * 32 + h * 16);
        }
    };
    auto mm = [&](bf16x8 (&xf)[2][2], bf16x8 (&wf)[NG][2], bf16x8 (&gf)[2]) {
#pragma unroll
        for (int kk = 0; kk < 2; ++kk)
#pragma unroll
            for (int rb = 0; rb < 2; ++rb) {
                if (TYPE == 0) {
#pragma unroll
                    for (int g = 0; g < 3; ++g) acc[g % NA][rb] = MFMA(wf[g % NG][kk], xf[rb][kk], acc[g % NA][rb]);
                } else {
                    acc[0][rb] = MFMA(xf[rb][kk], wf[0][kk], acc[0][rb]);
                    if (TYPE == 1) acc[NA - 1][rb] = MFMA(wf[NG - 1][kk], xf[rb][kk], acc[NA - 1][rb]);
                    if (TYPE == 3) acc[NA - 1][rb] = MFMA(gf[kk], xf[rb][kk], acc[NA - 1][rb]);
                }
            }
    };
    bar_only();
    {
        const int xcd = blockIdx.x & 7, idx = blockIdx.x >> 3;
        const unsigned dump = (unsigned)(uintptr_t)lds + 3 * K1_ST + 1024;
        if (w < 2) { const int L = idx * 128 + w * 64 + l; prefetch_line(w_in, (unsigned)((L >> 3) * 3584 + min((xcd >> 2) * 1024 + (L & 7) * 128, 1664)), dump); }
    }
    rdh(0, 0, xa, wa, ga);
#pragma unroll 1
    for (int s = 0; s < 7; ++s) {
        rdh(s, 1, xb, wb, gb); mm(xa, wa, ga); interleave<4 * NA, 2>(); SB();
        bar_only();
        rdh(s + 1, 0, xa, wa, ga); mm(xb, wb, gb); interleave<4 * NA, 2>(); SB();
    }
    rdh(7, 1, xb, wb, gb); mm(xa, wa, ga); interleave<4 * NA, 2>(); SB();
    mm(xb, wb, gb);
#pragma unroll
    for (int rb = 0; rb < 2; ++rb) {
        const int ttile = mt * 4 + rh * 2 + rb;
        if (TYPE == 0) {
            const int hd0 = 4 * p + 2 * ch;
            const f32x16 &q = acc[0][rb], &k = acc[1 % NA][rb];
            st16_wt(ws, WS_QF + ((size_t)(hd0 * 64 + ttile) * 1024) + l * 16, pack8(q, 0, QSCALE));
            st16_wt(ws, WS_QF + ((size_t)((hd0 + 1) * 64 + ttile) * 1024) + l * 16, pack8(q, 8, QSCALE));
            st16_wt(ws, WS_KV + ((size_t)(hd0 * 64 + ttile) * 2048) + l * 16, pack8(k, 0, 1.0f));
            st16_wt(ws, WS_KV + ((size_t)((hd0 + 1) * 64 + ttile) * 2048) + l * 16, pack8(k, 8, 1.0f));
            float n0 = 0.f, n1 = 0.f;
#pragma unroll
            for (int r = 0; r < 8; ++r) { n0 = __builtin_fmaf(k[r], k[r], n0); n1 = __builtin_fmaf(k[8 + r], k[8 + r], n1); }
            n0 = wave_max_fast(swap32_sum(n0)); n1 = wave_max_fast(swap32_sum(n1));
            if (l == 0) { float* km = (float*)(ws + WS_KMAX) + ttile * 32 + hd0; km[0] = n0 * 1.01f; km[1] = n1 * 1.01f; }
        } else {
            const int hd = 4 * p + 2 * ch + (r32 >> 4), d = r32 & 15;
            const size_t vp = WS_KV + ((size_t)(hd * 64 + ttile) * 2048) + 1024 + h * 256 + d * 16;
            st16_wt(ws, vp, pack8(acc[0][rb], 0, 1.0f)); st16_wt(ws, vp + 512, pack8(acc[0][rb], 8, 1.0f));
            if (TYPE == 3 && ch == 0 && h == 0)
                ((float*)(ws + WS_GATE))[ttile * 32 + r32] = __builtin_amdgcn_rcpf(1.0f + __builtin_amdgcn_exp2f(-1.4426950408889634f * acc[NA - 1][rb][0]));
        }
        if (TYPE <= 1) {
            f32x16& z = acc[NA - 1][rb];
            const int zg = TYPE == 0 ? p : 8 + p;
            float s1 = 0.f, s2 = 0.f;
#pragma unroll
            for (int r = 0; r < 16; ++r) { const float v = gelu_tanh(z[r]); z[r] = v; s1 += v; s2 = __builtin_fmaf(v, v, s2); }
            const int fb = 2 * zg + ch;
            const size_t zp = WS_Z2 + ((size_t)(ttile * 56 + 2 * fb) * 1024) + l * 16;
            st16_wt(ws, zp, pack8(z, 0, 1.0f)); st16_wt(ws, zp + 1024, pack8(z, 8, 1.0f));
            s1 = swap32_sum(s1); s2 = swap32_sum(s2);
            if (h == 0) ((f32x2*)(ws + WS_STATS))[(size_t)(ttile * 32 + r32) * 28 + fb] = f32x2{s1, s2};
        }
    }
    if (w < 2) drain_stores();
}

extern "C" __global__ __launch_bounds__(512) void k1_proj(const u16* __restrict__ x, const u16* __restrict__ wqkv, const u16* __restrict__ w_in, const u16* __restrict__ gate_w,
                                               const u16* __restrict__ w_sp, const u16* __restrict__ wo, const u16* __restrict__ w_out, char* __restrict__ ws) {
    __shared__ __attribute__((aligned(1024))) char lds[K1_LDS];
    const int b = blockIdx.x, xcd = b & 7, idx = b >> 3;
    const int mt = (xcd & 1) * 8 + (idx & 7), nt = (xcd >> 1) * 4 + (idx >> 3), p = nt >> 1;
    if (b == 255 && threadIdx.x < SY_N) *(unsigned*)(ws + WS_SYNC + threadIdx.x * 256) = 0u;
    if (!(nt & 1)) k1_body<0>(x, wqkv, w_in, gate_w, w_sp, wo, w_out, ws, lds, mt, p, idx >> 3, threadIdx.x);
    else if (p < 6) k1_body<1>(x, wqkv, w_in, gate_w, w_sp, wo, w_out, ws, lds, mt, p, idx >> 3, threadIdx.x);
    else if (p == 6) k1_body<2>(x, wqkv, w_in, gate_w, w_sp, wo, w_out, ws, lds, mt, p, idx >> 3, threadIdx.x);
    else k1_body<3>(x, wqkv, w_in, gate_w, w_sp, wo, w_out, ws, lds, mt, p, idx >> 3, threadIdx.x);
}

struct LnStat { float rstd, nm; };
DI LnStat ln_stats(const f32x4 (&pv)[7]) {
    float s1 = 0.f, s2 = 0.f;
#pragma unroll
    for (int i = 0; i < 7; ++i) { const f32x4 v = pv[i]; s1 += v[0] + v[2]; s2 += v[1] + v[3]; }
    s1 = swap32_sum(s1); s2 = swap32_sum(s2);
    const float mean = s1 * (1.0f / FH);
    const float var = fmaxf(s2 * (1.0f / FH) - mean * mean, 0.f);
    LnStat r; r.rstd = __builtin_amdgcn_rsqf(var + 1e-5f); r.nm = -mean * r.rstd; return r;
}
DI void ln_block(char* __restrict__ ws, char* scratch, int ttile, int fb, const u32x4& v0, const u32x4& v1, LnStat st, int l) {
    const int h = l >> 5, r32 = l & 31, q4 = (l & 15) >> 2, p4 = l & 3, b16 = (l >> 4) & 1;
    u32x4 ov[2];
#pragma unroll
    for (int g = 0; g < 2; ++g) {
        const u32x4 v = g ? v1 : v0;
        unsigned o[4];
#pragma unroll
        for (int e = 0; e < 4; ++e) o[e] = cvtpk(__builtin_fmaf(bf_lo(v[e]), st.rstd, st.nm), __builtin_fmaf(bf_hi(v[e]), st.rstd, st.nm));
        auto r0 = __builtin_amdgcn_permlane32_swap(o[0], o[2], false, false);
        auto r1 = __builtin_amdgcn_permlane32_swap(o[1], o[3], false, false);
        ov[g] = u32x4{r0[0], r1[0], r0[1], r1[1]};
    }
    lds_ptr tile = (lds_ptr)scratch;
#pragma unroll
    for (int kk = 0; kk < 2; ++kk) {
        if ((r32 >> 4) == kk) {
            *(LDS_AS u32x4*)(tile + (r32 & 15) * 64 + h * 16) = ov[0]; *(LDS_AS u32x4*)(tile + (r32 & 15) * 64 + 32 + h * 16) = ov[1]; }
        asm volatile("" ::: "memory");
        lds_cptr rp = (lds_cptr)tile + (8 * h + q4) * 64 + b16 * 32 + p4 * 8;
        const bf16x8 fr = trfrag(rp, rp + 256);
        asm volatile("" ::: "memory");
        st16_wt(ws, WS_Z2N + ((size_t)(fb * 128 + ttile * 2 + kk) * 1024) + l * 16, __builtin_bit_cast(u32x4, fr));
    }
}

constexpr int AT_TS = 2304, AT_SLOT = 8 * AT_TS, AT_LDS = 4 * AT_SLOT + 64;
#define ATTN_ASM \
    "s_mov_b32 %[sv], m0\n\t" \
    "v_mov_b32 v88, 0\n\t" \
    "v_mov_b32 v89, 0\n\t" \
    "v_mov_b32 v90, 0\n\t" \
    "v_mov_b32 v91, 0\n\t" \
    "v_mov_b32 v96, 0\n\t" \
    "v_mov_b32 v97, 0\n\t" \
    "v_mov_b32 v98, 0\n\t" \
    "v_mov_b32 v99, 0\n\t" \
    "v_mov_b32 v0, 0\n\t" \
    "v_mov_b32 v1, 0\n\t" \
    "v_mov_b32 v2, 0\n\t" \
    "v_mov_b32 v3, 0\n\t" \
    "v_mov_b32 v4, 0\n\t" \
    "v_mov_b32 v5, 0\n\t" \
    "v_mov_b32 v6, 0\n\t" \
    "v_mov_b32 v7, 0\n\t" \
    "v_mov_b32 v8, 0\n\t" \
    "v_mov_b32 v9, 0\n\t" \
    "v_mov_b32 v10, 0\n\t" \
    "v_mov_b32 v11, 0\n\t" \
    "v_mov_b32 v12, 0\n\t" \
    "v_mov_b32 v13, 0\n\t" \
    "v_mov_b32 v14, 0\n\t" \
    "v_mov_b32 v15, 0\n\t" \
    "s_mov_b32 %[cnt], 0\n\t" \
    "s_waitcnt vmcnt(4) lgkmcnt(0)\n\t" \
    "s_barrier\n\t" \
    "ds_read_b128 v[68:71], %[kl] offset:0\n\t" \
    "ds_read_b128 v[72:75], %[kl] offset:2304\n\t" \
    "ds_read_b128 v[76:79], %[v0l] offset:0\n\t" \
    "ds_read_b128 v[80:83], %[v1l] offset:0\n\t" \
    "s_waitcnt lgkmcnt(0)\n\t" \
    "s_setprio 3\n\t" \
    "v_mfma_f32_32x32x16_bf16 v[32:47], v[68:71], v[64:67], v[16:31]\n\t" \
    "s_setprio 0\n\t" \
    "s_nop 7\n\t" \
    "s_nop 3\n\t" \
    "Lloop%=:\n\t" \
    "s_setprio 3\n\t" \
    "v_mfma_f32_32x32x16_bf16 v[48:63], v[72:75], v[64:67], v[16:31]\n\t" \
    "s_setprio 2\n\t" \
    "v_exp_f32 v32, v32\n\t" \
    "ds_read_b128 v[68:71], %[kl] offset:4608\n\t" \
    "v_exp_f32 v33, v33\n\t" \
    "v_exp_f32 v34, v34\n\t" \
    "v_exp_f32 v35, v35\n\t" \
    "v_exp_f32 v36, v36\n\t" \
    "v_exp_f32 v37, v37\n\t" \
    "v_exp_f32 v38, v38\n\t" \
    "s_setprio 3\n\t" \
    "v_mfma_f32_32x32x16_bf16 v[0:15], v[88:91], v[96:99], v[0:15]\n\t" \
    "s_setprio 2\n\t" \
    "v_exp_f32 v39, v39\n\t" \
    "ds_read_b128 v[84:87], %[v0l] offset:2304\n\t" \
    "v_cvt_pk_bf16_f32 v92, v32, v33\n\t" \
    "v_cvt_pk_bf16_f32 v93, v34, v35\n\t" \
    "v_cvt_pk_bf16_f32 v94, v36, v37\n\t" \
    "v_cvt_pk_bf16_f32 v95, v38, v39\n\t" \
    "v_exp_f32 v40, v40\n\t" \
    "ds_read_b128 v[88:91], %[v1l] offset:2304\n\t" \
    "v_exp_f32 v41, v41\n\t" \
    "v_exp_f32 v42, v42\n\t" \
    "v_exp_f32 v43, v43\n\t" \
    "s_waitcnt lgkmcnt(4)\n\t" \
    "s_setprio 3\n\t" \
    "v_mfma_f32_32x32x16_bf16 v[0:15], v[76:79], v[92:95], v[0:15]\n\t" \
    "s_setprio 2\n\t" \
    "v_exp_f32 v44, v44\n\t" \
    "v_exp_f32 v45, v45\n\t" \
    "v_exp_f32 v46, v46\n\t" \
    "v_exp_f32 v47, v47\n\t" \
    "s_waitcnt lgkmcnt(2)\n\t" \
    "v_cvt_pk_bf16_f32 v96, v40, v41\n\t" \
    "v_cvt_pk_bf16_f32 v97, v42, v43\n\t" \
    "v_cvt_pk_bf16_f32 v98, v44, v45\n\t" \
    "v_cvt_pk_bf16_f32 v99, v46, v47\n\t" \
    "s_setprio 3\n\t" \
    "v_mfma_f32_32x32x16_bf16 v[32:47], v[68:71], v[64:67], v[16:31]\n\t" \
    "s_setprio 1\n\t" \
    "v_exp_f32 v48, v48\n\t" \
    "ds_read_b128 v[72:75], %[kl] offset:6912\n\t" \
    "v_exp_f32 v49, v49\n\t" \
    "v_exp_f32 v50, v50\n\t" \
    "v_exp_f32 v51, v51\n\t" \
    "v_exp_f32 v52, v52\n\t" \
    "v_exp_f32 v53, v53\n\t" \
    "v_exp_f32 v54, v54\n\t" \
    "s_setprio 3\n\t" \
    "v_mfma_f32_32x32x16_bf16 v[0:15], v[80:83], v[96:99], v[0:15]\n\t" \
    "s_setprio 1\n\t" \
    "v_exp_f32 v55, v55\n\t" \
    "ds_read_b128 v[76:79], %[v0l] offset:4608\n\t" \
    "v_cvt_pk_bf16_f32 v92, v48, v49\n\t" \
    "v_cvt_pk_bf16_f32 v93, v50, v51\n\t" \
    "v_cvt_pk_bf16_f32 v94, v52, v53\n\t" \
    "v_cvt_pk_bf16_f32 v95, v54, v55\n\t" \
    "v_exp_f32 v56, v56\n\t" \
    "ds_read_b128 v[80:83], %[v1l] offset:4608\n\t" \
    "v_exp_f32 v57, v57\n\t" \
    "v_exp_f32 v58, v58\n\t" \
    "v_exp_f32 v59, v59\n\t" \
    "s_waitcnt lgkmcnt(4)\n\t" \
    "s_setprio 3\n\t" \
    "v_mfma_f32_32x32x16_bf16 v[0:15], v[84:87], v[92:95], v[0:15]\n\t" \
    "s_setprio 1\n\t" \
    "v_exp_f32 v60, v60\n\t" \
    "v_exp_f32 v61, v61\n\t" \
    "v_exp_f32 v62, v62\n\t" \
    "v_exp_f32 v63, v63\n\t" \
    "s_waitcnt lgkmcnt(2)\n\t" \
    "v_cvt_pk_bf16_f32 v96, v56, v57\n\t" \
    "v_cvt_pk_bf16_f32 v97, v58, v59\n\t" \
    "v_cvt_pk_bf16_f32 v98, v60, v61\n\t" \
    "v_cvt_pk_bf16_f32 v99, v62, v63\n\t" \
    "s_setprio 3\n\t" \
    "v_mfma_f32_32x32x16_bf16 v[48:63], v[72:75], v[64:67], v[16:31]\n\t" \
    "s_setprio 0\n\t" \
    "v_exp_f32 v32, v32\n\t" \
    "ds_read_b128 v[68:71], %[kl] offset:9216\n\t" \
    "v_exp_f32 v33, v33\n\t" \
    "v_exp_f32 v34, v34\n\t" \
    "v_exp_f32 v35, v35\n\t" \
    "v_exp_f32 v36, v36\n\t" \
    "v_exp_f32 v37, v37\n\t" \
    "v_exp_f32 v38, v38\n\t" \
    "s_setprio 3\n\t" \
    "v_mfma_f32_32x32x16_bf16 v[0:15], v[88:91], v[96:99], v[0:15]\n\t" \
    "s_setprio 0\n\t" \
    "v_exp_f32 v39, v39\n\t" \
    "ds_read_b128 v[84:87], %[v0l] offset:6912\n\t" \
    "v_cvt_pk_bf16_f32 v92, v32, v33\n\t" \
    "v_cvt_pk_bf16_f32 v93, v34, v35\n\t" \
    "v_cvt_pk_bf16_f32 v94, v36, v37\n\t" \
    "v_cvt_pk_bf16_f32 v95, v38, v39\n\t" \
    "v_exp_f32 v40, v40\n\t" \
    "ds_read_b128 v[88:91], %[v1l] offset:6912\n\t" \
    "v_exp_f32 v41, v41\n\t" \
    "v_exp_f32 v42, v42\n\t" \
    "v_exp_f32 v43, v43\n\t" \
    "s_waitcnt lgkmcnt(4)\n\t" \
    "s_setprio 3\n\t" \
    "v_mfma_f32_32x32x16_bf16 v[0:15], v[76:79], v[92:95], v[0:15]\n\t" \
    "s_setprio 0\n\t" \
    "v_exp_f32 v44, v44\n\t" \
    "v_exp_f32 v45, v45\n\t" \
    "v_exp_f32 v46, v46\n\t" \
    "v_exp_f32 v47, v47\n\t" \
    "s_waitcnt lgkmcnt(2)\n\t" \
    "v_cvt_pk_bf16_f32 v96, v40, v41\n\t" \
    "v_cvt_pk_bf16_f32 v97, v42, v43\n\t" \
    "v_cvt_pk_bf16_f32 v98, v44, v45\n\t" \
    "v_cvt_pk_bf16_f32 v99, v46, v47\n\t" \
    "s_setprio 3\n\t" \
    "v_mfma_f32_32x32x16_bf16 v[32:47], v[68:71], v[64:67], v[16:31]\n\t" \
    "s_setprio 0\n\t" \
    "v_exp_f32 v48, v48\n\t" \
    "ds_read_b128 v[72:75], %[kl] offset:11520\n\t" \
    "v_exp_f32 v49, v49\n\t" \
    "v_exp_f32 v50, v50\n\t" \
    "v_exp_f32 v51, v51\n\t" \
    "v_exp_f32 v52, v52\n\t" \
    "v_exp_f32 v53, v53\n\t" \
    "v_exp_f32 v54, v54\n\t" \
    "s_setprio 3\n\t" \
    "v_mfma_f32_32x32x16_bf16 v[0:15], v[80:83], v[96:99], v[0:15]\n\t" \
    "s_setprio 0\n\t" \
    "v_exp_f32 v55, v55\n\t" \
    "ds_read_b128 v[76:79], %[v0l] offset:9216\n\t" \
    "v_cvt_pk_bf16_f32 v92, v48, v49\n\t" \
    "v_cvt_pk_bf16_f32 v93, v50, v51\n\t" \
    "v_cvt_pk_bf16_f32 v94, v52, v53\n\t" \
    "v_cvt_pk_bf16_f32 v95, v54, v55\n\t" \
    "v_exp_f32 v56, v56\n\t" \
    "ds_read_b128 v[80:83], %[v1l] offset:9216\n\t" \
    "v_exp_f32 v57, v57\n\t" \
    "v_exp_f32 v58, v58\n\t" \
    "v_exp_f32 v59, v59\n\t" \
    "s_waitcnt lgkmcnt(4)\n\t" \
    "s_setprio 3\n\t" \
    "v_mfma_f32_32x32x16_bf16 v[0:15], v[84:87], v[92:95], v[0:15]\n\t" \
    "s_setprio 0\n\t" \
    "v_exp_f32 v60, v60\n\t" \
    "v_exp_f32 v61, v61\n\t" \
    "v_exp_f32 v62, v62\n\t" \
    "v_exp_f32 v63, v63\n\t" \
    "s_waitcnt lgkmcnt(2)\n\t" \
    "v_cvt_pk_bf16_f32 v96, v56, v57\n\t" \
    "v_cvt_pk_bf16_f32 v97, v58, v59\n\t" \
    "v_cvt_pk_bf16_f32 v98, v60, v61\n\t" \
    "v_cvt_pk_bf16_f32 v99, v62, v63\n\t" \
    "s_setprio 3\n\t" \
    "v_mfma_f32_32x32x16_bf16 v[48:63], v[72:75], v[64:67], v[16:31]\n\t" \
    "s_setprio 2\n\t" \
    "v_exp_f32 v32, v32\n\t" \
    "ds_read_b128 v[68:71], %[kl] offset:13824\n\t" \
    "v_exp_f32 v33, v33\n\t" \
    "v_exp_f32 v34, v34\n\t" \
    "v_exp_f32 v35, v35\n\t" \
    "v_exp_f32 v36, v36\n\t" \
    "v_exp_f32 v37, v37\n\t" \
    "v_exp_f32 v38, v38\n\t" \
    "s_setprio 3\n\t" \
    "v_mfma_f32_32x32x16_bf16 v[0:15], v[88:91], v[96:99], v[0:15]\n\t" \
    "s_setprio 2\n\t" \
    "v_exp_f32 v39, v39\n\t" \
    "ds_read_b128 v[84:87], %[v0l] offset:11520\n\t" \
    "v_cvt_pk_bf16_f32 v92, v32, v33\n\t" \
    "v_cvt_pk_bf16_f32 v93, v34, v35\n\t" \
    "v_cvt_pk_bf16_f32 v94, v36, v37\n\t" \
    "v_cvt_pk_bf16_f32 v95, v38, v39\n\t" \
    "v_exp_f32 v40, v40\n\t" \
    "ds_read_b128 v[88:91], %[v1l] offset:11520\n\t" \
    "v_exp_f32 v41, v41\n\t" \
    "v_exp_f32 v42, v42\n\t" \
    "v_exp_f32 v43, v43\n\t" \
    "s_waitcnt lgkmcnt(4)\n\t" \
    "s_setprio 3\n\t" \
    "v_mfma_f32_32x32x16_bf16 v[0:15], v[76:79], v[92:95], v[0:15]\n\t" \
    "s_setprio 2\n\t" \
    "v_exp_f32 v44, v44\n\t" \
    "v_exp_f32 v45, v45\n\t" \
    "v_exp_f32 v46, v46\n\t" \
    "v_exp_f32 v47, v47\n\t" \
    "s_waitcnt lgkmcnt(2)\n\t" \
    "v_cvt_pk_bf16_f32 v96, v40, v41\n\t" \
    "v_cvt_pk_bf16_f32 v97, v42, v43\n\t" \
    "v_cvt_pk_bf16_f32 v98, v44, v45\n\t" \
    "v_cvt_pk_bf16_f32 v99, v46, v47\n\t" \
    "s_setprio 3\n\t" \
    "v_mfma_f32_32x32x16_bf16 v[32:47], v[68:71], v[64:67], v[16:31]\n\t" \
    "s_setprio 1\n\t" \
    "v_exp_f32 v48, v48\n\t" \
    "ds_read_b128 v[72:75], %[kl] offset:16128\n\t" \
    "s_and_b32 %[t0], %[cnt], 3\n\t" \
    "s_cmp_eq_u32 %[t0], 3\n\t" \
    "s_mov_b32 %[dl], 18432\n\t" \
    "s_cselect_b32 %[dl], -55296, %[dl]\n\t" \
    "v_add_u32 %[kl], %[dl], %[kl]\n\t" \
    "v_exp_f32 v49, v49\n\t" \
    "v_exp_f32 v50, v50\n\t" \
    "v_exp_f32 v51, v51\n\t" \
    "v_exp_f32 v52, v52\n\t" \
    "v_exp_f32 v53, v53\n\t" \
    "v_exp_f32 v54, v54\n\t" \
    "s_setprio 3\n\t" \
    "v_mfma_f32_32x32x16_bf16 v[0:15], v[80:83], v[96:99], v[0:15]\n\t" \
    "s_setprio 1\n\t" \
    "v_exp_f32 v55, v55\n\t" \
    "ds_read_b128 v[76:79], %[v0l] offset:13824\n\t" \
    "v_cvt_pk_bf16_f32 v92, v48, v49\n\t" \
    "v_cvt_pk_bf16_f32 v93, v50, v51\n\t" \
    "v_cvt_pk_bf16_f32 v94, v52, v53\n\t" \
    "v_cvt_pk_bf16_f32 v95, v54, v55\n\t" \
    "v_exp_f32 v56, v56\n\t" \
    "ds_read_b128 v[80:83], %[v1l] offset:13824\n\t" \
    "v_exp_f32 v57, v57\n\t" \
    "v_exp_f32 v58, v58\n\t" \
    "v_exp_f32 v59, v59\n\t" \
    "s_waitcnt lgkmcnt(4)\n\t" \
    "s_setprio 3\n\t" \
    "v_mfma_f32_32x32x16_bf16 v[0:15], v[84:87], v[92:95], v[0:15]\n\t" \
    "s_setprio 1\n\t" \
    "v_exp_f32 v60, v60\n\t" \
    "v_exp_f32 v61, v61\n\t" \
    "v_exp_f32 v62, v62\n\t" \
    "v_exp_f32 v63, v63\n\t" \
    "s_waitcnt lgkmcnt(2)\n\t" \
    "v_cvt_pk_bf16_f32 v96, v56, v57\n\t" \
    "v_cvt_pk_bf16_f32 v97, v58, v59\n\t" \
    "v_cvt_pk_bf16_f32 v98, v60, v61\n\t" \
    "v_cvt_pk_bf16_f32 v99, v62, v63\n\t" \
    "s_cmp_eq_u32 %[cnt], 7\n\t" \
    "s_cbranch_scc1 Lskip%=\n\t" \
    "s_cmp_eq_u32 %[cnt], 6\n\t" \
    "s_cbranch_scc1 Lw0%=\n\t" \
    "s_waitcnt vmcnt(2)\n\t" \
    "s_barrier\n\t" \
    "s_cmp_gt_u32 %[cnt], 4\n\t" \
    "s_cbranch_scc1 Lskip%=\n\t" \
    "s_add_u32 %[t0], %[cnt], 3\n\t" \
    "s_and_b32 %[t0], %[t0], 3\n\t" \
    "s_mul_i32 %[t0], %[t0], 18432\n\t" \
    "s_add_u32 m0, %[t0], %[dst]\n\t" \
    "s_setprio 3\n\t" \
    "global_load_lds_dwordx4 %[goff], %[base]\n\t" \
    "global_load_lds_dwordx4 %[goff], %[base] offset:1024\n\t" \
    "s_setprio 1\n\t" \
    "v_add_u32 %[goff], 0x4000, %[goff]\n\t" \
    "s_branch Lskip%=\n\t" \
    "Lw0%=:\n\t" \
    "s_waitcnt vmcnt(0)\n\t" \
    "s_barrier\n\t" \
    "Lskip%=:\n\t" \
    "s_setprio 3\n\t" \
    "v_mfma_f32_32x32x16_bf16 v[48:63], v[72:75], v[64:67], v[16:31]\n\t" \
    "s_setprio 0\n\t" \
    "v_exp_f32 v32, v32\n\t" \
    "ds_read_b128 v[68:71], %[kl] offset:0\n\t" \
    "v_exp_f32 v33, v33\n\t" \
    "v_exp_f32 v34, v34\n\t" \
    "v_exp_f32 v35, v35\n\t" \
    "v_exp_f32 v36, v36\n\t" \
    "v_exp_f32 v37, v37\n\t" \
    "v_exp_f32 v38, v38\n\t" \
    "s_setprio 3\n\t" \
    "v_mfma_f32_32x32x16_bf16 v[0:15], v[88:91], v[96:99], v[0:15]\n\t" \
    "s_setprio 0\n\t" \
    "v_exp_f32 v39, v39\n\t" \
    "ds_read_b128 v[84:87], %[v0l] offset:16128\n\t" \
    "v_cvt_pk_bf16_f32 v92, v32, v33\n\t" \
    "v_cvt_pk_bf16_f32 v93, v34, v35\n\t" \
    "v_cvt_pk_bf16_f32 v94, v36, v37\n\t" \
    "v_cvt_pk_bf16_f32 v95, v38, v39\n\t" \
    "v_exp_f32 v40, v40\n\t" \
    "ds_read_b128 v[88:91], %[v1l] offset:16128\n\t" \
    "v_add_u32 %[v0l], %[dl], %[v0l]\n\t" \
    "v_add_u32 %[v1l], %[dl], %[v1l]\n\t" \
    "v_exp_f32 v41, v41\n\t" \
    "v_exp_f32 v42, v42\n\t" \
    "v_exp_f32 v43, v43\n\t" \
    "s_waitcnt lgkmcnt(4)\n\t" \
    "s_setprio 3\n\t" \
    "v_mfma_f32_32x32x16_bf16 v[0:15], v[76:79], v[92:95], v[0:15]\n\t" \
    "s_setprio 0\n\t" \
    "v_exp_f32 v44, v44\n\t" \
    "v_exp_f32 v45, v45\n\t" \
    "v_exp_f32 v46, v46\n\t" \
    "v_exp_f32 v47, v47\n\t" \
    "s_waitcnt lgkmcnt(2)\n\t" \
    "v_cvt_pk_bf16_f32 v96, v40, v41\n\t" \
    "v_cvt_pk_bf16_f32 v97, v42, v43\n\t" \
    "v_cvt_pk_bf16_f32 v98, v44, v45\n\t" \
    "v_cvt_pk_bf16_f32 v99, v46, v47\n\t" \
    "s_setprio 3\n\t" \
    "v_mfma_f32_32x32x16_bf16 v[32:47], v[68:71], v[64:67], v[16:31]\n\t" \
    "s_setprio 0\n\t" \
    "v_exp_f32 v48, v48\n\t" \
    "ds_read_b128 v[72:75], %[kl] offset:2304\n\t" \
    "v_exp_f32 v49, v49\n\t" \
    "v_exp_f32 v50, v50\n\t" \
    "v_exp_f32 v51, v51\n\t" \
    "v_exp_f32 v52, v52\n\t" \
    "v_exp_f32 v53, v53\n\t" \
    "v_exp_f32 v54, v54\n\t" \
    "s_setprio 3\n\t" \
    "v_mfma_f32_32x32x16_bf16 v[0:15], v[80:83], v[96:99], v[0:15]\n\t" \
    "s_setprio 0\n\t" \
    "v_exp_f32 v55, v55\n\t" \
    "ds_read_b128 v[76:79], %[v0l] offset:0\n\t" \
    "v_cvt_pk_bf16_f32 v92, v48, v49\n\t" \
    "v_cvt_pk_bf16_f32 v93, v50, v51\n\t" \
    "v_cvt_pk_bf16_f32 v94, v52, v53\n\t" \
    "v_cvt_pk_bf16_f32 v95, v54, v55\n\t" \
    "v_exp_f32 v56, v56\n\t" \
    "ds_read_b128 v[80:83], %[v1l] offset:0\n\t" \
    "v_exp_f32 v57, v57\n\t" \
    "v_exp_f32 v58, v58\n\t" \
    "v_exp_f32 v59, v59\n\t" \
    "s_waitcnt lgkmcnt(4)\n\t" \
    "s_setprio 3\n\t" \
    "v_mfma_f32_32x32x16_bf16 v[0:15], v[84:87], v[92:95], v[0:15]\n\t" \
    "s_setprio 0\n\t" \
    "v_exp_f32 v60, v60\n\t" \
    "v_exp_f32 v61, v61\n\t" \
    "v_exp_f32 v62, v62\n\t" \
    "v_exp_f32 v63, v63\n\t" \
    "s_waitcnt lgkmcnt(2)\n\t" \
    "v_cvt_pk_bf16_f32 v96, v56, v57\n\t" \
    "v_cvt_pk_bf16_f32 v97, v58, v59\n\t" \
    "v_cvt_pk_bf16_f32 v98, v60, v61\n\t" \
    "v_cvt_pk_bf16_f32 v99, v62, v63\n\t" \
    "s_add_u32 %[cnt], %[cnt], 1\n\t" \
    "s_cmp_lt_u32 %[cnt], 8\n\t" \
    "s_cbranch_scc1 Lloop%=\n\t" \
    "s_waitcnt lgkmcnt(0)\n\t" \
    "s_setprio 3\n\t" \
    "v_mfma_f32_32x32x16_bf16 v[0:15], v[88:91], v[96:99], v[0:15]\n\t" \
    "s_setprio 0\n\t" \
    "s_mov_b32 m0, %[sv]\n\t" \
    "s_setprio 0\n\t" \
    "s_nop 7\n\t" \
    "s_nop 7\n\t"
#define ATTN_CLOBBERS "v32", "v33", "v34", "v35", "v36", "v37", "v38", "v39", "v40", "v41", "v42", "v43", "v44", "v45", "v46", "v47", "v48", "v49", "v50", "v51", "v52", "v53", "v54", "v55", "v56", "v57", "v58", "v59", "v60", "v61", "v62", "v63", "v68", "v69", "v70", "v71", "v72", "v73", "v74", "v75", "v76", "v77", "v78", "v79", "v80", "v81", "v82", "v83", "v84", "v85", "v86", "v87", "v88", "v89", "v90", "v91", "v92", "v93", "v94", "v95", "v96", "v97", "v98", "v99"

DI void attn_block(char* __restrict__ ws, char* lds, int head, int chunk, int tid) {
    const int l = tid & 63, w = __builtin_amdgcn_readfirstlane(tid >> 6), h = l >> 5;
    const int qtile = chunk * 8 + w;
    const unsigned lds0 = (unsigned)(uintptr_t)lds;
    const char* kvbase = ws + WS_KV + (size_t)head * 64 * 2048;
    const unsigned dst = lds0 + w * AT_TS;
    __builtin_amdgcn_s_setprio(3);
    bf16x8 qf; float km, gate;
    asm volatile("global_load_dwordx4 %0, %3, off\n\tglobal_load_dword %1, %4, off\n\tglobal_load_dword %2, %5, off"
                 : "=&v"(qf), "=&v"(km), "=&v"(gate)
                 : "v"(ws + WS_QF + ((size_t)(head * 64 + qtile) * 1024) + l * 16), "v"((const float*)(ws + WS_KMAX) + l * 32 + head),
                   "v"((const float*)(ws + WS_GATE) + qtile * 32 + (l & 31)) : "memory");
    auto issue3 = [&]() {
#pragma unroll
        for (int c = 0; c < 3; ++c) { glds16(kvbase + c * 16384 + w * 2048 + l * 16, dst + c * AT_SLOT); glds16(kvbase + c * 16384 + w * 2048 + 1024 + l * 16, dst + c * AT_SLOT + 1024); }
    };
    issue3();
    {
        const u32x4 ones = {0x3f803f80u, 0x3f803f80u, 0x3f803f80u, 0x3f803f80u};
        *(LDS_AS u32x4*)((lds_ptr)lds + (tid >> 4) * AT_TS + 2048 + (tid & 15) * 16) = ones;
        if (tid == 0) *(LDS_AS unsigned*)((lds_ptr)lds + 4 * AT_SLOT) = 0u;
    }
    asm volatile("s_waitcnt vmcnt(6)" : "+v"(qf), "+v"(km), "+v"(gate));
    float qn = 0.f;
    { const u32x4 qu = __builtin_bit_cast(u32x4, qf);
#pragma unroll
      for (int e = 0; e < 4; ++e) { qn = __builtin_fmaf(bf_lo(qu[e]), bf_lo(qu[e]), qn); qn = __builtin_fmaf(bf_hi(qu[e]), bf_hi(qu[e]), qn); } }
    qn = swap32_sum(qn);
    float ref = __builtin_amdgcn_sqrtf(qn * wave_max_fast(km));
    unsigned kl = lds0 + l * 16;
    const bool onesl = (l & 16) != 0;
    const unsigned ol = lds0 + 2048 + ((((l & 15) >= 4) && ((l & 15) < 12)) ? 64 : 0);
    unsigned v0l = onesl ? ol : lds0 + 1024 + h * 256 + (l & 15) * 16, v1l = onesl ? ol : v0l + 512;
    LDS_AS volatile unsigned* flag = (LDS_AS volatile unsigned*)((lds_ptr)lds + 4 * AT_SLOT);
    const size_t ooff = WS_HO + ((size_t)(qtile * 88 + head) * 1024) + l * 16;
#pragma unroll 1
    for (int pass = 0;; ++pass) {
        f32x16 negm, o;
#pragma unroll
        for (int r = 0; r < 16; ++r) negm[r] = -ref;
        unsigned sv, cnt, t0, dl, goff = 3 * 16384 + w * 2048 + l * 16;
        asm volatile(ATTN_ASM
                     : "=&{v[0:15]}"(o), [sv] "=&s"(sv), [cnt] "=&s"(cnt), [t0] "=&s"(t0), [dl] "=&s"(dl), [goff] "+v"(goff), [kl] "+v"(kl), [v0l] "+v"(v0l), [v1l] "+v"(v1l)
                     : "{v[16:31]}"(negm), "{v[64:67]}"(qf), [dst] "s"(dst), [base] "s"(kvbase)
                     : ATTN_CLOBBERS, "scc", "memory");
        st16_wt(ws, ooff, pack8(o, 0, gate * __builtin_amdgcn_rcpf(o[8])));
        drain_stores();
        if (pass == 1) { __syncthreads(); break; }
        const bool bad = !(o[8] >= 5.421011e-20f);
        if (__any(bad) && l == 0) *flag = 1u;
        __syncthreads();
        if (*flag == 0u) break;
        float m = -3.0e38f;
        for (int t = 0; t < 64; ++t) {
            const bf16x8 kf = *(const bf16x8*)(kvbase + (size_t)t * 2048 + l * 16);
            const f32x16 s = MFMA(kf, qf, f32x16{});
#pragma unroll
            for (int r = 0; r < 16; ++r) m = fmaxf(m, s[r]);
        }
        ref = swap32_max(m);
        asm volatile("s_waitcnt vmcnt(0)" ::: "memory");
        issue3();
    }
    if (tid == 0) __hip_atomic_fetch_add(sync_word(ws, SY_O + chunk), 1u, RLX_AGENT);
}
constexpr int SP_ST = 24576, SP_LDS = 3 * SP_ST + 256, FN_ST = 16384;

template <int N> DI void wait_vm() { asm volatile("s_waitcnt vmcnt(%0)" :: "i"(N) : "memory"); }

DI void gate_block(const u16* __restrict__ x, const u16* __restrict__ w_in, const u16* __restrict__ w_sp, const u16* __restrict__ b_sp,
                   const u16* __restrict__ wo, const u16* __restrict__ w_out, char* __restrict__ ws, u16* __restrict__ out, char* lds, int g, int tid) {
    const int l = tid & 63, w = __builtin_amdgcn_readfirstlane(tid >> 6), h = l >> 5, r32 = l & 31;
    const int tl = (g >> 3) & 7, tt = (g & 3) * 8 + tl, ft = ((g >> 2) & 1) * 4 + (g >> 6);
    const int rot = tl * 4 + (g >> 6) * 2;
    const unsigned lds0 = (unsigned)(uintptr_t)lds;
    const int q4 = (l & 15) >> 2, p4 = l & 3, b16 = (l >> 4) & 1;
    const unsigned lo = l * 16;
    __builtin_amdgcn_s_setprio(2);
    const int j = w - 4, lrow = 8 * j + (l >> 3);
    const unsigned xv = (unsigned)(lrow * D_ + ((l & 7) ^ ((lrow >> 1) & 7)) * 8) * 2;
    const int wr = 4 * j + (l >> 4), wc = ((((l & 15) >> 2) ^ (wr & 3)) << 2) | (l & 3);
    const unsigned wv = (unsigned)(wr * 1792 + wc * 8) * 2;
    auto issue_z = [&](int s) {
        const char* xu = (const char*)(x + (size_t)tt * 64 * D_); const char* wu = (const char*)(w_in + ft * 128);
        const unsigned dst = lds0 + (s % 3) * SP_ST + j * 1024; const int sr = (s + rot) & 7;
        glds16s(xu + sr * 128, xv, dst); glds16s(xu + 32 * D_ * 2 + sr * 128, xv, dst + 4096);
#pragma unroll
        for (int i = 0; i < 4; ++i) glds16s(wu + (size_t)(sr * 64 + 16 * i) * 3584, wv, dst + 8192 + i * 4096);
    };
    if (w >= 4 && ft < 7) { issue_z(0); issue_z(1); }
    if (w < 7) {
        const int ttile = g >> 2, fb = (g & 3) * 7 + w;
        const char* z2 = ws + WS_Z2 + (size_t)(ttile * 56 + 2 * fb) * 1024 + lo;
        const u32x4 z0 = *(const u32x4*)z2, z1 = *(const u32x4*)(z2 + 1024);
        f32x4 pv[7];
        const f32x4* sp = (const f32x4*)(ws + WS_STATS) + (size_t)(ttile * 32 + r32) * 14 + h * 7;
#pragma unroll
        for (int i = 0; i < 7; ++i) pv[i] = sp[i];
        ln_block(ws, lds + 2 * SP_ST + w * 1024, ttile, fb, z0, z1, ln_stats(pv), l);
    }
    if (ft >= 7) { drain_stores(); bar_only(); if (tid == 0) __hip_atomic_fetch_add(sync_word(ws, SY_LN + (g & 15)), 1u, RLX_AGENT); }
    if (ft < 7) {
        if (w >= 4) {
            if (w < 7) wait_bar<2>(); else wait_bar<0>();
            issue_z(2);
#pragma unroll 1
            for (int s = 0; s < 7; ++s) {
                if (s == 0) bar_only(); else if (s + 2 < 8) wait_bar<6>(); else wait_bar<0>();
                if (s + 3 < 8) issue_z(s + 3);
            }
            bar_only();
            if (j == 3) {
                wait_words(ws, SY_LN, 16, 16u, l);
                const char* zu = ws + WS_Z2N + (size_t)(ft * 4) * 128 * 1024;
                const unsigned pv = (unsigned)((tl >> 1) * 128 * 1024 + ((tl & 1) * 16 + (l & 15)) * 128);
                auto pf = [&](int s4) { const int sr = (4 * s4 + (l >> 4) + rot) & 31; prefetch_line(zu, pv + sr * 4096, lds0 + 3 * SP_ST); };
                pf(0); pf(1); pf(2);
                bar_only();
                bar_only();
#pragma unroll 1
                for (int s = 0; s < 31; ++s) { bar_only(); if ((s & 3) == 0 && s + 12 < 32) pf((s >> 2) + 3); }
                drain_stores();
            } else {
                const unsigned a0 = (unsigned)((l >> 3) * S_ * 2 + (((l & 7) ^ ((l >> 4) & 7)) << 4)), a1 = a0 ^ 64u;
                const unsigned va = (j & 1) ? a1 : a0, vb = (j & 1) ? a0 : a1;
                const char* au = (const char*)(w_sp + (size_t)(tt * 64 + 8 * j) * S_);
                auto issue = [&](int s) {
                    const unsigned dst = lds0 + (s % 6) * 8192 + j * 1024; const int sr = (s + rot) & 31;
                    glds16s(au + sr * 128, va, dst); glds16s(au + 24 * S_ * 2 + sr * 128, vb, dst + 3072);
                    if (j < 2) glds16s(au + 48 * S_ * 2 + sr * 128, va, dst + 6144);
                };
                auto waitst = [&](int younger) {
                    if (j < 2) { if (younger >= 4) wait_vm<12>(); else if (younger == 3) wait_vm<9>(); else if (younger == 2) wait_vm<6>(); else if (younger == 1) wait_vm<3>(); else wait_vm<0>(); }
                    else { if (younger >= 4) wait_vm<8>(); else if (younger == 3) wait_vm<6>(); else if (younger == 2) wait_vm<4>(); else if (younger == 1) wait_vm<2>(); else wait_vm<0>(); }
                };
#pragma unroll 1
                for (int s = 0; s < 5; ++s) issue(s);
                bar_only();
                waitst(4); bar_only();
                issue(5);
#pragma unroll 1
                for (int s = 0; s < 31; ++s) {
                    waitst(min(s + 5, 31) - (s + 1)); bar_only();
                    if (s + 6 < 32) issue(s + 6);
                }
            }
            bar_only();
        } else {
            {
                const int idx = (w & 1) * 64 + l, fl = g >> 6;
                if (w < 2) prefetch_line(x + (size_t)tt * 64 * D_, (unsigned)((16 * fl + (idx >> 3)) * D_ * 2 + (idx & 7) * 128), lds0 + 3 * SP_ST);
                else prefetch_line(w_in + ft * 128, (unsigned)((64 * tl + (idx >> 1)) * 3584 + (idx & 1) * 128), lds0 + 3 * SP_ST);
            }
            const int fb = w;
            int boff[4];
#pragma unroll
            for (int ks = 0; ks < 4; ++ks) { const int f = (r32 >> 1) & 7; boff[ks] = r32 * 128 + (((ks * 2 + h) ^ f) << 4); }
            const int fbg = ft * 4 + fb, fg = 2 * fbg;
            const float gt0 = ((const float*)(ws + WS_GATE))[(tt * 2) * 32 + r32], gt1 = ((const float*)(ws + WS_GATE))[(tt * 2 + 1) * 32 + r32];
            const float bs0 = __uint_as_float((unsigned)b_sp[(tt * 2) * 32 + r32] << 16), bs1 = __uint_as_float((unsigned)b_sp[(tt * 2 + 1) * 32 + r32] << 16);
            LDS_AS u32x4* z1p = (LDS_AS u32x4*)((lds_ptr)lds + 49152 + w * 4096) + l;
            {
                f32x16 acc[2]; acc[0] = f32x16{}; acc[1] = f32x16{};
                const int zoff = 8192 + (8 * h + q4) * 256 + ((fb ^ q4) << 6) + b16 * 32 + p4 * 8;
                bf16x8 aa[2], ba[2][2], ab[2], bb[2][2];
                auto rdh = [&](int s, int half, bf16x8 (&af)[2], bf16x8 (&bf)[2][2]) {
                    lds_cptr base = (lds_cptr)lds + (s % 3) * SP_ST;
#pragma unroll
                    for (int kk = 0; kk < 2; ++kk) {
                        const int ks = 2 * half + kk;
                        af[kk] = trfrag(base + zoff + ks * 4096, base + zoff + ks * 4096 + 1024);
                        bf[0][kk] = ldsfrag(base + boff[ks]); bf[1][kk] = ldsfrag(base + boff[ks] + 4096);
                    }
                };
                auto mm = [&](bf16x8 (&af)[2], bf16x8 (&bf)[2][2]) {
#pragma unroll
                    for (int kk = 0; kk < 2; ++kk) { acc[0] = MFMA(af[kk], bf[0][kk], acc[0]); acc[1] = MFMA(af[kk], bf[1][kk], acc[1]); }
                };
                bar_only();
                rdh(0, 0, aa, ba);
#pragma unroll 1
                for (int s = 0; s < 7; ++s) {
                    rdh(s, 1, ab, bb); mm(aa, ba); interleave<4, 2>(); SB();
                    if (s == 1) drain_stores();
                    bar_only();
                    if (s == 1 && tid == 0) __hip_atomic_fetch_add(sync_word(ws, SY_LN + (g & 15)), 1u, RLX_AGENT);
                    rdh(s + 1, 0, aa, ba); mm(ab, bb); interleave<4, 2>(); SB();
                }
                rdh(7, 1, ab, bb); mm(aa, ba); interleave<4, 2>(); SB();
                mm(ab, bb);
                bar_only();
#pragma unroll
                for (int tb = 0; tb < 2; ++tb) {
                    const float og = 1.0f - (tb ? gt1 : gt0);
#pragma unroll
                    for (int gq = 0; gq < 2; ++gq) {
                        u32x4 zv;
#pragma unroll
                        for (int e = 0; e < 4; ++e) zv[e] = cvtpk(gelu_tanh(acc[tb][8 * gq + 2 * e]) * og, gelu_tanh(acc[tb][8 * gq + 2 * e + 1]) * og);
                        z1p[(tb * 2 + gq) * 64] = zv;
                    }
                }
            }
            if (w < 3) {
                const int row = min(176 * tl + w * 64 + l, 176 * tl + 175);
                prefetch_ptr(row < 896 ? (const char*)(w_out + (size_t)row * D_ + ft * 64) : (const char*)(wo + (size_t)(row - 896) * D_ + ft * 64), lds0 + 3 * SP_ST);
            }
            f32x16 acc[2]; acc[0] = f32x16{}; acc[1] = f32x16{};
            {
                bar_only();
                bf16x8 za[4], zb[4];
                const size_t zoff = WS_Z2N + (size_t)fbg * 128 * 1024 + lo;
                auto ldz = [&](int s, int ks) { const int sr = (min(s, 31) + rot) & 31; return ld16_dev(ws, zoff + (size_t)((sr * 4 + ks) * 1024)); };
#pragma unroll
                for (int ks = 0; ks < 4; ++ks) za[ks] = ldz(0, ks);
#pragma unroll
                for (int ks = 0; ks < 4; ++ks) zb[ks] = ldz(1, ks);
                bf16x8 bx[2][2], by[2][2];
                auto rdh = [&](int s, int half, bf16x8 (&bf)[2][2]) {
                    lds_cptr base = (lds_cptr)lds + (s % 6) * 8192;
#pragma unroll
                    for (int kk = 0; kk < 2; ++kk) { bf[0][kk] = ldsfrag(base + boff[2 * half + kk]); bf[1][kk] = ldsfrag(base + boff[2 * half + kk] + 4096); }
                };
                auto stage = [&](int s, bf16x8 (&z)[4], bool last) {
                    rdh(s, 1, by);
                    acc[0] = MFMA(z[0], bx[0][0], acc[0]); acc[1] = MFMA(z[0], bx[1][0], acc[1]);
                    acc[0] = MFMA(z[1], bx[0][1], acc[0]); acc[1] = MFMA(z[1], bx[1][1], acc[1]); interleave<4, 1>(); SB();
                    z[0] = ldz(s + 2, 0); z[1] = ldz(s + 2, 1); SB();
                    if (!last) { bar_only(); rdh(s + 1, 0, bx); }
                    acc[0] = MFMA(z[2], by[0][0], acc[0]); acc[1] = MFMA(z[2], by[1][0], acc[1]);
                    acc[0] = MFMA(z[3], by[0][1], acc[0]); acc[1] = MFMA(z[3], by[1][1], acc[1]); interleave<4, 1>(); SB();
                    z[2] = ldz(s + 2, 2); z[3] = ldz(s + 2, 3); SB();
                };
                bar_only();
                rdh(0, 0, bx);
#pragma unroll 1
                for (int s = 0; s < 30; s += 2) { stage(s, za, false); stage(s + 1, zb, false); }
                stage(30, za, false); stage(31, zb, true);
            }
#pragma unroll
            for (int tb = 0; tb < 2; ++tb) {
                const float bs = tb ? bs1 : bs0;
#pragma unroll
                for (int gq = 0; gq < 2; ++gq) {
                    u32x4 ov; const u32x4 zv = z1p[(tb * 2 + gq) * 64];
#pragma unroll
                    for (int e = 0; e < 4; ++e) ov[e] = cvtpk((acc[tb][8 * gq + 2 * e] + bs) * bf_lo(zv[e]), (acc[tb][8 * gq + 2 * e + 1] + bs) * bf_hi(zv[e]));
                    st16_wt(ws, WS_HO + ((size_t)((tt * 2 + tb) * 88 + 32 + fg + gq) * 1024) + lo, ov);
                }
            }
            drain_stores();
            bar_only();
            if (tid == 0) __hip_atomic_fetch_add(sync_word(ws, SY_H + tt), 1u, RLX_AGENT);
        }
    }
    {
        __builtin_amdgcn_s_setprio(2);
        const int tb = w & 1, kq = w >> 1;
        const int prow = 8 * w + (l >> 3);
        const unsigned pv = (unsigned)(prow * D_ + ((l & 7) ^ (((prow >> 1) & 1) << 2)) * 8) * 2;
        const char* w0 = (const char*)(w_out + ft * 64);
        const char* w1 = (const char*)(wo + ft * 64);
        auto issue = [&](int s) {
            const unsigned dst = lds0 + (s & 3) * FN_ST + w * 1024;
            const char* wp = s < 7 ? w0 + (size_t)s * 128 * D_ * 2 : w1 + (size_t)(s - 7) * 128 * D_ * 2;
            glds16s(wp, pv, dst); glds16s(wp + (size_t)64 * D_ * 2, pv, dst + 8192);
        };
        issue(0); issue(1); issue(2); issue(3);
        const int woff0 = (kq * 32 + 4 * h + q4) * 128 + (((b16 * 2 + (p4 >> 1)) ^ ((q4 >> 1) << 2)) << 4) + (p4 & 1) * 8, woff1 = woff0 ^ 64;
        const size_t hoff = WS_HO + ((size_t)((tt * 2 + tb) * 88 + 2 * kq) * 1024) + lo;
        f32x16 acc[2]; acc[0] = f32x16{}; acc[1] = f32x16{};
        bf16x8 hf[14];
        auto comp = [&](int s, const bf16x8& h0, const bf16x8& h1) {
            lds_cptr base = (lds_cptr)lds + (s & 3) * FN_ST;
            const bf16x8 w00 = trfrag(base + woff0, base + woff0 + 1024), w01 = trfrag(base + woff1, base + woff1 + 1024);
            const bf16x8 w10 = trfrag(base + woff0 + 2048, base + woff0 + 3072), w11 = trfrag(base + woff1 + 2048, base + woff1 + 3072);
            acc[0] = MFMA(w00, h0, acc[0]); acc[1] = MFMA(w01, h0, acc[1]);
            acc[0] = MFMA(w10, h1, acc[0]); acc[1] = MFMA(w11, h1, acc[1]);
            SB();
        };
        if (w == 7) wait_words(ws, SY_H + tt, 1, 7u, l);
        bar_only();
#pragma unroll
        for (int q = 0; q < 14; ++q) hf[q] = ld16_dev(ws, hoff + (size_t)((32 + 8 * (q >> 1) + (q & 1)) * 1024));
        wait_vm<20>(); bar_only();            comp(0, hf[0], hf[1]);
        wait_vm<18>(); bar_only(); issue(4);  comp(1, hf[2], hf[3]);
        wait_vm<18>(); bar_only(); issue(5);  comp(2, hf[4], hf[5]);
        wait_vm<18>(); bar_only(); issue(6);  comp(3, hf[6], hf[7]);
        wait_vm<4>();  bar_only(); issue(7);  comp(4, hf[8], hf[9]);
        wait_vm<4>();  bar_only(); issue(8);  comp(5, hf[10], hf[11]);
        wait_vm<4>();  bar_only(); issue(9);  comp(6, hf[12], hf[13]);
        if (w == 7) wait_words(ws, SY_O + (tt >> 2), 1, 32u, l);
        bar_only();
#pragma unroll
        for (int q = 0; q < 8; ++q) hf[q] = ld16_dev(ws, hoff + (size_t)((8 * (q >> 1) + (q & 1)) * 1024));
        wait_vm<12>(); bar_only(); issue(10); comp(7, hf[0], hf[1]);
        wait_vm<12>(); bar_only();            comp(8, hf[2], hf[3]);
        wait_vm<10>(); bar_only();            comp(9, hf[4], hf[5]);
        wait_vm<0>();  bar_only();            comp(10, hf[6], hf[7]);
        bar_only();
        LDS_AS f32x4* red = (LDS_AS f32x4*)lds;
#pragma unroll
        for (int db = 0; db < 2; ++db)
#pragma unroll
            for (int q = 0; q < 4; ++q) red[((w * 2 + db) * 4 + q) * 64 + l] = f32x4{acc[db][4 * q], acc[db][4 * q + 1], acc[db][4 * q + 2], acc[db][4 * q + 3]};
        bar_only();
        const int db = kq & 1, gq = kq >> 1;
        f32x4 v0 = {0.f, 0.f, 0.f, 0.f}, v1 = v0;
#pragma unroll
        for (int q = 0; q < 4; ++q) { const int ww = q * 2 + tb; v0 += red[((ww * 2 + db) * 4 + 2 * gq) * 64 + l]; v1 += red[((ww * 2 + db) * 4 + 2 * gq + 1) * 64 + l]; }
        const unsigned a0 = cvtpk(v0[0], v0[1]), a1 = cvtpk(v0[2], v0[3]), b0 = cvtpk(v1[0], v1[1]), b1 = cvtpk(v1[2], v1[3]);
        auto r0 = __builtin_amdgcn_permlane32_swap(a0, b0, false, false);
        auto r1 = __builtin_amdgcn_permlane32_swap(a1, b1, false, false);
        const u32x4 ov = {r0[0], r1[0], r0[1], r1[1]};
        *(u32x4*)(out + (size_t)(tt * 64 + tb * 32 + r32) * D_ + ft * 64 + db * 32 + gq * 16 + h * 8) = ov;
    }
}

extern "C" __global__ __launch_bounds__(512, 4) void k_main(const u16* __restrict__ x, const u16* __restrict__ w_in, const u16* __restrict__ w_sp, const u16* __restrict__ b_sp,
                                                 const u16* __restrict__ wo, const u16* __restrict__ w_out, char* __restrict__ ws, u16* __restrict__ out) {
    __shared__ __attribute__((aligned(1024))) char lds[AT_LDS > SP_LDS ? AT_LDS : SP_LDS];
    const int b = blockIdx.x;
    if (b < 256) {
        attn_block(ws, lds, (b & 7) * 4 + ((b >> 3) & 3), b >> 5, threadIdx.x);
    } else {
        gate_block(x, w_in, w_sp, b_sp, wo, w_out, ws, out, lds, b - 256, threadIdx.x);
    }
}

extern "C" void kernel_launch(void* const* d_in, const int* in_sizes, int n_in, void* d_out, int out_size, void* d_ws, size_t ws_size, hipStream_t stream) {
    const u16* x = (const u16*)d_in[0];
    const u16* wqkv = (const u16*)d_in[1];
    const u16* wo = (const u16*)d_in[2];
    const u16* w_in = (const u16*)d_in[4];
    const u16* w_sp = (const u16*)d_in[5];
    const u16* b_sp = (const u16*)d_in[6];
    const u16* w_out = (const u16*)d_in[7];
    const u16* gate_w = (const u16*)d_in[8];
    char* ws = (char*)d_ws;
    k1_proj<<<256, 512, 0, stream>>>(x, wqkv, w_in, gate_w, w_sp, wo, w_out, ws);
    k_main<<<512, 512, 0, stream>>>(x, w_in, w_sp, b_sp, wo, w_out, ws, (u16*)d_out);
}
```
